```python
import torch

def _softmax(x, axis=-1):
    return torch.softmax(x, dim=axis)

def _gelu(x):
    return torch.nn.functional.gelu(x, approximate='tanh')

def _silu(x):
    return torch.nn.functional.silu(x)

def _sigmoid(x):
    return torch.sigmoid(x)

def _layernorm(x, axis=-1, eps=1e-05):
    m = x.mean(dim=axis, keepdim=True)
    v = x.var(dim=axis, keepdim=True, unbiased=False)
    return (x - m) / torch.sqrt(v + eps)

def _scatter_add(tgt, idx, src, axis=0):
    return tgt.clone().index_add_(axis, idx, src)

def _segment_sum(src, seg, n):
    out = torch.zeros((n,) + src.shape[1:], dtype=src.dtype)
    return out.index_add_(0, seg, src)

def _pad(x, pw):
    flat = tuple((v for pair in reversed(pw) for v in pair))
    return torch.nn.functional.pad(x, flat)
kernel_name = 'adaLN_cond__mha_block_cached__swiglu_ffn__decode'

def setup_inputs():

    _w = lambda a: (lambda t: t.to(torch.bfloat16) if t.is_floating_point() else t)(torch.as_tensor(a))

    def _A_setup():
        B, Sq, D = (1, 1, 576)
        H, Sk, Dh = (32, 8192, 128)
        return {'x': _w((lambda *s: torch.randn(*s, dtype=torch.bfloat16) * 0.05)(B, Sq, D)), 'k_cache': _w((lambda *s: torch.randn(*s, dtype=torch.bfloat16) * 3.2)(B, H, Sk, Dh)), 'v_cache': _w((lambda *s: torch.randn(*s, dtype=torch.bfloat16) * 0.4)(B, H, Sk, Dh)), 'wq': _w((lambda *s: torch.randn(*s, dtype=torch.bfloat16) * 3.2)(D, H * Dh) * D ** (-0.5)), 'wo': _w((lambda *s: torch.randn(*s, dtype=torch.bfloat16) * 0.4)(H * Dh, D) * (H * Dh) ** (-0.5))}
    _A_in = _A_setup()

    def _B_setup():
        B, S, D, F = (1, 1, 576, 4608)
        return {'x': _w((lambda *s: torch.randn(*s, dtype=torch.bfloat16) * 0.05)(B, S, D)), 'w_gate': _w((lambda *s: torch.randn(*s, dtype=torch.bfloat16) * 0.05)(D, F)), 'w_up': _w((lambda *s: torch.randn(*s, dtype=torch.bfloat16) * 0.05)(D, F)), 'w_down': _w((lambda *s: torch.randn(*s, dtype=torch.bfloat16) * 0.05)(F, D))}
    _B_in = _B_setup()
    return {'A_x': _A_in['x'], 'A_k_cache': _A_in['k_cache'], 'A_v_cache': _A_in['v_cache'], 'A_wq': _A_in['wq'], 'A_wo': _A_in['wo'], 'B_x': _B_in['x'], 'B_w_gate': _B_in['w_gate'], 'B_w_up': _B_in['w_up'], 'B_w_down': _B_in['w_down'], 'c': _w((lambda *s: torch.randn(*s, dtype=torch.bfloat16) * 0.1)(1, 576) * 8.0), 'w_mod': _w((lambda *s: torch.randn(*s, dtype=torch.bfloat16) * 0.1)(576, 6 * 576) * 576 ** (-0.5) * 8.0)}

def reference(A_x, A_k_cache, A_v_cache, A_wq, A_wo, B_x, B_w_gate, B_w_up, B_w_down, c, w_mod):

    def _A_ref(x, k_cache, v_cache, wq, wo):
        B, Sq, D = x.shape
        H, Dh = (32, 128)
        q = (lambda x, *d: x.permute(*d))((x @ wq).reshape(B, Sq, H, Dh), 0, 2, 1, 3)
        scores = torch.einsum('bhqd,bhkd->bhqk', q, k_cache) * Dh ** (-0.5)
        p = _softmax(scores, axis=-1)
        o = torch.einsum('bhqk,bhkd->bhqd', p, v_cache)
        return (lambda x, *d: x.permute(*d))(o, 0, 2, 1, 3).reshape(B, Sq, H * Dh) @ wo

    def _B_ref(x, w_gate, w_up, w_down):
        g = _silu(x @ w_gate)
        u = x @ w_up
        return g * u @ w_down
    x = A_x
    mod = (_silu(c) @ w_mod).reshape(c.shape[0], 6, 1, -1)
    sa, ca, ga, sb, cb, gb = (mod[:, 0].clone(), mod[:, 1].clone(), mod[:, 2].clone(), mod[:, 3].clone(), mod[:, 4].clone(), mod[:, 5].clone())
    h = _layernorm(x, axis=-1) * (1 + ca) + sa
    x = x + ga * _A_ref(h, A_k_cache, A_v_cache, A_wq, A_wo)
    h = _layernorm(x, axis=-1) * (1 + cb) + sb
    x = x + gb * _B_ref(h, B_w_gate, B_w_up, B_w_down)
    return x
```

```cpp
#include <hip/hip_runtime.h>
#include <hip/hip_bf16.h>
#include <stdint.h>

#define MIMIC 0

typedef unsigned short u16;
typedef unsigned v4u __attribute__((ext_vector_type(4)));
typedef __bf16 bf16x2 __attribute__((ext_vector_type(2)));

constexpr int D = 576, H = 32, SK = 8192, DH = 128, F = 4608, HD = H * DH;
constexpr int NSPLIT = 8;

__device__ __forceinline__ float bf2f(u16 b) { return __builtin_bit_cast(float, (unsigned)b << 16); }
__device__ __forceinline__ float rbf(float f) {
  return f;
}
__device__ __forceinline__ float lo16(unsigned u) { return __builtin_bit_cast(float, u << 16); }
__device__ __forceinline__ float hi16(unsigned u) { return __builtin_bit_cast(float, u & 0xffff0000u); }
__device__ __forceinline__ float wave_sum(float v) { for (int o = 32; o > 0; o >>= 1) v += __shfl_xor(v, o); return v; }
__device__ __forceinline__ float wave_max(float v) { for (int o = 32; o > 0; o >>= 1) v = fmaxf(v, __shfl_xor(v, o)); return v; }

template <int K, int N>
__device__ __forceinline__ void gemv_cols(const float* xs, const u16* W, int n0, float* red, float& o0, float& o1) {
  int tid = threadIdx.x, c = tid & 31, kg = tid >> 5;
  float a0 = 0.f, a1 = 0.f;
  for (int k = kg; k < K; k += 8) {
    unsigned w = *(const unsigned*)(W + (size_t)k * N + n0 + 2 * c);
    a0 = fmaf(xs[k], lo16(w), a0); a1 = fmaf(xs[k], hi16(w), a1);
  }
  red[kg * 64 + 2 * c] = a0; red[kg * 64 + 2 * c + 1] = a1;
  __syncthreads();
  o0 = 0.f;
  if (tid < 64) for (int g = 0; g < 8; ++g) o0 += red[g * 64 + tid];
  __syncthreads();
}

__global__ void __launch_bounds__(256) k_mod(const u16* c, const u16* wmod, float* mod) {
  __shared__ float xs[D]; __shared__ float red[512];
  for (int i = threadIdx.x; i < D; i += 256) { float v = bf2f(c[i]); xs[i] = rbf(v / (1.f + __expf(-v))); }
  __syncthreads();
  float o0, o1; gemv_cols<D, 6 * D>(xs, wmod, blockIdx.x * 64, red, o0, o1);
  if (threadIdx.x < 64) mod[blockIdx.x * 64 + threadIdx.x] = rbf(o0);
}

__device__ __forceinline__ void ln_mod(const float* xin, const float* shift, const float* scale, float* xs, float* red) {
  int tid = threadIdx.x;
  float s = 0.f;
  for (int i = tid; i < D; i += 256) s += xin[i];
  s = wave_sum(s); if ((tid & 63) == 0) red[tid >> 6] = s; __syncthreads();
  float mean = (red[0] + red[1] + red[2] + red[3]) * (1.f / D); __syncthreads();
  float q = 0.f;
  for (int i = tid; i < D; i += 256) { float d = xin[i] - mean; q += d * d; }
  q = wave_sum(q); if ((tid & 63) == 0) red[tid >> 6] = q; __syncthreads();
  float var = (red[0] + red[1] + red[2] + red[3]) * (1.f / D); __syncthreads();
  float m = rbf(mean), v = rbf(var);
  float sd = rbf(sqrtf(rbf(v + 1e-5f)));
  for (int i = tid; i < D; i += 256) {
    float t = rbf(rbf(xin[i] - m) / sd);
    xs[i] = rbf(rbf(t * rbf(1.f + scale[i])) + shift[i]);
  }
  __syncthreads();
}

__global__ void __launch_bounds__(256) k_q(const u16* x, const float* mod, const u16* wq, float* q) {
  __shared__ float xin[D]; __shared__ float xs[D]; __shared__ float red[512];
  for (int i = threadIdx.x; i < D; i += 256) xin[i] = bf2f(x[i]);
  __syncthreads();
  ln_mod(xin, mod, mod + D, xs, red);
  float o0, o1; gemv_cols<D, HD>(xs, wq, blockIdx.x * 64, red, o0, o1);
  if (threadIdx.x < 64) q[blockIdx.x * 64 + threadIdx.x] = rbf(o0);
}

__global__ void __launch_bounds__(256) k_attn(const float* q, const u16* Kc, const u16* Vc, float* part) {
  __shared__ float sm[4][132];
  int h = blockIdx.x / NSPLIT, s = blockIdx.x % NSPLIT;
  int tid = threadIdx.x, lane = tid & 63, wave = tid >> 6, r = lane >> 4, c = lane & 15;
  float qf[8];
  for (int j = 0; j < 8; ++j) qf[j] = q[h * DH + c * 8 + j];
  const int KPS = SK / NSPLIT, KPW = KPS / 4;
  size_t base = ((size_t)h * SK + s * KPS + wave * KPW) * DH;
  float m = -INFINITY, l = 0.f, acc[8] = {0, 0, 0, 0, 0, 0, 0, 0};
  for (int k0 = 0; k0 < KPW; k0 += 16) {
    v4u kk[4], vv[4];
#pragma unroll
    for (int t = 0; t < 4; ++t) {
      size_t off = base + (size_t)(k0 + t * 4 + r) * DH + c * 8;
      kk[t] = __builtin_nontemporal_load((const v4u*)(Kc + off));
      vv[t] = __builtin_nontemporal_load((const v4u*)(Vc + off));
    }
    float sc[4];
#pragma unroll
    for (int t = 0; t < 4; ++t) {
      float d = 0.f;
#pragma unroll
      for (int j = 0; j < 4; ++j) { d = fmaf(lo16(kk[t][j]), qf[2 * j], d); d = fmaf(hi16(kk[t][j]), qf[2 * j + 1], d); }
      d += __shfl_xor(d, 1); d += __shfl_xor(d, 2); d += __shfl_xor(d, 4); d += __shfl_xor(d, 8);
      sc[t] = rbf(rbf(d) * 0.08838834764831845f);
    }
    float mn = fmaxf(fmaxf(fmaxf(sc[0], sc[1]), fmaxf(sc[2], sc[3])), m);
    float al = __expf(m - mn); l *= al;
#pragma unroll
    for (int j = 0; j < 8; ++j) acc[j] *= al;
    m = mn;
#pragma unroll
    for (int t = 0; t < 4; ++t) {
      float p = __expf(sc[t] - m); l += p;
#pragma unroll
      for (int j = 0; j < 4; ++j) { acc[2 * j] = fmaf(p, lo16(vv[t][j]), acc[2 * j]); acc[2 * j + 1] = fmaf(p, hi16(vv[t][j]), acc[2 * j + 1]); }
    }
  }
  float M = fmaxf(m, __shfl_xor(m, 16)); M = fmaxf(M, __shfl_xor(M, 32));
  float w = __expf(m - M); l *= w;
  l += __shfl_xor(l, 16); l += __shfl_xor(l, 32);
  for (int j = 0; j < 8; ++j) { float a = acc[j] * w; a += __shfl_xor(a, 16); a += __shfl_xor(a, 32); acc[j] = a; }
  if (r == 0) { for (int j = 0; j < 8; ++j) sm[wave][4 + c * 8 + j] = acc[j]; if (c == 0) { sm[wave][0] = M; sm[wave][1] = l; } }
  __syncthreads();
  if (tid < 128) {
    float MM = fmaxf(fmaxf(sm[0][0], sm[1][0]), fmaxf(sm[2][0], sm[3][0]));
    float L = 0.f, o = 0.f;
    for (int w2 = 0; w2 < 4; ++w2) { float e = __expf(sm[w2][0] - MM); L += e * sm[w2][1]; o += e * sm[w2][4 + tid]; }
    float* p = part + (size_t)blockIdx.x * 132;
    p[4 + tid] = o; if (tid == 0) { p[0] = MM; p[1] = L; }
  }
}

__global__ void __launch_bounds__(256) k_wo(const float* part, const u16* wo, float* y) {
  __shared__ float o[DH];
  int h = blockIdx.x, tid = threadIdx.x;
  if (tid < 128) {
    const float* p = part + (size_t)h * NSPLIT * 132;
    float M = -INFINITY; for (int s = 0; s < NSPLIT; ++s) M = fmaxf(M, p[s * 132]);
    float L = 0.f, a = 0.f;
    for (int s = 0; s < NSPLIT; ++s) { float e = __expf(p[s * 132] - M); L += e * p[s * 132 + 1]; a += e * p[s * 132 + 4 + tid]; }
    o[tid] = rbf(a / L);
  }
  __syncthreads();
  for (int n = tid; n < D / 2; n += 256) {
    float a0 = 0.f, a1 = 0.f;
    for (int k = 0; k < DH; ++k) { unsigned w = *(const unsigned*)(wo + (size_t)(h * DH + k) * D + 2 * n); a0 = fmaf(o[k], lo16(w), a0); a1 = fmaf(o[k], hi16(w), a1); }
    y[h * D + 2 * n] = a0; y[h * D + 2 * n + 1] = a1;
  }
}

__global__ void __launch_bounds__(256) k_ffn1(const u16* x, const float* mod, const float* y, const u16* wg, const u16* wu, float* act, float* x1out) {
  __shared__ float xin[D]; __shared__ float xs[D]; __shared__ float red[512];
  for (int i = threadIdx.x; i < D; i += 256) {
    float s = 0.f; for (int h = 0; h < H; ++h) s += y[h * D + i];
    float v = rbf(bf2f(x[i]) + rbf(mod[2 * D + i] * rbf(s)));
    xin[i] = v; if (blockIdx.x == 0) x1out[i] = v;
  }
  __syncthreads();
  ln_mod(xin, mod + 3 * D, mod + 4 * D, xs, red);
  float g, u, t;
  gemv_cols<D, F>(xs, wg, blockIdx.x * 64, red, g, t);
  gemv_cols<D, F>(xs, wu, blockIdx.x * 64, red, u, t);
  if (threadIdx.x < 64) { g = rbf(g); u = rbf(u); float sg = rbf(g / (1.f + __expf(-g))); act[blockIdx.x * 64 + threadIdx.x] = rbf(sg * u); }
}

__global__ void __launch_bounds__(256) k_ffn2(const float* act, const u16* wd, const float* mod, const float* x1, __hip_bfloat16* out) {
  __shared__ float xs[F]; __shared__ float red[512];
  for (int i = threadIdx.x; i < F; i += 256) xs[i] = act[i];
  __syncthreads();
  float o, t; gemv_cols<F, D>(xs, wd, blockIdx.x * 64, red, o, t);
  if (threadIdx.x < 64) { int n = blockIdx.x * 64 + threadIdx.x; float r = x1[n] + rbf(mod[5 * D + n] * rbf(o)); out[n] = __float2bfloat16(r); }
}

extern "C" void kernel_launch(void* const* d_in, const int* in_sizes, int n_in, void* d_out, int out_size, void* d_ws, size_t ws_size, hipStream_t stream) {
  const u16* x = (const u16*)d_in[0]; const u16* Kc = (const u16*)d_in[1]; const u16* Vc = (const u16*)d_in[2];
  const u16* wq = (const u16*)d_in[3]; const u16* wo = (const u16*)d_in[4];
  const u16* wg = (const u16*)d_in[6]; const u16* wu = (const u16*)d_in[7]; const u16* wd = (const u16*)d_in[8];
  const u16* c = (const u16*)d_in[9]; const u16* wmod = (const u16*)d_in[10];
  float* ws = (float*)d_ws;
  float* mod = ws; float* q = ws + 4096; float* part = ws + 8192; float* y = part + 256 * 132; float* act = y + H * D; float* x1 = act + F;
  k_mod<<<6 * D / 64, 256, 0, stream>>>(c, wmod, mod);
  k_q<<<HD / 64, 256, 0, stream>>>(x, mod, wq, q);
  k_attn<<<H * NSPLIT, 256, 0, stream>>>(q, Kc, Vc, part);
  k_wo<<<H, 256, 0, stream>>>(part, wo, y);
  k_ffn1<<<F / 64, 256, 0, stream>>>(x, mod, y, wg, wu, act, x1);
  k_ffn2<<<D / 64, 256, 0, stream>>>(act, wd, mod, x1, (__hip_bfloat16*)d_out);
}
```

```cpp
#include <hip/hip_runtime.h>
#include <hip/hip_bf16.h>
#include <stdint.h>

typedef unsigned short u16;
typedef unsigned long long u64;
typedef unsigned v4u __attribute__((ext_vector_type(4)));
typedef unsigned v3u __attribute__((ext_vector_type(3)));
typedef unsigned v2u __attribute__((ext_vector_type(2)));
typedef float v2f __attribute__((ext_vector_type(2)));
typedef float v4f __attribute__((ext_vector_type(4)));
typedef __bf16 bf2 __attribute__((ext_vector_type(2)));
typedef __amdgpu_buffer_rsrc_t rsrc_t;

#define THR 4
#define RAMP 1
#define STAMPS 0

constexpr int AUX_NT = 2, AUX_SC1 = 16;
constexpr unsigned OOB = 0x40000000u;
constexpr unsigned OFF_U = 4096;
constexpr unsigned OFF_W = 16384;
constexpr unsigned OFF_Q = 36864;
constexpr unsigned OFF_AO = 69632;
constexpr unsigned OFF_AM = OFF_AO + 131072;
constexpr unsigned OFF_B = OFF_AM + 4096;
constexpr unsigned OFF_C = OFF_B + 18432;
constexpr unsigned WS_END = OFF_C + 331776;
constexpr unsigned OFF_STAMP = 1u << 20;
constexpr int GCAP = 1 << 15;
constexpr int LCAP = 1 << 20;
constexpr float LOG2E = 1.4426950408889634f;

__device__ __forceinline__ float asf(unsigned u) { return __builtin_bit_cast(float, u); }
__device__ __forceinline__ unsigned asu(float f) { return __builtin_bit_cast(unsigned, f); }
__device__ __forceinline__ float lo16(unsigned u) { return asf(u << 16); }
__device__ __forceinline__ float hi16(unsigned u) { return asf(u & 0xffff0000u); }
__device__ __forceinline__ float rbf(float f) { __bf16 b = (__bf16)f; return (float)b; }
__device__ __forceinline__ unsigned pack2(float a, float b) { bf2 v = {(__bf16)a, (__bf16)b}; return __builtin_bit_cast(unsigned, v); }
__device__ __forceinline__ float dot2(unsigned a, unsigned b, float c) {
  return __builtin_amdgcn_fdot2_f32_bf16(__builtin_bit_cast(bf2, a), __builtin_bit_cast(bf2, b), c, false);
}
template <int CTRL> __device__ __forceinline__ unsigned dppu(unsigned v) { return __builtin_amdgcn_update_dpp(0u, v, CTRL, 0xf, 0xf, true); }
template <int CTRL> __device__ __forceinline__ float dppf(float v) { return asf(dppu<CTRL>(asu(v))); }
constexpr int X1 = 0xB1, X2 = 0x4E, HM = 0x141, R4 = 0x124, R8 = 0x128;
__device__ __forceinline__ float rs32(float a, float b) {
  auto r = __builtin_amdgcn_permlane32_swap(asu(a), asu(b), false, false);
  unsigned r0 = r[0], r1 = r[1]; return asf(r0) + asf(r1);
}
__device__ __forceinline__ float rs16(float a, float b) {
  auto r = __builtin_amdgcn_permlane16_swap(asu(a), asu(b), false, false);
  unsigned r0 = r[0], r1 = r[1]; return asf(r0) + asf(r1);
}
__device__ __forceinline__ float mx32(float a) {
  auto r = __builtin_amdgcn_permlane32_swap(asu(a), asu(a), false, false);
  unsigned r0 = r[0], r1 = r[1]; return fmaxf(asf(r0), asf(r1));
}
__device__ __forceinline__ float mx16(float a) {
  auto r = __builtin_amdgcn_permlane16_swap(asu(a), asu(a), false, false);
  unsigned r0 = r[0], r1 = r[1]; return fmaxf(asf(r0), asf(r1));
}
__device__ __forceinline__ float wave_sum(float v) {
  v += dppf<X1>(v); v += dppf<X2>(v); v += dppf<R4>(v); v += dppf<R8>(v);
  v = rs16(v, v); v = rs32(v, v); return v;
}
__device__ __forceinline__ float ex2(float x) { return __builtin_amdgcn_exp2f(x); }
__device__ __forceinline__ float rcp(float x) { return __builtin_amdgcn_rcpf(x); }
__device__ __forceinline__ float silu(float g) { return g * rcp(1.f + ex2(-g * LOG2E)); }
__device__ __forceinline__ unsigned canon(float f) { unsigned u = asu(f); return u < 0xfffffffeu ? u : 0xfffffffeu; }
__device__ __forceinline__ unsigned umax(unsigned a, unsigned b) { return a > b ? a : b; }
__device__ __forceinline__ bool all_true(bool ok) { return __builtin_amdgcn_ballot_w64(!ok) == 0; }
__device__ __forceinline__ rsrc_t mk(const void* p, unsigned n) { return __builtin_amdgcn_make_buffer_rsrc((void*)p, 0, n, 0x00020000); }
#define LD128(r, v, aux) __builtin_amdgcn_raw_buffer_load_b128(r, v, 0, aux)
#define LD96(r, v, aux) __builtin_amdgcn_raw_buffer_load_b96(r, v, 0, aux)
#define LD64(r, v, aux) __builtin_amdgcn_raw_buffer_load_b64(r, v, 0, aux)
#define LD32(r, v, aux) __builtin_amdgcn_raw_buffer_load_b32(r, v, 0, aux)
#define LD16(r, v, aux) __builtin_amdgcn_raw_buffer_load_b16(r, v, 0, aux)
#define SB() __builtin_amdgcn_sched_barrier(0)
#define MEMFENCE() asm volatile("" ::: "memory")
#define WAIT_LGKM0() asm volatile("s_waitcnt lgkmcnt(0)" ::: "memory")
#define WAIT_VM0() asm volatile("s_waitcnt vmcnt(0)" ::: "memory")
#define PIN(x) asm volatile("" : "+v"(x))
__device__ __forceinline__ void wg_barrier() { WAIT_LGKM0(); __builtin_amdgcn_s_barrier(); MEMFENCE(); }

struct __attribute__((aligned(16))) Lds {
  float rec_o[8][128];
  float rec_ml[8][2];
  float dn[4][576];
  float hp[4][64];
  unsigned ob[4][64];
  unsigned qs[64];
  float st[4][2];
  unsigned cnt[4];
};

__device__ __forceinline__ void lds_wait(const unsigned* w, unsigned target) {
  _Pragma("unroll 1") for (int it = 0; it < LCAP; ++it) {
    unsigned v = __hip_atomic_load(w, __ATOMIC_RELAXED, __HIP_MEMORY_SCOPE_WORKGROUP);
    if (__builtin_amdgcn_readfirstlane(v) >= target) break;
    __builtin_amdgcn_s_sleep(1);
  }
  MEMFENCE();
}

struct Slab { unsigned p[4][8]; unsigned z[4]; };
__device__ __forceinline__ void interleave(unsigned (&o)[8], v4u a, v4u b) {
#pragma unroll
  for (int d = 0; d < 4; ++d) {
    unsigned x = a[d], y = b[d];
    o[2 * d] = __builtin_amdgcn_perm(y, x, 0x05040100u);
    o[2 * d + 1] = __builtin_amdgcn_perm(y, x, 0x07060302u);
  }
}
__device__ __forceinline__ void slab_make(Slab& s, const v4u (&w)[9]) {
  interleave(s.p[0], w[0], w[1]); interleave(s.p[1], w[3], w[4]); interleave(s.p[2], w[6], w[7]); interleave(s.p[3], w[2], w[5]);
#pragma unroll
  for (int d = 0; d < 4; ++d) s.z[d] = w[8][d];
#pragma unroll
  for (int p = 0; p < 4; ++p) {
#pragma unroll
    for (int d = 0; d < 8; ++d) PIN(s.p[p][d]);
    PIN(s.z[p]);
  }
}
struct Hv { unsigned p[4], za, zb; };
__device__ __forceinline__ void hv_make(Hv& h, float a, float b, float c) {
  unsigned ab = pack2(a, b);
  h.p[0] = dppu<0x00>(ab); h.p[1] = dppu<0x55>(ab); h.p[2] = dppu<0xAA>(ab);
  unsigned c0 = dppu<0x00>(asu(c)), c1 = dppu<0x55>(asu(c)), c2 = dppu<0xAA>(asu(c));
  h.p[3] = __builtin_amdgcn_perm(c1, c0, 0x07060302u);
  h.zb = c2 & 0xffff0000u; h.za = c2 >> 16;
}
__device__ __forceinline__ void slab_dot(float (&acc)[8], const Slab& s, const Hv& h) {
#pragma unroll
  for (int d = 0; d < 4; ++d) { acc[2 * d] = dot2(s.z[d], h.za, 0.f); acc[2 * d + 1] = dot2(s.z[d], h.zb, 0.f); }
#pragma unroll
  for (int p = 0; p < 4; ++p)
#pragma unroll
    for (int d = 0; d < 8; ++d) acc[d] = dot2(s.p[p][d], h.p[p], acc[d]);
}
__device__ __forceinline__ void ln_stats(float s, float q, float& m, float& rsd) {
  float mean = s * (1.f / 576.f);
  float var = fmaxf(q * (1.f / 576.f) - mean * mean, 0.f);
  m = rbf(mean);
  float sd = rbf(__builtin_sqrtf(rbf(rbf(var) + 1e-5f)));
  rsd = 1.f / sd;
}
__device__ __forceinline__ float modulate(float t, float scale, float shift) { return rbf(rbf(t * rbf(1.f + scale)) + shift); }

#define STAMP(k) do {} while (0)
#define FLUSH() do {} while (0)

__global__ void __launch_bounds__(768)
fused_block(const u16* __restrict__ xin, const u16* __restrict__ Kc, const u16* __restrict__ Vc, const u16* __restrict__ wq,
            const u16* __restrict__ wo, const u16* __restrict__ wg, const u16* __restrict__ wu, const u16* __restrict__ wd,
            const u16* __restrict__ cin, const u16* __restrict__ wmod, u16* __restrict__ out, char* ws) {
  __shared__ Lds L;
  const int tid = threadIdx.x, lane = tid & 63, wave = __builtin_amdgcn_readfirstlane(tid >> 6);
  const int b = blockIdx.x, x = b & 7, head = b >> 3, r = (head + 4 * x) & 31;
  asm volatile("" :: "s"(xin), "s"(Kc), "s"(Vc), "s"(wq), "s"(wo), "s"(wg), "s"(wu), "s"(wd), "s"(cin), "s"(wmod), "s"(out), "s"(ws));
  STAMP(0);

  if (wave >= 4) {
    const int s = wave - 4, k = r >> 2;
    const int step = (head & 3) ? 58 : 62;
    const int start = k < 3 ? k * step : 182 + (k - 3) * 66;
    const int cnt = r >= 9 ? 66 : ((r & 3) == 0 && r < 8 ? 62 : 58);
    const int n = (cnt - s + 7) >> 3;
    const size_t base = ((size_t)head * 512 + start) * 2048;
    const rsrc_t rk = mk(Kc + base, cnt * 4096), rv = mk(Vc + base, cnt * 4096);
    const unsigned vo = lane * 16;
    unsigned so = s * 4096;
    __builtin_amdgcn_s_barrier();
    v4u Kr[3][4], Vr[3][4];
#pragma unroll
    for (int i = 0; i < 3; ++i) {
#pragma unroll
      for (int t = 0; t < 4; ++t) {
        Kr[i][t] = __builtin_amdgcn_raw_buffer_load_b128(rk, vo + t * 1024, so + i * 32768, AUX_NT);
        if (RAMP && i == 0 && t == RAMP - 1) { SB(); asm volatile("s_waitcnt vmcnt(%0)" :: "n"(RAMP ? RAMP - 1 : 0)); }
        if (THR) { SB(); asm volatile("s_waitcnt vmcnt(%0)" :: "n"(THR ? THR - 1 : 0)); }
      }
#pragma unroll
      for (int t = 0; t < 4; ++t) {
        Vr[i][t] = __builtin_amdgcn_raw_buffer_load_b128(rv, vo + t * 1024, so + i * 32768, AUX_NT);
        if (THR) { SB(); asm volatile("s_waitcnt vmcnt(%0)" :: "n"(THR ? THR - 1 : 0)); }
      }
    }
    so += 3 * 32768;
    SB();
    STAMP(1);
    lds_wait(&L.cnt[1], 1);
    const v4u qv = *(const v4u*)&L.qs[(lane & 15) * 4];
    const unsigned q0 = qv[0], q1 = qv[1], q2 = qv[2], q3 = qv[3];
    STAMP(2);
    float m = -INFINITY, l = 0.f;
    v2f acc[4] = {{0.f, 0.f}, {0.f, 0.f}, {0.f, 0.f}, {0.f, 0.f}};
#define STEP(i, REFILL)                                                                             \
      {                                                                                             \
        float sc[4];                                                                                \
        _Pragma("unroll") for (int t = 0; t < 4; ++t) {                                             \
          const v4u kk = Kr[i][t];                                                                  \
          sc[t] = dot2(kk[3], q3, dot2(kk[2], q2, dot2(kk[1], q1, dot2(kk[0], q0, 0.f))));          \
        }                                                                                           \
        SB();                                                                                       \
        if (REFILL) { _Pragma("unroll") for (int t = 0; t < 4; ++t) { Kr[i][t] = __builtin_amdgcn_raw_buffer_load_b128(rk, vo + t * 1024, so, AUX_NT); SB(); } } \
        _Pragma("unroll") for (int t = 0; t < 4; ++t) sc[t] += dppf<X1>(sc[t]);                     \
        SB();                                                                                       \
        _Pragma("unroll") for (int t = 0; t < 4; ++t) sc[t] += dppf<X2>(sc[t]);                     \
        SB();                                                                                       \
        _Pragma("unroll") for (int t = 0; t < 4; ++t) sc[t] += dppf<R4>(sc[t]);                     \
        SB();                                                                                       \
        _Pragma("unroll") for (int t = 0; t < 4; ++t) sc[t] += dppf<R8>(sc[t]);                     \
        SB();                                                                                       \
        _Pragma("unroll") for (int t = 0; t < 4; ++t) sc[t] = rbf(rbf(sc[t]) * 0.08838834764831845f); \
        const float mn = fmaxf(fmaxf(fmaxf(sc[0], sc[1]), fmaxf(sc[2], sc[3])), m);                 \
        const float al = ex2((m - mn) * LOG2E);                                                     \
        m = mn;                                                                                     \
        float p[4];                                                                                 \
        _Pragma("unroll") for (int t = 0; t < 4; ++t) p[t] = ex2((sc[t] - mn) * LOG2E);             \
        l = l * al + ((p[0] + p[1]) + (p[2] + p[3]));                                               \
        _Pragma("unroll") for (int j = 0; j < 4; ++j) acc[j] *= al;                                 \
        _Pragma("unroll") for (int t = 0; t < 4; ++t) {                                             \
          const v4u vv = Vr[i][t];                                                                  \
          const v2f pp = {p[t], p[t]};                                                              \
          _Pragma("unroll") for (int j = 0; j < 4; ++j) {                                           \
            const v2f e = {lo16(vv[j]), hi16(vv[j])};                                               \
            acc[j] = __builtin_elementwise_fma(pp, e, acc[j]);                                      \
          }                                                                                         \
        }                                                                                           \
        PIN(acc[0]); PIN(acc[1]); PIN(acc[2]); PIN(acc[3]); PIN(l); PIN(m);                         \
        SB();                                                                                       \
        if (REFILL) { _Pragma("unroll") for (int t = 0; t < 4; ++t) { Vr[i][t] = __builtin_amdgcn_raw_buffer_load_b128(rv, vo + t * 1024, so, AUX_NT); SB(); } so += 32768; } \
      }
    int left = n;
    _Pragma("unroll 1") do { STEP(0, 1) STEP(1, 1) STEP(2, 1) left -= 3; } while (left > 3);
    if (lane == 0) __hip_atomic_fetch_add(&L.cnt[2], 1u, __ATOMIC_RELAXED, __HIP_MEMORY_SCOPE_WORKGROUP);
    STAMP(3);
    __builtin_amdgcn_s_setprio(3);
    STEP(0, 0)
    if (left > 1) { STEP(1, 0) if (left > 2) STEP(2, 0) }
#undef STEP
    STAMP(4);
    const float M = mx32(mx16(m));
    const float w = ex2((m - M) * LOG2E);
    l *= w; l = rs16(l, l); l = rs32(l, l);
    float a[8];
#pragma unroll
    for (int j = 0; j < 4; ++j) { a[2 * j] = acc[j].x * w; a[2 * j + 1] = acc[j].y * w; }
    float y[4];
#pragma unroll
    for (int j = 0; j < 4; ++j) y[j] = rs32(a[j], a[j + 4]);
    v2f z = {rs16(y[0], y[2]), rs16(y[1], y[3])};
    *(v2f*)&L.rec_o[s][(lane & 15) * 8 + (lane >> 4) * 2] = z;
    if (lane == 0) { v2f ml = {M, l}; *(v2f*)&L.rec_ml[s][0] = ml; }
    WAIT_LGKM0();
    unsigned old = 0;
    if (lane == 0) old = __hip_atomic_fetch_add(&L.cnt[3], 1u, __ATOMIC_RELAXED, __HIP_MEMORY_SCOPE_WORKGROUP);
    old = __builtin_amdgcn_readfirstlane(old);
    if (old != 7) { FLUSH(); return; }
    MEMFENCE();
    {
      float ms[8], ls[8];
#pragma unroll
      for (int j = 0; j < 4; ++j) { v4f t = *(const v4f*)&L.rec_ml[2 * j][0]; ms[2 * j] = t.x; ls[2 * j] = t.y; ms[2 * j + 1] = t.z; ls[2 * j + 1] = t.w; }
      v4f o[8];
#pragma unroll
      for (int j = 0; j < 8; ++j) o[j] = *(const v4f*)&L.rec_o[j][(lane & 31) * 4];
      const float MM = fmaxf(fmaxf(fmaxf(ms[0], ms[1]), fmaxf(ms[2], ms[3])), fmaxf(fmaxf(ms[4], ms[5]), fmaxf(ms[6], ms[7])));
      float LL = 0.f; v4f oo = {0.f, 0.f, 0.f, 0.f};
#pragma unroll
      for (int j = 0; j < 8; ++j) { const float e = ex2((ms[j] - MM) * LOG2E); LL += e * ls[j]; oo += o[j] * e; }
      if (lane >= 32) { oo.x = MM; oo.y = LL; oo.z = 0.f; oo.w = 0.f; }
      v4u pv = {canon(oo.x), canon(oo.y), canon(oo.z), canon(oo.w)};
      const unsigned off = lane < 32 ? OFF_AO + b * 512 + lane * 16 : (lane == 32 ? OFF_AM + b * 16 : OOB);
      __builtin_amdgcn_raw_buffer_store_b128(pv, mk(ws, WS_END), off, 0, AUX_SC1);
      if (lane == 0) __hip_atomic_fetch_add(&L.cnt[3], 1u, __ATOMIC_RELAXED, __HIP_MEMORY_SCOPE_WORKGROUP);
      STAMP(5);
    }
    FLUSH();
    return;
  }

  __builtin_amdgcn_s_setprio(3);
  const rsrc_t rws = mk(ws, WS_END);
  unsigned tag_hi, tag_lo;
  auto set_tag = [&](v2u e) { const u64 t = ((((u64)e[1] << 32) | e[0]) & ~0xffffull) + 0x10000ull; tag_lo = (unsigned)t; tag_hi = (unsigned)(t >> 32); };
  const int qr = lane >> 2, qc = lane & 3;
  const int row0 = 144 * wave + 9 * qr;
  const int rowl = row0 + 3 * qc;
  const bool act3 = qc < 3;
  const int u = r - 1 - (r > 4);
  const int task = (r == 0 || r == 4) ? 999 : 2 * ((u >> 1) * 8 + x) + (u & 1);
  if (tid < 4) L.cnt[tid] = 0;

  if (task < 236) {
    const bool is_mod = task < 108;
    const unsigned ld = is_mod ? 3456 * 2 : 4096 * 2;
    const rsrc_t rw = mk(is_mod ? wmod + task * 32 : wq + (task - 108) * 32, 575 * ld + 64);
    const rsrc_t rin = mk(is_mod ? cin : xin, 1152);
    const unsigned vi = act3 ? rowl * 2 : OOB;
    const unsigned i0 = LD16(rin, vi, 0), i1 = LD16(rin, vi + 2, 0), i2 = LD16(rin, vi + 4, 0);
    const v4u xa = LD128(rin, lane * 16, 0), xb = LD128(rin, lane * 16 + 1024, 0);
    v4u w[9];
#pragma unroll
    for (int t = 0; t < 9; ++t) w[t] = LD128(rw, (row0 + t) * ld + qc * 16, AUX_NT);
    const v2u ep = LD64(rws, 0, 0);
    if (task < 36) {
      const v4u ones = {~0u, ~0u, ~0u, ~0u};
#pragma unroll
      for (int t = 0; t < 4; ++t)
        __builtin_amdgcn_raw_buffer_store_b128(ones, rws, OFF_AO + ((task * 4 + wave) * 4 + t) * 1024 + lane * 16, 0, AUX_SC1);
    }
    SB();
    wg_barrier();
    float h0 = lo16(i0), h1 = lo16(i1), h2 = lo16(i2);
    set_tag(ep);
    Slab S;
    if (is_mod) {
      h0 = rbf(silu(h0)); h1 = rbf(silu(h1)); h2 = rbf(silu(h2));
      slab_make(S, w);
      STAMP(1);
    } else {
      float s = 0.f, q = 0.f;
#pragma unroll
      for (int d = 0; d < 4; ++d) {
        float e0 = lo16(xa[d]), e1 = hi16(xa[d]), e2 = lo16(xb[d]), e3 = hi16(xb[d]);
        s += (e0 + e1) + (e2 + e3); q += (e0 * e0 + e1 * e1) + (e2 * e2 + e3 * e3);
      }
      s = wave_sum(s); q = wave_sum(q);
      float m, rsd; ln_stats(s, q, m, rsd);
      h0 = rbf(rbf(h0 - m) * rsd); h1 = rbf(rbf(h1 - m) * rsd); h2 = rbf(rbf(h2 - m) * rsd);
      PIN(h0); PIN(h1); PIN(h2);
      slab_make(S, w);
      STAMP(1);
      const unsigned vu = act3 ? OFF_U + wave * 2304 + (3 * qr + qc) * 16 : OOB;
      v4u g0, g1, g2;
      _Pragma("unroll 1") for (int it = 0; it < GCAP; ++it) {
        g0 = LD128(rws, vu, AUX_SC1); g1 = LD128(rws, vu + 768, AUX_SC1); g2 = LD128(rws, vu + 1536, AUX_SC1);
        unsigned bad = (((g0[0] ^ tag_lo) | (g0[2] ^ tag_lo) | (g1[0] ^ tag_lo) | (g1[2] ^ tag_lo) | (g2[0] ^ tag_lo) | (g2[2] ^ tag_lo)) >> 16) |
                       (g0[1] ^ tag_hi) | (g0[3] ^ tag_hi) | (g1[1] ^ tag_hi) | (g1[3] ^ tag_hi) | (g2[1] ^ tag_hi) | (g2[3] ^ tag_hi);
        if (all_true(bad == 0 || !act3)) break;
        MEMFENCE();
      }
      h0 = modulate(h0, lo16(g0[2]), lo16(g0[0])); h1 = modulate(h1, lo16(g1[2]), lo16(g1[0])); h2 = modulate(h2, lo16(g2[2]), lo16(g2[0]));
    }
    STAMP(2);
    Hv H; hv_make(H, h0, h1, h2);
    float acc[8]; slab_dot(acc, S, H);
    float y[4];
#pragma unroll
    for (int j = 0; j < 4; ++j) y[j] = rs32(acc[j], acc[j + 4]);
    float z0 = rs16(y[0], y[2]), z1 = rs16(y[1], y[3]);
    z0 += dppf<R4>(z0); z1 += dppf<R4>(z1); z0 += dppf<R8>(z0); z1 += dppf<R8>(z1);
    if ((lane & 12) == 0) { v2f z = {z0, z1}; *(v2f*)&L.hp[wave][qc * 8 + (lane >> 4) * 2] = z; }
    WAIT_VM0();
    WAIT_LGKM0();
    STAMP(9);
    if (wave != 0) {
      if (lane == 0) __hip_atomic_fetch_add(&L.cnt[0], 1u, __ATOMIC_RELAXED, __HIP_MEMORY_SCOPE_WORKGROUP);
    } else {
      lds_wait(&L.cnt[0], 3);
      const int cl = lane & 31;
      const float v = (L.hp[0][cl] + L.hp[1][cl]) + (L.hp[2][cl] + L.hp[3][cl]);
      const unsigned bits = pack2(v, 0.f) & 0xffffu;
      unsigned off;
      if (is_mod) {
        const int kind = task / 18, col = (task - kind * 18) * 32 + cl;
        const int cw = col / 144, cr = col - cw * 144, cj = cr / 3, cu = cr - cj * 3;
        off = kind < 2 ? OFF_U + cw * 2304 + cu * 768 + cj * 16 + kind * 8 : OFF_W + col * 32 + (kind - 2) * 8;
      } else {
        off = OFF_Q + ((task - 108) * 32 + cl) * 8;
      }
      v2u g = {bits | tag_lo, tag_hi};
      __builtin_amdgcn_raw_buffer_store_b64(g, rws, lane < 32 ? off : OOB, 0, AUX_SC1);
      STAMP(3);
    }
  } else {
    const v2u ep = LD64(rws, 0, 0);
    wg_barrier();
    set_tag(ep);
  }

  if (wave == 0) {
    v4u g;
    _Pragma("unroll 1") for (int it = 0; it < GCAP; ++it) {
      g = LD128(rws, OFF_Q + head * 1024 + lane * 16, AUX_SC1);
      unsigned bad = (((g[0] ^ tag_lo) | (g[2] ^ tag_lo)) >> 16) | (g[1] ^ tag_hi) | (g[3] ^ tag_hi);
      if (all_true(bad == 0)) break;
      MEMFENCE();
    }
    L.qs[lane] = (g[0] & 0xffffu) | (g[2] << 16);
    WAIT_LGKM0();
    if (lane == 0) __hip_atomic_store(&L.cnt[1], 1u, __ATOMIC_RELAXED, __HIP_MEMORY_SCOPE_WORKGROUP);
    STAMP(4);
  } else {
    lds_wait(&L.cnt[1], 1);
  }

  if (r < 9) {
    const int r8 = lane >> 3, c8 = lane & 7;
    const rsrc_t rw = mk(wo + (size_t)(512 * x + 128 * wave) * 576 + 64 * r, 127 * 1152 + 128);
    v4u w[16];
#pragma unroll
    for (int t = 0; t < 16; ++t) w[t] = LD128(rw, (16 * r8 + t) * 1152 + c8 * 16, AUX_NT);
    unsigned P[8][8];
#pragma unroll
    for (int p = 0; p < 8; ++p) {
      interleave(P[p], w[2 * p], w[2 * p + 1]);
#pragma unroll
      for (int d = 0; d < 8; ++d) PIN(P[p][d]);
    }
    lds_wait(&L.cnt[3], 9);
    STAMP(5);
    const int rec = (4 * x + wave) * 8;
    v2u o[8], ml;
    _Pragma("unroll 1") for (int it = 0; it < GCAP; ++it) {
      ml = LD64(rws, OFF_AM + (rec + c8) * 16, AUX_SC1);
#pragma unroll
      for (int j = 0; j < 8; ++j) o[j] = LD64(rws, OFF_AO + (rec + j) * 512 + lane * 8, AUX_SC1);
      unsigned mxu = umax(ml[0], ml[1]);
#pragma unroll
      for (int j = 0; j < 8; ++j) mxu = umax(mxu, umax(o[j][0], o[j][1]));
      if (all_true(mxu != ~0u)) break;
      MEMFENCE();
    }
    STAMP(6);
    const float mi = asf(ml[0]);
    float M = fmaxf(mi, dppf<X1>(mi)); M = fmaxf(M, dppf<X2>(M)); M = fmaxf(M, dppf<HM>(M));
    const float e = ex2((mi - M) * LOG2E);
    float Ls = e * asf(ml[1]); Ls += dppf<X1>(Ls); Ls += dppf<X2>(Ls); Ls += dppf<HM>(Ls);
    float o0 = 0.f, o1 = 0.f;
#pragma unroll
    for (int j = 0; j < 8; ++j) {
      const float ej = asf(__builtin_amdgcn_readlane(asu(e), j));
      o0 = fmaf(ej, asf(o[j][0]), o0); o1 = fmaf(ej, asf(o[j][1]), o1);
    }
    const float rl = rcp(Ls);
    L.ob[wave][lane] = pack2(o0 * rl, o1 * rl);
    WAIT_LGKM0();
    const v4u oa = *(const v4u*)&L.ob[wave][r8 * 8], ob = *(const v4u*)&L.ob[wave][r8 * 8 + 4];
    float acc[8] = {0.f, 0.f, 0.f, 0.f, 0.f, 0.f, 0.f, 0.f};
#pragma unroll
    for (int p = 0; p < 8; ++p) {
      const unsigned hp = p < 4 ? oa[p & 3] : ob[p & 3];
#pragma unroll
      for (int d = 0; d < 8; ++d) acc[d] = dot2(P[p][d], hp, acc[d]);
    }
    float y[4];
#pragma unroll
    for (int j = 0; j < 4; ++j) y[j] = rs32(acc[j], acc[j + 4]);
    float z0 = rs16(y[0], y[2]), z1 = rs16(y[1], y[3]);
    z0 += dppf<R8>(z0); z1 += dppf<R8>(z1);
    if ((lane & 8) == 0) { v2f z = {z0, z1}; *(v2f*)&L.hp[wave][c8 * 8 + (lane >> 4) * 2] = z; }
    wg_barrier();
    if (wave == 0) {
      const int c4 = (lane & 15) * 4;
      const v4f t = (*(const v4f*)&L.hp[0][c4] + *(const v4f*)&L.hp[1][c4]) + (*(const v4f*)&L.hp[2][c4] + *(const v4f*)&L.hp[3][c4]);
      v4u pv = {canon(t.x), canon(t.y), canon(t.z), canon(t.w)};
      __builtin_amdgcn_raw_buffer_store_b128(pv, rws, lane < 16 ? OFF_B + x * 2304 + r * 256 + lane * 16 : OOB, 0, AUX_SC1);
      STAMP(7);
    }
  } else if (r < 27) {
    const int f = 18 * x + (r - 9);
    const rsrc_t rx = mk(xin, 1152);
    const unsigned vi = act3 ? rowl * 2 : OOB;
    const float x0 = lo16(LD16(rx, vi, 0)), x1 = lo16(LD16(rx, vi + 2, 0)), x2 = lo16(LD16(rx, vi + 4, 0));
    float ga[3], sb[3], cb[3];
    {
      const unsigned vw = act3 ? OFF_W + rowl * 32 : OOB;
      v4u g[3]; v2u h[3];
      _Pragma("unroll 1") for (int it = 0; it < GCAP; ++it) {
        unsigned bad = 0;
#pragma unroll
        for (int j = 0; j < 3; ++j) {
          if (it == 0) { g[j] = LD128(rws, vw + j * 32, 0); h[j] = LD64(rws, vw + j * 32 + 16, 0); }
          else { g[j] = LD128(rws, vw + j * 32, AUX_SC1); h[j] = LD64(rws, vw + j * 32 + 16, AUX_SC1); }
        }
#pragma unroll
        for (int j = 0; j < 3; ++j)
          bad |= (((g[j][0] ^ tag_lo) | (g[j][2] ^ tag_lo) | (h[j][0] ^ tag_lo)) >> 16) | (g[j][1] ^ tag_hi) | (g[j][3] ^ tag_hi) | (h[j][1] ^ tag_hi);
        if (all_true(bad == 0 || !act3)) break;
        MEMFENCE();
      }
#pragma unroll
      for (int j = 0; j < 3; ++j) { ga[j] = lo16(g[j][0]); sb[j] = lo16(g[j][2]); cb[j] = rbf(1.f + lo16(h[j][0])); PIN(ga[j]); PIN(sb[j]); PIN(cb[j]); }
    }
    __builtin_amdgcn_s_setprio(0);
    lds_wait(&L.cnt[2], 8);
    STAMP(5);
    const rsrc_t rg = mk(wg + f * 32, 575 * 9216 + 64), ru = mk(wu + f * 32, 575 * 9216 + 64);
    const rsrc_t rd = mk(wd + (size_t)(32 * f + 8 * wave) * 576, 8 * 1152);
    v4u w1[9], w2[9], w3[8]; unsigned w4[8];
#pragma unroll
    for (int t = 0; t < 9; ++t) w1[t] = LD128(rg, (row0 + t) * 9216 + qc * 16, AUX_NT);
#pragma unroll
    for (int t = 0; t < 9; ++t) w2[t] = LD128(ru, (row0 + t) * 9216 + qc * 16, AUX_NT);
#pragma unroll
    for (int t = 0; t < 8; ++t) { w3[t] = LD128(rd, t * 1152 + lane * 16, AUX_NT); w4[t] = LD16(rd, t * 1152 + 1024 + lane * 2, AUX_NT); }
    SB();
    Slab G, U; slab_make(G, w1); slab_make(U, w2);
    unsigned Dp[4][8], Dt[4];
#pragma unroll
    for (int p = 0; p < 4; ++p) {
      interleave(Dp[p], w3[2 * p], w3[2 * p + 1]); Dt[p] = w4[2 * p] | (w4[2 * p + 1] << 16);
#pragma unroll
      for (int d = 0; d < 8; ++d) PIN(Dp[p][d]);
      PIN(Dt[p]);
    }
    STAMP(6);
    const unsigned vb = act3 ? OFF_B + rowl * 4 : OOB;
    v3u yb[8];
    _Pragma("unroll 1") for (int it = 0; it < GCAP; ++it) {
#pragma unroll
      for (int j = 0; j < 8; ++j) yb[j] = LD96(rws, vb + j * 2304, AUX_SC1);
      unsigned mxu = 0;
#pragma unroll
      for (int j = 0; j < 8; ++j) mxu = umax(umax(mxu, yb[j][0]), umax(yb[j][1], yb[j][2]));
      if (all_true(mxu != ~0u)) break;
      MEMFENCE();
    }
    STAMP(7);
    float xr[3];
#pragma unroll
    for (int j = 0; j < 3; ++j) {
      const float t = ((asf(yb[0][j]) + asf(yb[1][j])) + (asf(yb[2][j]) + asf(yb[3][j]))) + ((asf(yb[4][j]) + asf(yb[5][j])) + (asf(yb[6][j]) + asf(yb[7][j])));
      xr[j] = rbf((j == 0 ? x0 : j == 1 ? x1 : x2) + rbf(ga[j] * rbf(t)));
    }
    float s = (xr[0] + xr[1]) + xr[2], q = (xr[0] * xr[0] + xr[1] * xr[1]) + xr[2] * xr[2];
    s = wave_sum(s); q = wave_sum(q);
    if (lane == 0) { v2f t = {s, q}; *(v2f*)&L.st[wave][0] = t; }
    wg_barrier();
    {
      const v4f t0 = *(const v4f*)&L.st[0][0], t1 = *(const v4f*)&L.st[2][0];
      s = (t0.x + t0.z) + (t1.x + t1.z); q = (t0.y + t0.w) + (t1.y + t1.w);
    }
    float m, rsd; ln_stats(s, q, m, rsd);
#pragma unroll
    for (int j = 0; j < 3; ++j) xr[j] = rbf(rbf(rbf(rbf(xr[j] - m) * rsd) * cb[j]) + sb[j]);
    Hv H; hv_make(H, xr[0], xr[1], xr[2]);
    float ag[8], au[8]; slab_dot(ag, G, H); slab_dot(au, U, H);
    float y[8];
#pragma unroll
    for (int j = 0; j < 8; ++j) y[j] = rs32(ag[j], au[j]);
    v4f z;
#pragma unroll
    for (int j = 0; j < 4; ++j) { float t = rs16(y[j], y[j + 4]); t += dppf<R4>(t); t += dppf<R8>(t); z[j] = t; }
    if ((lane & 12) == 0) *(v4f*)&L.hp[wave][(lane >> 5) * 32 + qc * 8 + ((lane >> 4) & 1) * 4] = z;
    wg_barrier();
    const int un = 8 * wave + (lane & 7);
    const float g = rbf((L.hp[0][un] + L.hp[1][un]) + (L.hp[2][un] + L.hp[3][un]));
    const float up = rbf((L.hp[0][32 + un] + L.hp[1][32 + un]) + (L.hp[2][32 + un] + L.hp[3][32 + un]));
    const float av = rbf(silu(g)) * up;
    const unsigned ap = pack2(av, dppf<X1>(av));
    float d8[8] = {0.f, 0.f, 0.f, 0.f, 0.f, 0.f, 0.f, 0.f}, dt = 0.f;
#pragma unroll
    for (int p = 0; p < 4; ++p) {
      const unsigned a2 = __builtin_amdgcn_readlane(ap, 2 * p);
#pragma unroll
      for (int d = 0; d < 8; ++d) d8[d] = dot2(Dp[p][d], a2, d8[d]);
      dt = dot2(Dt[p], a2, dt);
    }
    { v4f t0 = {d8[0], d8[1], d8[2], d8[3]}, t1 = {d8[4], d8[5], d8[6], d8[7]};
      *(v4f*)&L.dn[wave][lane * 8] = t0; *(v4f*)&L.dn[wave][lane * 8 + 4] = t1; L.dn[wave][512 + lane] = dt; }
    wg_barrier();
    {
      const int c4 = 144 * wave + (lane < 36 ? lane : 0) * 4;
      const v4f t = (*(const v4f*)&L.dn[0][c4] + *(const v4f*)&L.dn[1][c4]) + (*(const v4f*)&L.dn[2][c4] + *(const v4f*)&L.dn[3][c4]);
      v4u pv = {canon(t.x), canon(t.y), canon(t.z), canon(t.w)};
      __builtin_amdgcn_raw_buffer_store_b128(pv, rws, lane < 36 ? OFF_C + f * 2304 + c4 * 4 : OOB, 0, AUX_SC1);
    }
    STAMP(8);
  } else {
    const int j = (r - 27) * 8 + x;
    if (wave != 0 || j >= 36) { FLUSH(); return; }
    const int col = 16 * j + 4 * qc + (lane >> 4);
    const float xv = lo16(LD16(mk(xin, 1152), col * 2, 0));
    v2u ga, gb;
    _Pragma("unroll 1") for (int it = 0; it < GCAP; ++it) {
      ga = LD64(rws, OFF_W + col * 32, AUX_SC1); gb = LD64(rws, OFF_W + col * 32 + 24, AUX_SC1);
      unsigned bad = (((ga[0] ^ tag_lo) | (gb[0] ^ tag_lo)) >> 16) | (ga[1] ^ tag_hi) | (gb[1] ^ tag_hi);
      if (all_true(bad == 0)) break;
      MEMFENCE();
    }
    lds_wait(&L.cnt[3], 9);
    STAMP(5);
    unsigned yb[8];
    _Pragma("unroll 1") for (int it = 0; it < GCAP; ++it) {
#pragma unroll
      for (int t = 0; t < 8; ++t) yb[t] = LD32(rws, OFF_B + t * 2304 + col * 4, AUX_SC1);
      unsigned mxu = 0;
#pragma unroll
      for (int t = 0; t < 8; ++t) mxu = umax(mxu, yb[t]);
      if (all_true(mxu != ~0u)) break;
      MEMFENCE();
    }
    const float ys = ((asf(yb[0]) + asf(yb[1])) + (asf(yb[2]) + asf(yb[3]))) + ((asf(yb[4]) + asf(yb[5])) + (asf(yb[6]) + asf(yb[7])));
    float x1 = rbf(xv + rbf(lo16(ga[0]) * rbf(ys)));
    PIN(x1);
    STAMP(6);
    v4u cc[9];
    _Pragma("unroll 1") for (int it = 0; it < GCAP; ++it) {
#pragma unroll
      for (int t = 0; t < 9; ++t) cc[t] = LD128(rws, OFF_C + (16 * t + qr) * 2304 + j * 64 + qc * 16, AUX_SC1);
      unsigned mxu = 0;
#pragma unroll
      for (int t = 0; t < 9; ++t) mxu = umax(umax(mxu, umax(cc[t][0], cc[t][1])), umax(cc[t][2], cc[t][3]));
      if (all_true(mxu != ~0u)) break;
      MEMFENCE();
    }
    STAMP(7);
    float a[4];
#pragma unroll
    for (int e = 0; e < 4; ++e)
      a[e] = (((asf(cc[0][e]) + asf(cc[1][e])) + (asf(cc[2][e]) + asf(cc[3][e]))) + ((asf(cc[4][e]) + asf(cc[5][e])) + (asf(cc[6][e]) + asf(cc[7][e])))) + asf(cc[8][e]);
    float t = rs16(rs32(a[0], a[2]), rs32(a[1], a[3]));
    t += dppf<R4>(t); t += dppf<R8>(t);
    const float res = x1 + rbf(lo16(gb[0]) * rbf(t));
    __builtin_amdgcn_raw_buffer_store_b16((u16)pack2(res, 0.f), mk(out, 1152), (lane & 12) == 0 ? col * 2 : OOB, 0, 0);
    if (j == 0) { v2u e = {tag_lo, tag_hi}; __builtin_amdgcn_raw_buffer_store_b64(e, rws, lane == 0 ? 0u : OOB, 0, AUX_SC1); }
    STAMP(8);
  }
  FLUSH();
}

extern "C" void kernel_launch(void* const* d_in, const int* in_sizes, int n_in, void* d_out, int out_size, void* d_ws,
                              size_t ws_size, hipStream_t stream) {
  fused_block<<<256, 768, 0, stream>>>((const u16*)d_in[0], (const u16*)d_in[1], (const u16*)d_in[2], (const u16*)d_in[3],
                                       (const u16*)d_in[4], (const u16*)d_in[6], (const u16*)d_in[7], (const u16*)d_in[8],
                                       (const u16*)d_in[9], (const u16*)d_in[10], (u16*)d_out, (char*)d_ws);
}
```

```cpp
#define PROBE_K 0
#define PROBE_W0 0
#define PROBE_W1 1
#define PROBE_PRED (b == 0)
#include <hip/hip_runtime.h>
#include <hip/hip_bf16.h>
#include <stdint.h>

typedef unsigned short u16;
typedef unsigned long long u64;
typedef unsigned v4u __attribute__((ext_vector_type(4)));
typedef unsigned v3u __attribute__((ext_vector_type(3)));
typedef unsigned v2u __attribute__((ext_vector_type(2)));
typedef float v2f __attribute__((ext_vector_type(2)));
typedef float v4f __attribute__((ext_vector_type(4)));
typedef __bf16 bf2 __attribute__((ext_vector_type(2)));
typedef __amdgpu_buffer_rsrc_t rsrc_t;

#define TF 4
#define TS 5
#define RAMP 0
#define STAMPS 0
#define DYN 1
#define FTRIG 8
#define WTRIG 0
#define FEARLY 9
#define STG 8

constexpr int AUX_NT = 2, AUX_SC1 = 16;
constexpr unsigned OOB = 0x40000000u;
constexpr unsigned OFF_T = 4096;
constexpr unsigned OFF_M = 16384;
constexpr unsigned M_COPY = 236 * 128;
constexpr unsigned OFF_AO = 262144;
constexpr unsigned OFF_AM = OFF_AO + 131072;
constexpr unsigned OFF_B = OFF_AM + 4096;
constexpr unsigned OFF_C = OFF_B + 4 * 18432;
constexpr unsigned WS_END = OFF_C + 331776;
static_assert(OFF_M + 8 * M_COPY <= OFF_AO && WS_END - OFF_AO <= 36 * 16384, "workspace map");
constexpr unsigned OFF_STAMP = 1u << 20;
constexpr int GCAP = 1 << 15;
constexpr int LCAP = 1 << 20;
constexpr float LOG2E = 1.4426950408889634f;

__device__ __forceinline__ float asf(unsigned u) { return __builtin_bit_cast(float, u); }
__device__ __forceinline__ unsigned asu(float f) { return __builtin_bit_cast(unsigned, f); }
__device__ __forceinline__ float lo16(unsigned u) { return asf(u << 16); }
__device__ __forceinline__ float hi16(unsigned u) { return asf(u & 0xffff0000u); }
__device__ __forceinline__ float rbf(float f) { __bf16 b = (__bf16)f; return (float)b; }
__device__ __forceinline__ unsigned pack2(float a, float b) { bf2 v = {(__bf16)a, (__bf16)b}; return __builtin_bit_cast(unsigned, v); }
__device__ __forceinline__ float dot2(unsigned a, unsigned b, float c) {
  return __builtin_amdgcn_fdot2_f32_bf16(__builtin_bit_cast(bf2, a), __builtin_bit_cast(bf2, b), c, false);
}
template <int CTRL> __device__ __forceinline__ unsigned dppu(unsigned v) { return __builtin_amdgcn_update_dpp(0u, v, CTRL, 0xf, 0xf, true); }
template <int CTRL> __device__ __forceinline__ float dppf(float v) { return asf(dppu<CTRL>(asu(v))); }
constexpr int X1 = 0xB1, X2 = 0x4E, HM = 0x141, R4 = 0x124, R8 = 0x128;
__device__ __forceinline__ float rs32(float a, float b) {
  auto r = __builtin_amdgcn_permlane32_swap(asu(a), asu(b), false, false);
  unsigned r0 = r[0], r1 = r[1]; return asf(r0) + asf(r1);
}
__device__ __forceinline__ float rs16(float a, float b) {
  auto r = __builtin_amdgcn_permlane16_swap(asu(a), asu(b), false, false);
  unsigned r0 = r[0], r1 = r[1]; return asf(r0) + asf(r1);
}
__device__ __forceinline__ float mx32(float a) {
  auto r = __builtin_amdgcn_permlane32_swap(asu(a), asu(a), false, false);
  unsigned r0 = r[0], r1 = r[1]; return fmaxf(asf(r0), asf(r1));
}
__device__ __forceinline__ float mx16(float a) {
  auto r = __builtin_amdgcn_permlane16_swap(asu(a), asu(a), false, false);
  unsigned r0 = r[0], r1 = r[1]; return fmaxf(asf(r0), asf(r1));
}
__device__ __forceinline__ float wave_sum(float v) {
  v += dppf<X1>(v); v += dppf<X2>(v); v += dppf<R4>(v); v += dppf<R8>(v);
  v = rs16(v, v); v = rs32(v, v); return v;
}
__device__ __forceinline__ float ex2(float x) { return __builtin_amdgcn_exp2f(x); }
__device__ __forceinline__ float rcp(float x) { return __builtin_amdgcn_rcpf(x); }
__device__ __forceinline__ float silu(float g) { return g * rcp(1.f + ex2(-g * LOG2E)); }
__device__ __forceinline__ unsigned canon(float f) { unsigned u = asu(f); return u < 0xfffffffeu ? u : 0xfffffffeu; }
__device__ __forceinline__ unsigned umax(unsigned a, unsigned b) { return a > b ? a : b; }
__device__ __forceinline__ bool all_true(bool ok) { return __builtin_amdgcn_ballot_w64(!ok) == 0; }
__device__ __forceinline__ rsrc_t mk(const void* p, unsigned n) { return __builtin_amdgcn_make_buffer_rsrc((void*)p, 0, n, 0x00020000); }
#define LD128(r, v, aux) __builtin_amdgcn_raw_buffer_load_b128(r, v, 0, aux)
#define LD96(r, v, aux) __builtin_amdgcn_raw_buffer_load_b96(r, v, 0, aux)
#define LD64(r, v, aux) __builtin_amdgcn_raw_buffer_load_b64(r, v, 0, aux)
#define LD32(r, v, aux) __builtin_amdgcn_raw_buffer_load_b32(r, v, 0, aux)
#define LD16(r, v, aux) __builtin_amdgcn_raw_buffer_load_b16(r, v, 0, aux)
#define SB() __builtin_amdgcn_sched_barrier(0)
#define MEMFENCE() asm volatile("" ::: "memory")
#define WAIT_LGKM0() asm volatile("s_waitcnt lgkmcnt(0)" ::: "memory")
#define WAIT_VM0() asm volatile("s_waitcnt vmcnt(0)" ::: "memory")
#define PIN(x) asm volatile("" : "+v"(x))
__device__ __forceinline__ void wg_barrier() { WAIT_LGKM0(); __builtin_amdgcn_s_barrier(); MEMFENCE(); }

struct __attribute__((aligned(16))) Lds {
  float rec_o[8][128];
  float rec_ml[8][2];
  float dn[4][576];
  float hp[4][64];
  float tp[8][64];
  unsigned ob[4][64];
  unsigned qs[64];
  float st[4][2];
  unsigned t0, pad_[3];
  unsigned cnt[8];
};

__device__ __forceinline__ void lds_wait(const unsigned* w, unsigned target) {
  _Pragma("unroll 1") for (int it = 0; it < LCAP; ++it) {
    unsigned v = __hip_atomic_load(w, __ATOMIC_RELAXED, __HIP_MEMORY_SCOPE_WORKGROUP);
    if (__builtin_amdgcn_readfirstlane(v) >= target) break;
    __builtin_amdgcn_s_sleep(1);
  }
  MEMFENCE();
}

struct Slab { unsigned p[4][8]; unsigned z[4]; };
__device__ __forceinline__ void interleave(unsigned (&o)[8], v4u a, v4u b) {
#pragma unroll
  for (int d = 0; d < 4; ++d) {
    unsigned x = a[d], y = b[d];
    o[2 * d] = __builtin_amdgcn_perm(y, x, 0x05040100u);
    o[2 * d + 1] = __builtin_amdgcn_perm(y, x, 0x07060302u);
  }
}
__device__ __forceinline__ void slab_make(Slab& s, const v4u (&w)[9]) {
  interleave(s.p[0], w[0], w[1]); interleave(s.p[1], w[3], w[4]); interleave(s.p[2], w[6], w[7]); interleave(s.p[3], w[2], w[5]);
#pragma unroll
  for (int d = 0; d < 4; ++d) s.z[d] = w[8][d];
#pragma unroll
  for (int p = 0; p < 4; ++p) {
#pragma unroll
    for (int d = 0; d < 8; ++d) PIN(s.p[p][d]);
    PIN(s.z[p]);
  }
}
struct Hv { unsigned p[4], za, zb; };
__device__ __forceinline__ void hv_make(Hv& h, float a, float b, float c) {
  unsigned ab = pack2(a, b);
  h.p[0] = dppu<0x00>(ab); h.p[1] = dppu<0x55>(ab); h.p[2] = dppu<0xAA>(ab);
  unsigned c0 = dppu<0x00>(asu(c)), c1 = dppu<0x55>(asu(c)), c2 = dppu<0xAA>(asu(c));
  h.p[3] = __builtin_amdgcn_perm(c1, c0, 0x07060302u);
  h.zb = c2 & 0xffff0000u; h.za = c2 >> 16;
}
__device__ __forceinline__ void slab_dot(float (&acc)[8], const Slab& s, const Hv& h) {
#pragma unroll
  for (int d = 0; d < 4; ++d) { acc[2 * d] = dot2(s.z[d], h.za, 0.f); acc[2 * d + 1] = dot2(s.z[d], h.zb, 0.f); }
#pragma unroll
  for (int p = 0; p < 4; ++p)
#pragma unroll
    for (int d = 0; d < 8; ++d) acc[d] = dot2(s.p[p][d], h.p[p], acc[d]);
}
__device__ __forceinline__ void ln_stats(float s, float q, float& m, float& rsd) {
  float mean = s * (1.f / 576.f);
  float var = fmaxf(q * (1.f / 576.f) - mean * mean, 0.f);
  m = rbf(mean);
  float sd = rbf(__builtin_sqrtf(rbf(rbf(var) + 1e-5f)));
  rsd = 1.f / sd;
}
__device__ __forceinline__ float modulate(float t, float scale, float shift) { return rbf(rbf(t * rbf(1.f + scale)) + shift); }

#define STAMP(k) do { MEMFENCE(); ts[k] = __builtin_amdgcn_s_memrealtime(); MEMFENCE(); } while (0)
#define FLUSH() do { unsigned ev_ = (wave >= PROBE_W0 && wave < PROBE_W1 && (PROBE_PRED) && ts[PROBE_K] != 0) ? (unsigned)(ts[PROBE_K] - ts[0]) + 1000u + ((unsigned)ts[0] - L.t0) : 0u; \
    if (lane == 0) __hip_atomic_fetch_max(&L.cnt[7], ev_, __ATOMIC_RELAXED, __HIP_MEMORY_SCOPE_WORKGROUP); \
    if (wave == 0) { WAIT_LGKM0(); unsigned e2_ = __builtin_amdgcn_readfirstlane(__hip_atomic_load(&L.cnt[7], __ATOMIC_RELAXED, __HIP_MEMORY_SCOPE_WORKGROUP)); \
    if (e2_) { const u64 tend = ts[0] + e2_ + 4000; \
    for (int i_ = 0; i_ < 200000; ++i_) { if (__builtin_amdgcn_s_memrealtime() >= tend) break; __builtin_amdgcn_s_sleep(4); } } } } while (0)

__global__ void __launch_bounds__(768)
fused_block(const u16* __restrict__ xin, const u16* __restrict__ Kc, const u16* __restrict__ Vc, const u16* __restrict__ wq,
            const u16* __restrict__ wo, const u16* __restrict__ wg, const u16* __restrict__ wu, const u16* __restrict__ wd,
            const u16* __restrict__ cin, const u16* __restrict__ wmod, u16* __restrict__ out, char* ws) {
  __shared__ Lds L;
  const int tid = threadIdx.x, lane = tid & 63, wave = __builtin_amdgcn_readfirstlane(tid >> 6);
  const int b = blockIdx.x, x = b & 7, head = b >> 3, r = (head + 4 * x) & 31;
  u64 ts[12] = {0, 0, 0, 0, 0, 0, 0, 0, 0, 0, 0, 0};
  STAMP(0);
  asm volatile("" :: "s"(xin), "s"(Kc), "s"(Vc), "s"(wq), "s"(wo), "s"(wg), "s"(wu), "s"(wd), "s"(cin), "s"(wmod), "s"(out), "s"(ws));
  STAMP(9);
  const int k = r >> 2, grp = wave >> 2, wv = wave & 3;
  const rsrc_t rws = mk(ws, WS_END);
  const int qr = lane >> 2, qc = lane & 3;
  const int row0 = 144 * wv + 9 * qr;
  const int rowl = row0 + 3 * qc;
  const bool act3 = qc < 3;
  const unsigned mcopy = OFF_M + x * M_COPY;
  const unsigned vm = act3 ? mcopy + (rowl >> 1) * 8 : OOB, msh = (rowl & 1) * 16;
  int task = 999;
  if (k >= 6) { if (grp < 2) task = 108 + 4 * head + 2 * (k - 6); }
  else if (grp == 0) task = k == 2 ? 36 + head : k == 3 ? 68 + head : k == 5 ? head : (k == 4 && head < 12) ? (head < 4 ? 32 : 96) + head : 999;
  const int parked = k >= 6 ? 2 : (k == 5 || (k == 4 && head < 4)) ? 1 : 0;
  unsigned tag = 0;
  if (tid < 8) L.cnt[tid] = 0;
  if (tid == 0) L.t0 = (unsigned)ts[0];

  if (task < 236) {
    __builtin_amdgcn_s_setprio(3);
    const bool is_mod = task < 108;
    const unsigned ld = is_mod ? 3456 * 2 : 4096 * 2;
    const rsrc_t rw = mk(is_mod ? wmod + task * 32 : wq + (task - 108) * 32, 575 * ld + 128);
    const rsrc_t rin = mk(is_mod ? cin : xin, 1152);
    const int t0 = is_mod ? row0 : 72 * wave + 9 * (lane >> 3), tl = t0 + 3 * qc;
    const unsigned vi = act3 ? tl * 2 : OOB;
    const unsigned ep = LD32(rws, 0, 0);
    const unsigned i0 = LD16(rin, vi, 0), i1 = LD16(rin, vi + 2, 0), i2 = LD16(rin, vi + 4, 0);
    const v4u xa = LD128(rin, lane * 16, 0), xb = LD128(rin, lane * 16 + 1024, 0);
    SB();
    v4u w[9];
    const unsigned vw = t0 * ld + (lane & (is_mod ? 3 : 7)) * 16;
#pragma unroll
    for (int t = 0; t < 9; ++t) w[t] = __builtin_amdgcn_raw_buffer_load_b128(rw, vw, t * ld, AUX_NT);
    if (task < 36) {
      const v4u ones = {~0u, ~0u, ~0u, ~0u};
#pragma unroll
      for (int t = 0; t < 4; ++t)
        __builtin_amdgcn_raw_buffer_store_b128(ones, rws, OFF_AO + ((task * 4 + wv) * 4 + t) * 1024 + lane * 16, 0, AUX_SC1);
      __builtin_amdgcn_raw_buffer_store_b32(0u, rws, (lane == 0 && wv == 0 && task < 32) ? OFF_T + task * 256 : OOB, 0, AUX_SC1);
    }
    SB();
    wg_barrier();
    float h0 = lo16(i0), h1 = lo16(i1), h2 = lo16(i2);
    tag = ep + 1;
    Slab S;
    if (is_mod) {
      h0 = rbf(silu(h0)); h1 = rbf(silu(h1)); h2 = rbf(silu(h2));
      slab_make(S, w);
      STAMP(1);
    } else {
      float s = 0.f, q = 0.f;
#pragma unroll
      for (int d = 0; d < 4; ++d) {
        float e0 = lo16(xa[d]), e1 = hi16(xa[d]), e2 = lo16(xb[d]), e3 = hi16(xb[d]);
        s += (e0 + e1) + (e2 + e3); q += (e0 * e0 + e1 * e1) + (e2 * e2 + e3 * e3);
      }
      s = wave_sum(s); q = wave_sum(q);
      float m, rsd; ln_stats(s, q, m, rsd);
      h0 = rbf(rbf(h0 - m) * rsd); h1 = rbf(rbf(h1 - m) * rsd); h2 = rbf(rbf(h2 - m) * rsd);
      PIN(h0); PIN(h1); PIN(h2);
      slab_make(S, w);
      STAMP(1);
      const unsigned vu = act3 ? mcopy + (tl >> 1) * 8 : OOB, ush = (tl & 1) * 16;
      v4u g0, g1;
      _Pragma("unroll 1") for (int it = 0; it < GCAP; ++it) {
        g0 = LD128(rws, vu, AUX_SC1); g1 = LD128(rws, vu + 2304, AUX_SC1);
        const unsigned bad = ((g0[1] ^ tag) | (g0[3] ^ tag)) | ((g1[1] ^ tag) | (g1[3] ^ tag));
        if (all_true(bad == 0 || !act3)) break;
        MEMFENCE();
      }
      const unsigned sa = __builtin_amdgcn_alignbit(g0[2], g0[0], ush), sc = g0[2] >> ush;
      const unsigned ca = __builtin_amdgcn_alignbit(g1[2], g1[0], ush), cc = g1[2] >> ush;
      h0 = modulate(h0, lo16(ca), lo16(sa)); h1 = modulate(h1, hi16(ca), hi16(sa)); h2 = modulate(h2, lo16(cc), lo16(sc));
    }
    STAMP(2);
    Hv H; hv_make(H, h0, h1, h2);
    float acc[8]; slab_dot(acc, S, H);
    float y[4];
#pragma unroll
    for (int j = 0; j < 4; ++j) y[j] = rs32(acc[j], acc[j + 4]);
    float z0 = rs16(y[0], y[2]), z1 = rs16(y[1], y[3]);
    z0 += dppf<R8>(z0); z1 += dppf<R8>(z1);
    if (is_mod) { z0 += dppf<R4>(z0); z1 += dppf<R4>(z1); }
    if ((lane & (is_mod ? 12 : 8)) == 0) { v2f z = {z0, z1}; *(v2f*)&L.tp[wave][(lane & 7) * 8 + (lane >> 4) * 2] = z; }
    if (task < 36) WAIT_VM0();
    WAIT_LGKM0();
    if (lane == 0) __hip_atomic_fetch_add(&L.cnt[0], 1u, __ATOMIC_RELAXED, __HIP_MEMORY_SCOPE_WORKGROUP);
    if (wv == 0) {
      lds_wait(&L.cnt[0], is_mod ? 4 : 8);
      const int c4 = grp * 32 + (lane & 7) * 4;
      v4f t = (*(const v4f*)&L.tp[0][c4] + *(const v4f*)&L.tp[1][c4]) + (*(const v4f*)&L.tp[2][c4] + *(const v4f*)&L.tp[3][c4]);
      if (!is_mod) t += (*(const v4f*)&L.tp[4][c4] + *(const v4f*)&L.tp[5][c4]) + (*(const v4f*)&L.tp[6][c4] + *(const v4f*)&L.tp[7][c4]);
      const v4u g = {pack2(t.x, t.y), tag, pack2(t.z, t.w), tag};
      __builtin_amdgcn_raw_buffer_store_b128(g, rws, OFF_M + (lane >> 3) * M_COPY + task * 128 + c4 * 4, 0, AUX_SC1);
      if (lane == 0) __hip_atomic_fetch_add(&L.cnt[6], 1u, __ATOMIC_RELAXED, __HIP_MEMORY_SCOPE_WORKGROUP);
      STAMP(3);
    }
  } else if (grp == 0) {
    __builtin_amdgcn_s_setprio(3);
    const unsigned ep = LD32(rws, 0, 0);
    wg_barrier();
    tag = ep + 1;
  } else {
    __builtin_amdgcn_s_barrier();
  }

  if (grp != 0) {
    __builtin_amdgcn_s_setprio(0);
    const int s = wave - 4;
    const u64 ka = (u64)(Kc + ((size_t)head << 20)), va = (u64)(Vc + ((size_t)head << 20)), wa = (u64)ws;
    const v4u rk = {(unsigned)ka, (unsigned)(ka >> 32) & 0xffffu, 1u << 21, 0x00020000u};
    const v4u rv = {(unsigned)va, (unsigned)(va >> 32) & 0xffffu, 1u << 21, 0x00020000u};
    const v4u rt = {(unsigned)wa, (unsigned)(wa >> 32) & 0xffffu, OFF_M, 0x00020000u};
    const unsigned vo = lane * 16, vt = lane == 0 ? OFF_T + head * 256 : OOB;
    const unsigned rot = (head >> 2) * 64, p0 = rot + k * 8 + s;
    if (parked) lds_wait(&L.cnt[6], parked);
    v4u Kr[3][4], Vr[3][4];
#define S_(x) #x
#define S(x) S_(x)
#define LD1(n, off, T) "s_waitcnt vmcnt(" S(T) "-1)\n\tbuffer_load_dwordx4 %" #n ", %[vo], %[rs], %[so] offen offset:" #off " nt\n\t"
#define LD4(T) LD1(0, 0, T) LD1(1, 1024, T) LD1(2, 2048, T) LD1(3, 3072, T)
#pragma unroll
    for (int i = 0; i < 3; ++i) {
      const unsigned so = ((p0 + 64 * i) & 511) << 12;
      if (RAMP && i == 0)
        asm volatile(LD1(0, 0, TF) LD1(1, 1024, 1) LD1(2, 2048, TF) LD1(3, 3072, TF)
                     : "=&v"(Kr[i][0]), "=&v"(Kr[i][1]), "=&v"(Kr[i][2]), "=&v"(Kr[i][3]) : [vo] "v"(vo), [rs] "s"(rk), [so] "s"(so));
      else
        asm volatile(LD4(TF) : "=&v"(Kr[i][0]), "=&v"(Kr[i][1]), "=&v"(Kr[i][2]), "=&v"(Kr[i][3]) : [vo] "v"(vo), [rs] "s"(rk), [so] "s"(so));
      asm volatile(LD4(TF) : "=&v"(Vr[i][0]), "=&v"(Vr[i][1]), "=&v"(Vr[i][2]), "=&v"(Vr[i][3]) : [vo] "v"(vo), [rs] "s"(rv), [so] "s"(so));
    }
    STAMP(6);
    lds_wait(&L.cnt[4], 1);
    const v4u qv = *(const v4u*)&L.qs[(lane & 15) * 4];
    const unsigned q0 = qv[0], q1 = qv[1], q2 = qv[2], q3 = qv[3];
    STAMP(7);
    float m = -INFINITY, l = 0.f;
    v2f acc[4] = {{0.f, 0.f}, {0.f, 0.f}, {0.f, 0.f}, {0.f, 0.f}};
    unsigned tk = k * 8 + s - 64;
    int rem = 3;
#define TICKET "v_mov_b32 %[tk], 1\n\tbuffer_atomic_add %[tk], %[vt], %[rt], 0 offen sc0 sc1\n\t"
#define STEP(i)                                                                                     \
      {                                                                                             \
        float sc[4];                                                                                \
        asm volatile("" : "+v"(Kr[i][0]), "+v"(Kr[i][1]), "+v"(Kr[i][2]), "+v"(Kr[i][3]));          \
        _Pragma("unroll") for (int t = 0; t < 4; ++t) {                                             \
          const v4u kk = Kr[i][t];                                                                  \
          sc[t] = dot2(kk[3], q3, dot2(kk[2], q2, dot2(kk[1], q1, dot2(kk[0], q0, 0.f))));          \
        }                                                                                           \
        asm volatile("s_waitcnt vmcnt(4)" : "+v"(tk), "+v"(sc[0]), "+v"(sc[1]), "+v"(sc[2]), "+v"(sc[3])); \
        const int tn = __builtin_amdgcn_readfirstlane(tk);                                          \
        rem = tn < 256 ? 3 : rem - 1;                                                               \
        const unsigned so = ((tn + 256 + rot) & 511) << 12;                                         \
        asm volatile("s_cmp_eq_u32 %[rem], 3\n\ts_cbranch_scc0 1f\n\ts_nop 1\n\t" LD4(TS) "1:"      \
                     : "+v"(Kr[i][0]), "+v"(Kr[i][1]), "+v"(Kr[i][2]), "+v"(Kr[i][3])               \
                     : [vo] "v"(vo), [rs] "s"(rk), [so] "s"(so), [rem] "s"(rem) : "scc");           \
        if (rem == 2) {                                                                             \
          if (lane == 0) __hip_atomic_fetch_add(&L.cnt[5], 1u, __ATOMIC_RELAXED, __HIP_MEMORY_SCOPE_WORKGROUP); \
          __builtin_amdgcn_s_setprio(3);                                                            \
          STAMP(8);                                                                                 \
        }                                                                                           \
        SB();                                                                                       \
        _Pragma("unroll") for (int t = 0; t < 4; ++t) sc[t] += dppf<X1>(sc[t]);                     \
        SB();                                                                                       \
        _Pragma("unroll") for (int t = 0; t < 4; ++t) sc[t] += dppf<X2>(sc[t]);                     \
        SB();                                                                                       \
        _Pragma("unroll") for (int t = 0; t < 4; ++t) sc[t] += dppf<R4>(sc[t]);                     \
        SB();                                                                                       \
        _Pragma("unroll") for (int t = 0; t < 4; ++t) sc[t] += dppf<R8>(sc[t]);                     \
        SB();                                                                                       \
        _Pragma("unroll") for (int t = 0; t < 4; ++t) sc[t] = rbf(rbf(sc[t]) * 0.08838834764831845f); \
        const float mn = fmaxf(fmaxf(fmaxf(sc[0], sc[1]), fmaxf(sc[2], sc[3])), m);                 \
        const float al = ex2((m - mn) * LOG2E);                                                     \
        m = mn;                                                                                     \
        float p[4];                                                                                 \
        _Pragma("unroll") for (int t = 0; t < 4; ++t) p[t] = ex2((sc[t] - mn) * LOG2E);             \
        l = l * al + ((p[0] + p[1]) + (p[2] + p[3]));                                               \
        _Pragma("unroll") for (int j = 0; j < 4; ++j) acc[j] *= al;                                 \
        asm volatile("s_cmp_eq_u32 %[rem], 0\n\ts_cbranch_scc0 1f\n\ts_waitcnt vmcnt(0)\n1:"        \
                     : "+v"(Vr[i][0]), "+v"(Vr[i][1]), "+v"(Vr[i][2]), "+v"(Vr[i][3]), "+v"(l),     \
                       "+v"(acc[0]), "+v"(acc[1]), "+v"(acc[2]), "+v"(acc[3]) : [rem] "s"(rem) : "scc"); \
        _Pragma("unroll") for (int t = 0; t < 4; ++t) {                                             \
          const v4u vv = Vr[i][t];                                                                  \
          const v2f pp = {p[t], p[t]};                                                              \
          _Pragma("unroll") for (int j = 0; j < 4; ++j) {                                           \
            const v2f e = {lo16(vv[j]), hi16(vv[j])};                                               \
            acc[j] = __builtin_elementwise_fma(pp, e, acc[j]);                                      \
          }                                                                                         \
        }                                                                                           \
        asm volatile("s_cmp_eq_u32 %[rem], 3\n\ts_cbranch_scc0 1f\n\t" TICKET LD4(TS) "1:"          \
                     : "+v"(Vr[i][0]), "+v"(Vr[i][1]), "+v"(Vr[i][2]), "+v"(Vr[i][3]), [tk] "+v"(tk), \
                       "+v"(acc[0]), "+v"(acc[1]), "+v"(acc[2]), "+v"(acc[3])                       \
                     : [vo] "v"(vo), [rs] "s"(rv), [so] "s"(so), [rem] "s"(rem), [vt] "v"(vt), [rt] "s"(rt) : "scc", "memory"); \
        if (rem == 0) break;                                                                        \
      }
    _Pragma("unroll 1") for (int it = 0; it < 200; ++it) { STEP(0) STEP(1) STEP(2) }
#undef STEP
    STAMP(4);
    const float M = mx32(mx16(m));
    const float w = ex2((m - M) * LOG2E);
    l *= w; l = rs16(l, l); l = rs32(l, l);
    float a[8];
#pragma unroll
    for (int j = 0; j < 4; ++j) { a[2 * j] = acc[j].x * w; a[2 * j + 1] = acc[j].y * w; }
    float y[4];
#pragma unroll
    for (int j = 0; j < 4; ++j) y[j] = rs32(a[j], a[j + 4]);
    v2f z = {rs16(y[0], y[2]), rs16(y[1], y[3])};
    *(v2f*)&L.rec_o[s][(lane & 15) * 8 + (lane >> 4) * 2] = z;
    if (lane == 0) { v2f ml = {M, l}; *(v2f*)&L.rec_ml[s][0] = ml; }
    WAIT_LGKM0();
    unsigned old = 0;
    if (lane == 0) old = __hip_atomic_fetch_add(&L.cnt[3], 1u, __ATOMIC_RELAXED, __HIP_MEMORY_SCOPE_WORKGROUP);
    old = __builtin_amdgcn_readfirstlane(old);
    if (old != 7) { FLUSH(); return; }
    MEMFENCE();
    {
      float ms[8], ls[8];
#pragma unroll
      for (int j = 0; j < 4; ++j) { v4f t = *(const v4f*)&L.rec_ml[2 * j][0]; ms[2 * j] = t.x; ls[2 * j] = t.y; ms[2 * j + 1] = t.z; ls[2 * j + 1] = t.w; }
      v4f o[8];
#pragma unroll
      for (int j = 0; j < 8; ++j) o[j] = *(const v4f*)&L.rec_o[j][(lane & 31) * 4];
      const float MM = fmaxf(fmaxf(fmaxf(ms[0], ms[1]), fmaxf(ms[2], ms[3])), fmaxf(fmaxf(ms[4], ms[5]), fmaxf(ms[6], ms[7])));
      float LL = 0.f; v4f oo = {0.f, 0.f, 0.f, 0.f};
#pragma unroll
      for (int j = 0; j < 8; ++j) { const float e = ex2((ms[j] - MM) * LOG2E); LL += e * ls[j]; oo += o[j] * e; }
      if (lane >= 32) { oo.x = MM; oo.y = LL; oo.z = 0.f; oo.w = 0.f; }
      v4u pv = {canon(oo.x), canon(oo.y), canon(oo.z), canon(oo.w)};
      const unsigned off = lane < 32 ? OFF_AO + b * 512 + lane * 16 : (lane == 32 ? OFF_AM + b * 16 : OOB);
      __builtin_amdgcn_raw_buffer_store_b128(pv, rws, off, 0, AUX_SC1);
      if (lane == 0) __hip_atomic_fetch_add(&L.cnt[3], 1u, __ATOMIC_RELAXED, __HIP_MEMORY_SCOPE_WORKGROUP);
      STAMP(5);
    }
    FLUSH();
    return;
  }

  {
    if (wave & 1) __builtin_amdgcn_s_sleep(STG);
    if (wave & 2) { __builtin_amdgcn_s_sleep(STG); __builtin_amdgcn_s_sleep(STG); }
    v2u g;
    _Pragma("unroll 1") for (int it = 0; it < GCAP; ++it) {
      g = LD64(rws, mcopy + (108 + 4 * head) * 128 + lane * 8, AUX_SC1);
      if (all_true(g[1] == tag)) break;
      MEMFENCE();
    }
    L.qs[lane] = g[0];
    WAIT_LGKM0();
    if (lane == 0) __hip_atomic_store(&L.cnt[4], 1u, __ATOMIC_RELAXED, __HIP_MEMORY_SCOPE_WORKGROUP);
    STAMP(4);
  }

  if (r < 9) {
    const int r8 = lane >> 3, c8 = lane & 7;
    const rsrc_t rw = mk(wo + (size_t)(512 * x + 128 * wave) * 576 + 64 * r, 127 * 1152 + 128);
    v4u w[16];
    if (WTRIG) lds_wait(&L.cnt[5], WTRIG);
#pragma unroll
    for (int t = 0; t < 16; ++t) w[t] = LD128(rw, (16 * r8 + t) * 1152 + c8 * 16, AUX_NT);
    unsigned P[8][8];
#pragma unroll
    for (int p = 0; p < 8; ++p) {
      interleave(P[p], w[2 * p], w[2 * p + 1]);
#pragma unroll
      for (int d = 0; d < 8; ++d) PIN(P[p][d]);
    }
    lds_wait(&L.cnt[3], 9);
    STAMP(5);
    const int rec = (4 * x + wave) * 8;
    v2u o[8], ml;
    _Pragma("unroll 1") for (int it = 0; it < GCAP; ++it) {
      ml = LD64(rws, OFF_AM + (rec + c8) * 16, AUX_SC1);
#pragma unroll
      for (int j = 0; j < 8; ++j) o[j] = LD64(rws, OFF_AO + (rec + j) * 512 + lane * 8, AUX_SC1);
      unsigned mxu = umax(ml[0], ml[1]);
#pragma unroll
      for (int j = 0; j < 8; ++j) mxu = umax(mxu, umax(o[j][0], o[j][1]));
      if (all_true(mxu != ~0u)) break;
      MEMFENCE();
    }
    STAMP(6);
    const float mi = asf(ml[0]);
    float M = fmaxf(mi, dppf<X1>(mi)); M = fmaxf(M, dppf<X2>(M)); M = fmaxf(M, dppf<HM>(M));
    const float e = ex2((mi - M) * LOG2E);
    float Ls = e * asf(ml[1]); Ls += dppf<X1>(Ls); Ls += dppf<X2>(Ls); Ls += dppf<HM>(Ls);
    float o0 = 0.f, o1 = 0.f;
#pragma unroll
    for (int j = 0; j < 8; ++j) {
      const float ej = asf(__builtin_amdgcn_readlane(asu(e), j));
      o0 = fmaf(ej, asf(o[j][0]), o0); o1 = fmaf(ej, asf(o[j][1]), o1);
    }
    const float rl = rcp(Ls);
    L.ob[wave][lane] = pack2(o0 * rl, o1 * rl);
    WAIT_LGKM0();
    const v4u oa = *(const v4u*)&L.ob[wave][r8 * 8], ob = *(const v4u*)&L.ob[wave][r8 * 8 + 4];
    float acc[8] = {0.f, 0.f, 0.f, 0.f, 0.f, 0.f, 0.f, 0.f};
#pragma unroll
    for (int p = 0; p < 8; ++p) {
      const unsigned hp = p < 4 ? oa[p & 3] : ob[p & 3];
#pragma unroll
      for (int d = 0; d < 8; ++d) acc[d] = dot2(P[p][d], hp, acc[d]);
    }
    float y[4];
#pragma unroll
    for (int j = 0; j < 4; ++j) y[j] = rs32(acc[j], acc[j + 4]);
    float z0 = rs16(y[0], y[2]), z1 = rs16(y[1], y[3]);
    z0 += dppf<R8>(z0); z1 += dppf<R8>(z1);
    if ((lane & 8) == 0) { v2f z = {z0, z1}; *(v2f*)&L.hp[wave][c8 * 8 + (lane >> 4) * 2] = z; }
    wg_barrier();
    if (wave == 0) {
      const int c4 = (lane & 15) * 4;
      const v4f t = (*(const v4f*)&L.hp[0][c4] + *(const v4f*)&L.hp[1][c4]) + (*(const v4f*)&L.hp[2][c4] + *(const v4f*)&L.hp[3][c4]);
      v4u pv = {canon(t.x), canon(t.y), canon(t.z), canon(t.w)};
      __builtin_amdgcn_raw_buffer_store_b128(pv, rws, OFF_B + (lane >> 4) * 18432 + x * 2304 + r * 256 + c4 * 4, 0, AUX_SC1);
      STAMP(7);
    }
  } else if (r < 27) {
    const int f = 18 * x + (r - 9);
    const rsrc_t rx = mk(xin, 1152);
    const unsigned vi = act3 ? rowl * 2 : OOB;
    const float x0 = lo16(LD16(rx, vi, 0)), x1 = lo16(LD16(rx, vi + 2, 0)), x2 = lo16(LD16(rx, vi + 4, 0));
    float ga[3], sb[3], cb[3];
    {
      v4u g[3];
      _Pragma("unroll 1") for (int it = 0; it < GCAP; ++it) {
        unsigned bad = 0;
#pragma unroll
        for (int j = 0; j < 3; ++j) {
          if (it == 0) g[j] = LD128(rws, vm + (2 + j) * 2304, 0);
          else g[j] = LD128(rws, vm + (2 + j) * 2304, AUX_SC1);
        }
#pragma unroll
        for (int j = 0; j < 3; ++j) bad |= (g[j][1] ^ tag) | (g[j][3] ^ tag);
        if (all_true(bad == 0 || !act3)) break;
        MEMFENCE();
      }
      unsigned u01[3], u2[3];
#pragma unroll
      for (int j = 0; j < 3; ++j) { u01[j] = __builtin_amdgcn_alignbit(g[j][2], g[j][0], msh); u2[j] = g[j][2] >> msh; }
      ga[0] = lo16(u01[0]); ga[1] = hi16(u01[0]); ga[2] = lo16(u2[0]);
      sb[0] = lo16(u01[1]); sb[1] = hi16(u01[1]); sb[2] = lo16(u2[1]);
      cb[0] = rbf(1.f + lo16(u01[2])); cb[1] = rbf(1.f + hi16(u01[2])); cb[2] = rbf(1.f + lo16(u2[2]));
#pragma unroll
      for (int j = 0; j < 3; ++j) { PIN(ga[j]); PIN(sb[j]); PIN(cb[j]); }
    }
    __builtin_amdgcn_s_setprio(0);
    const rsrc_t rg = mk(wg + f * 32, 575 * 9216 + 64), ru = mk(wu + f * 32, 575 * 9216 + 64);
    const rsrc_t rd = mk(wd + (size_t)(32 * f + 8 * wave) * 576, 8 * 1152);
    v4u w1[9], w2[9], w3[8]; unsigned w4[8];
#pragma unroll
    for (int t = 0; t < 18; ++t) {
      if (t == FEARLY) { SB(); lds_wait(&L.cnt[5], FTRIG); STAMP(5); SB(); }
      if (t < 9) w1[t] = LD128(rg, (row0 + t) * 9216 + qc * 16, AUX_NT);
      else w2[t - 9] = LD128(ru, (row0 + t - 9) * 9216 + qc * 16, AUX_NT);
    }
    if (FEARLY >= 18) { SB(); lds_wait(&L.cnt[5], FTRIG); STAMP(5); SB(); }
#pragma unroll
    for (int t = 0; t < 8; ++t) { w3[t] = LD128(rd, t * 1152 + lane * 16, AUX_NT); w4[t] = LD16(rd, t * 1152 + 1024 + lane * 2, AUX_NT); }
    SB();
    Slab G, U; slab_make(G, w1); slab_make(U, w2);
    unsigned Dp[4][8], Dt[4];
#pragma unroll
    for (int p = 0; p < 4; ++p) {
      interleave(Dp[p], w3[2 * p], w3[2 * p + 1]); Dt[p] = w4[2 * p] | (w4[2 * p + 1] << 16);
#pragma unroll
      for (int d = 0; d < 8; ++d) PIN(Dp[p][d]);
      PIN(Dt[p]);
    }
    STAMP(6);
    const unsigned vb = act3 ? OFF_B + (x & 3) * 18432 + rowl * 4 : OOB;
    v3u yb[8];
    _Pragma("unroll 1") for (int it = 0; it < GCAP; ++it) {
#pragma unroll
      for (int j = 0; j < 8; ++j) yb[j] = LD96(rws, vb + j * 2304, AUX_SC1);
      unsigned mxu = 0;
#pragma unroll
      for (int j = 0; j < 8; ++j) mxu = umax(umax(mxu, yb[j][0]), umax(yb[j][1], yb[j][2]));
      if (all_true(mxu != ~0u)) break;
      MEMFENCE();
    }
    STAMP(7);
    float xr[3];
#pragma unroll
    for (int j = 0; j < 3; ++j) {
      const float t = ((asf(yb[0][j]) + asf(yb[1][j])) + (asf(yb[2][j]) + asf(yb[3][j]))) + ((asf(yb[4][j]) + asf(yb[5][j])) + (asf(yb[6][j]) + asf(yb[7][j])));
      xr[j] = rbf((j == 0 ? x0 : j == 1 ? x1 : x2) + rbf(ga[j] * rbf(t)));
    }
    float s = (xr[0] + xr[1]) + xr[2], q = (xr[0] * xr[0] + xr[1] * xr[1]) + xr[2] * xr[2];
    s = wave_sum(s); q = wave_sum(q);
    if (lane == 0) { v2f t = {s, q}; *(v2f*)&L.st[wave][0] = t; }
    wg_barrier();
    {
      const v4f t0 = *(const v4f*)&L.st[0][0], t1 = *(const v4f*)&L.st[2][0];
      s = (t0.x + t0.z) + (t1.x + t1.z); q = (t0.y + t0.w) + (t1.y + t1.w);
    }
    float m, rsd; ln_stats(s, q, m, rsd);
#pragma unroll
    for (int j = 0; j < 3; ++j) xr[j] = rbf(rbf(rbf(rbf(xr[j] - m) * rsd) * cb[j]) + sb[j]);
    Hv H; hv_make(H, xr[0], xr[1], xr[2]);
    float ag[8], au[8]; slab_dot(ag, G, H); slab_dot(au, U, H);
    float y[8];
#pragma unroll
    for (int j = 0; j < 8; ++j) y[j] = rs32(ag[j], au[j]);
    v4f z;
#pragma unroll
    for (int j = 0; j < 4; ++j) { float t = rs16(y[j], y[j + 4]); t += dppf<R4>(t); t += dppf<R8>(t); z[j] = t; }
    if ((lane & 12) == 0) *(v4f*)&L.hp[wave][(lane >> 5) * 32 + qc * 8 + ((lane >> 4) & 1) * 4] = z;
    wg_barrier();
    const int un = 8 * wave + (lane & 7);
    const float g = rbf((L.hp[0][un] + L.hp[1][un]) + (L.hp[2][un] + L.hp[3][un]));
    const float up = rbf((L.hp[0][32 + un] + L.hp[1][32 + un]) + (L.hp[2][32 + un] + L.hp[3][32 + un]));
    const float av = rbf(silu(g)) * up;
    const unsigned ap = pack2(av, dppf<X1>(av));
    float d8[8] = {0.f, 0.f, 0.f, 0.f, 0.f, 0.f, 0.f, 0.f}, dt = 0.f;
#pragma unroll
    for (int p = 0; p < 4; ++p) {
      const unsigned a2 = __builtin_amdgcn_readlane(ap, 2 * p);
#pragma unroll
      for (int d = 0; d < 8; ++d) d8[d] = dot2(Dp[p][d], a2, d8[d]);
      dt = dot2(Dt[p], a2, dt);
    }
    { v4f t0 = {d8[0], d8[1], d8[2], d8[3]}, t1 = {d8[4], d8[5], d8[6], d8[7]};
      *(v4f*)&L.dn[wave][lane * 8] = t0; *(v4f*)&L.dn[wave][lane * 8 + 4] = t1; L.dn[wave][512 + lane] = dt; }
    wg_barrier();
    {
      const int c4 = 144 * wave + (lane < 36 ? lane : 0) * 4;
      const v4f t = (*(const v4f*)&L.dn[0][c4] + *(const v4f*)&L.dn[1][c4]) + (*(const v4f*)&L.dn[2][c4] + *(const v4f*)&L.dn[3][c4]);
      v4u pv = {canon(t.x), canon(t.y), canon(t.z), canon(t.w)};
      __builtin_amdgcn_raw_buffer_store_b128(pv, rws, lane < 36 ? OFF_C + f * 2304 + c4 * 4 : OOB, 0, AUX_SC1);
    }
    STAMP(8);
  } else {
    const int j = (r - 27) * 8 + x;
    if (wave != 0 || j >= 36) { FLUSH(); return; }
    const int col = 16 * j + 4 * qc + (lane >> 4);
    const float xv = lo16(LD16(mk(xin, 1152), col * 2, 0));
    v2u ga, gb;
    _Pragma("unroll 1") for (int it = 0; it < GCAP; ++it) {
      ga = LD64(rws, mcopy + 2 * 2304 + (col >> 1) * 8, AUX_SC1); gb = LD64(rws, mcopy + 5 * 2304 + (col >> 1) * 8, AUX_SC1);
      unsigned bad = (ga[1] ^ tag) | (gb[1] ^ tag);
      if (all_true(bad == 0)) break;
      MEMFENCE();
    }
    const float gav = lo16(ga[0] >> ((col & 1) * 16)), gbv = lo16(gb[0] >> ((col & 1) * 16));
    lds_wait(&L.cnt[3], 9);
    STAMP(5);
    unsigned yb[8];
    _Pragma("unroll 1") for (int it = 0; it < GCAP; ++it) {
#pragma unroll
      for (int t = 0; t < 8; ++t) yb[t] = LD32(rws, OFF_B + (x & 3) * 18432 + t * 2304 + col * 4, AUX_SC1);
      unsigned mxu = 0;
#pragma unroll
      for (int t = 0; t < 8; ++t) mxu = umax(mxu, yb[t]);
      if (all_true(mxu != ~0u)) break;
      MEMFENCE();
    }
    const float ys = ((asf(yb[0]) + asf(yb[1])) + (asf(yb[2]) + asf(yb[3]))) + ((asf(yb[4]) + asf(yb[5])) + (asf(yb[6]) + asf(yb[7])));
    float x1 = rbf(xv + rbf(gav * rbf(ys)));
    PIN(x1);
    STAMP(6);
    v4u cc[9];
    _Pragma("unroll 1") for (int it = 0; it < GCAP; ++it) {
#pragma unroll
      for (int t = 0; t < 9; ++t) cc[t] = LD128(rws, OFF_C + (16 * t + qr) * 2304 + j * 64 + qc * 16, AUX_SC1);
      unsigned mxu = 0;
#pragma unroll
      for (int t = 0; t < 9; ++t) mxu = umax(umax(mxu, umax(cc[t][0], cc[t][1])), umax(cc[t][2], cc[t][3]));
      if (all_true(mxu != ~0u)) break;
      MEMFENCE();
    }
    STAMP(7);
    float a[4];
#pragma unroll
    for (int e = 0; e < 4; ++e)
      a[e] = (((asf(cc[0][e]) + asf(cc[1][e])) + (asf(cc[2][e]) + asf(cc[3][e]))) + ((asf(cc[4][e]) + asf(cc[5][e])) + (asf(cc[6][e]) + asf(cc[7][e])))) + asf(cc[8][e]);
    float t = rs16(rs32(a[0], a[2]), rs32(a[1], a[3]));
    t += dppf<R4>(t); t += dppf<R8>(t);
    const float res = x1 + rbf(gbv * rbf(t));
    __builtin_amdgcn_raw_buffer_store_b16((u16)pack2(res, 0.f), mk(out, 1152), (lane & 12) == 0 ? col * 2 : OOB, 0, 0);
    if (j == 0) __builtin_amdgcn_raw_buffer_store_b32(tag, rws, lane == 0 ? 0u : OOB, 0, AUX_SC1);
    STAMP(8);
  }
  FLUSH();
}

extern "C" void kernel_launch(void* const* d_in, const int* in_sizes, int n_in, void* d_out, int out_size, void* d_ws,
                              size_t ws_size, hipStream_t stream) {
  fused_block<<<256, 768, 0, stream>>>((const u16*)d_in[0], (const u16*)d_in[1], (const u16*)d_in[2], (const u16*)d_in[3],
                                       (const u16*)d_in[4], (const u16*)d_in[6], (const u16*)d_in[7], (const u16*)d_in[8],
                                       (const u16*)d_in[9], (const u16*)d_in[10], (u16*)d_out, (char*)d_ws);
}
```

```cpp
#include <hip/hip_runtime.h>
#include <hip/hip_bf16.h>
#include <stdint.h>

typedef unsigned short u16;
typedef unsigned long long u64;
typedef unsigned v4u __attribute__((ext_vector_type(4)));
typedef unsigned v3u __attribute__((ext_vector_type(3)));
typedef unsigned v2u __attribute__((ext_vector_type(2)));
typedef float v2f __attribute__((ext_vector_type(2)));
typedef float v4f __attribute__((ext_vector_type(4)));
typedef __bf16 bf2 __attribute__((ext_vector_type(2)));
typedef __amdgpu_buffer_rsrc_t rsrc_t;

#define TF 4
#define TS 5
#define STAMPS 0
#define WTRIG 0
#define ENDX 0
#define PAT 0
#define DTRIG 17
#define GTRIG 9
#define GDEL 10
#define SLIM 255
#define PTRIG 8
#define PDEL 0
#define PSTEP 1
#define GAPK 0
#define WOE 4
#define DDEL 0
#define ROTX ((head >> 2) * 64)
#define NL 1
#define CPRE 5
#define MDEL 2
#define PD1 30
#define PD2 40
#define LATEQ 1

constexpr int AUX_NT = 2, AUX_SC1 = 16;
constexpr unsigned TAG_STEP = 0x01010101u;
constexpr unsigned OOB = 0x40000000u;
constexpr unsigned OFF_T = 4096;
constexpr unsigned OFF_P = 16384;
constexpr unsigned OFF_Q = OFF_P + 64 * 896;
constexpr unsigned OFF_M = OFF_Q + 256 * 768;
constexpr unsigned M_COPY = 4 * 2304;
constexpr unsigned OFF_AO = OFF_M + 8 * M_COPY;
constexpr unsigned OFF_AM = OFF_AO + 131072;
constexpr unsigned OFF_B = OFF_AM + 4096;
constexpr unsigned OFF_C = OFF_B + 4 * 18432;
constexpr unsigned WS_END = OFF_C + 331776;
constexpr unsigned OFF_STAMP = 1u << 20;
constexpr int GCAP = 1 << 15;
constexpr int LCAP = 1 << 20;
constexpr float LOG2E = 1.4426950408889634f;

__device__ __forceinline__ float asf(unsigned u) { return __builtin_bit_cast(float, u); }
__device__ __forceinline__ unsigned asu(float f) { return __builtin_bit_cast(unsigned, f); }
__device__ __forceinline__ float lo16(unsigned u) { return asf(u << 16); }
__device__ __forceinline__ float hi16(unsigned u) { return asf(u & 0xffff0000u); }
__device__ __forceinline__ float rbf(float f) { __bf16 b = (__bf16)f; return (float)b; }
__device__ __forceinline__ unsigned pack2(float a, float b) { bf2 v = {(__bf16)a, (__bf16)b}; return __builtin_bit_cast(unsigned, v); }
__device__ __forceinline__ float dot2(unsigned a, unsigned b, float c) {
  return __builtin_amdgcn_fdot2_f32_bf16(__builtin_bit_cast(bf2, a), __builtin_bit_cast(bf2, b), c, false);
}
template <int CTRL> __device__ __forceinline__ unsigned dppu(unsigned v) { return __builtin_amdgcn_update_dpp(0u, v, CTRL, 0xf, 0xf, true); }
template <int CTRL> __device__ __forceinline__ float dppf(float v) { return asf(dppu<CTRL>(asu(v))); }
constexpr int X1 = 0xB1, X2 = 0x4E, HM = 0x141, R4 = 0x124, R8 = 0x128;
__device__ __forceinline__ float rs32(float a, float b) {
  auto r = __builtin_amdgcn_permlane32_swap(asu(a), asu(b), false, false);
  unsigned r0 = r[0], r1 = r[1]; return asf(r0) + asf(r1);
}
__device__ __forceinline__ float rs16(float a, float b) {
  auto r = __builtin_amdgcn_permlane16_swap(asu(a), asu(b), false, false);
  unsigned r0 = r[0], r1 = r[1]; return asf(r0) + asf(r1);
}
__device__ __forceinline__ float mx32(float a) {
  auto r = __builtin_amdgcn_permlane32_swap(asu(a), asu(a), false, false);
  unsigned r0 = r[0], r1 = r[1]; return fmaxf(asf(r0), asf(r1));
}
__device__ __forceinline__ float mx16(float a) {
  auto r = __builtin_amdgcn_permlane16_swap(asu(a), asu(a), false, false);
  unsigned r0 = r[0], r1 = r[1]; return fmaxf(asf(r0), asf(r1));
}
__device__ __forceinline__ float wave_sum(float v) {
  v += dppf<X1>(v); v += dppf<X2>(v); v += dppf<R4>(v); v += dppf<R8>(v);
  v = rs16(v, v); v = rs32(v, v); return v;
}
__device__ __forceinline__ float ex2(float x) { return __builtin_amdgcn_exp2f(x); }
__device__ __forceinline__ float rcp(float x) { return __builtin_amdgcn_rcpf(x); }
__device__ __forceinline__ float silu(float g) { return g * rcp(1.f + ex2(-g * LOG2E)); }
__device__ __forceinline__ v4u seal(v4f t, unsigned tg) {
  v4u o;
#pragma unroll
  for (int j = 0; j < 4; ++j) o[j] = (asu(t[j]) & ~0xffu) | ((tg >> (8 * j)) & 0xffu);
  return o;
}
__device__ __forceinline__ unsigned stale(v4u v, unsigned tg) {
  return ((v[0] ^ tg) | (v[1] ^ (tg >> 8))) | ((v[2] ^ (tg >> 16)) | (v[3] ^ (tg >> 24)));
}
__device__ __forceinline__ bool all_true(bool ok) { return __builtin_amdgcn_ballot_w64(!ok) == 0; }
__device__ __forceinline__ rsrc_t mk(const void* p, unsigned n) { return __builtin_amdgcn_make_buffer_rsrc((void*)p, 0, n, 0x00020000); }
#define LD128(r, v, aux) __builtin_amdgcn_raw_buffer_load_b128(r, v, 0, aux)
#define LD96(r, v, aux) __builtin_amdgcn_raw_buffer_load_b96(r, v, 0, aux)
#define LD64(r, v, aux) __builtin_amdgcn_raw_buffer_load_b64(r, v, 0, aux)
#define LD32(r, v, aux) __builtin_amdgcn_raw_buffer_load_b32(r, v, 0, aux)
#define LD16(r, v, aux) __builtin_amdgcn_raw_buffer_load_b16(r, v, 0, aux)
#define SB() __builtin_amdgcn_sched_barrier(0)
#define MEMFENCE() asm volatile("" ::: "memory")
#define WAIT_LGKM0() asm volatile("s_waitcnt lgkmcnt(0)" ::: "memory")
#define WAIT_VM0() asm volatile("s_waitcnt vmcnt(0)" ::: "memory")
#define PIN(x) asm volatile("" : "+v"(x))
__device__ __forceinline__ void wg_barrier() { WAIT_LGKM0(); __builtin_amdgcn_s_barrier(); MEMFENCE(); }

struct __attribute__((aligned(16))) Lds {
  float rec_o[8][128];
  float rec_ml[8][2];
  float dn[4][576];
  float hp[4][64];
  unsigned ob[4][64];
  unsigned qs[64];
  unsigned hs[40];
  unsigned sink[64];
  float st[4][2];
  v4u stg[8][NL ? NL : 1][8][64];
  unsigned cnt[8];
};

__device__ __forceinline__ void lds_wait(const unsigned* w, unsigned target) {
  _Pragma("unroll 1") for (int it = 0; it < LCAP; ++it) {
    unsigned v = __hip_atomic_load(w, __ATOMIC_RELAXED, __HIP_MEMORY_SCOPE_WORKGROUP);
    if (__builtin_amdgcn_readfirstlane(v) >= target) break;
    __builtin_amdgcn_s_sleep(1);
  }
  MEMFENCE();
}

struct Slab { unsigned p[4][8]; unsigned z[4]; };
__device__ __forceinline__ void interleave(unsigned (&o)[8], v4u a, v4u b) {
#pragma unroll
  for (int d = 0; d < 4; ++d) {
    unsigned x = a[d], y = b[d];
    o[2 * d] = __builtin_amdgcn_perm(y, x, 0x05040100u);
    o[2 * d + 1] = __builtin_amdgcn_perm(y, x, 0x07060302u);
  }
}
__device__ __forceinline__ void slab_make(Slab& s, const v4u (&w)[9]) {
#pragma unroll
  for (int p = 0; p < 4; ++p) {
    interleave(s.p[p], w[2 * p], w[2 * p + 1]); s.z[p] = w[8][p];
#pragma unroll
    for (int d = 0; d < 8; ++d) PIN(s.p[p][d]);
    PIN(s.z[p]);
  }
}
struct Hv { unsigned p[4], za, zb; };
__device__ __forceinline__ void hv_silu(Hv& h, v4u a, unsigned z) {
#pragma unroll
  for (int p = 0; p < 4; ++p) h.p[p] = pack2(silu(lo16(a[p])), silu(hi16(a[p])));
  h.za = pack2(silu(lo16(z)), 0.f); h.zb = h.za << 16;
}
__device__ __forceinline__ void slab_dot(float (&acc)[8], const Slab& s, const Hv& h) {
#pragma unroll
  for (int d = 0; d < 4; ++d) { acc[2 * d] = dot2(s.z[d], h.za, 0.f); acc[2 * d + 1] = dot2(s.z[d], h.zb, 0.f); }
#pragma unroll
  for (int p = 0; p < 4; ++p)
#pragma unroll
    for (int d = 0; d < 8; ++d) acc[d] = dot2(s.p[p][d], h.p[p], acc[d]);
}
__device__ __forceinline__ v4f fold8(const float (&acc)[8]) {
  v4f y;
#pragma unroll
  for (int j = 0; j < 4; ++j) { float t = rs32(acc[j], acc[j + 4]); t = rs16(t, t); y[j] = t + dppf<R8>(t); }
  return y;
}
__device__ __forceinline__ void ln_stats(float s, float q, float& m, float& rsd) {
  float mean = s * (1.f / 576.f);
  float var = fmaxf(q * (1.f / 576.f) - mean * mean, 0.f);
  m = rbf(mean);
  float sd = rbf(__builtin_sqrtf(rbf(rbf(var) + 1e-5f)));
  rsd = 1.f / sd;
}
__device__ __forceinline__ float modulate(float t, float scale, float shift) { return rbf(rbf(t * rbf(1.f + scale)) + shift); }

#define PROF() do {} while (0)
#define STAMP(k) do {} while (0)
#define FLUSH() do {} while (0)

struct Args { const u16 *xin, *wq, *cin, *wmod; char* ws; u16* out; const u16 *wo, *wd, *wg, *wu, *Kc, *Vc; };
typedef const Args __attribute__((address_space(4))) * args_t;
__device__ __forceinline__ args_t args_ptr() {
  args_t p = (args_t)__builtin_amdgcn_kernarg_segment_ptr();
  asm volatile("" : "+s"(p));
  return p;
}

__global__ void __launch_bounds__(768) fused_block(Args) {
  __shared__ Lds L;
  const int tid = threadIdx.x, lane = tid & 63, wave = __builtin_amdgcn_readfirstlane(tid >> 6);
  const int b = blockIdx.x, x = b & 7, head = b >> 3, r = (head + 4 * x) & 31;
  STAMP(0);
  const int k = r >> 2;
  const int r8 = lane >> 3, c8 = lane & 7;
  const unsigned sink = (unsigned)(size_t)(__attribute__((address_space(3))) void*)L.sink;

  const bool is_mod = wave == 0, ffn = r >= 9 && r < 27;
  unsigned ep, cz;
  v4u ci, w[9];
  const u16 *xin, *cin, *wmod, *wo, *wd, *wg, *wu; u16* out; char* ws;
  if (wave < 4) {
    __builtin_amdgcn_s_setprio(3);
    const args_t A = args_ptr();
    const u16* wq = A->wq;
    xin = A->xin; cin = A->cin; wmod = A->wmod; ws = A->ws; out = A->out; wo = A->wo; wd = A->wd; wg = A->wg; wu = A->wu;
    if (tid < 8) L.cnt[tid] = 0;
    if (CPRE && wave == 3) {
      u64 pc; unsigned junk;
      asm volatile("s_getpc_b64 %0" : "=s"(pc));
      const v4u rc = {(unsigned)pc & ~255u, (unsigned)(pc >> 32) & 0xffffu, CPRE * 4096u, 0x00020000u};
      asm volatile("s_mov_b32 m0, %2\n\ts_nop 1\n\tbuffer_load_dword %0, %1, 0 offen lds" :: "v"(lane < CPRE ? (lane * 32 + r) * 128 : OOB), "s"(rc), "s"(sink) : "m0");
    }
    unsigned ld = is_mod ? (ffn ? 6912 : 0) : (wave == 3 ? 0 : 8192);
    unsigned moff = 72 * x * ld + (is_mod ? r - 9 : 2 * head + wave - 1) * 128;
    unsigned v0 = 8 * r8 * ld + c8 * 16, v8 = (64 + r8) * ld + c8 * 16, vi = r8 * 16, vz = 128 + r8 * 2;
    unsigned vt = (lane == 0 && wave == 1 && x == 0) ? OFF_T + head * 256 : OOB, t0 = 64 * NL;
    asm volatile("" : "+s"(ld), "+s"(moff));
    asm volatile("" : "+v"(v0), "+v"(v8), "+v"(vz), "+v"(vi), "+v"(vt), "+v"(t0));
    SB();
    asm volatile("" :: "s"(xin), "s"(wq), "s"(wo), "s"(wg), "s"(wu), "s"(wd), "s"(cin), "s"(wmod), "s"(out), "s"(ws));
    const u64 wa = (u64)ws, ia = (u64)cin + 144 * x, ma = (u64)(is_mod ? wmod : wq) + moff;
    const v4u r0 = {(unsigned)wa, (unsigned)(wa >> 32) & 0xffffu, WS_END, 0x00020000u};
    const v4u ri = {(unsigned)ia, (unsigned)(ia >> 32) & 0xffffu, 144u, 0x00020000u};
    const v4u rm = {(unsigned)ma, (unsigned)(ma >> 32) & 0xffffu, 71 * ld + 128, 0x00020000u};
    unsigned so;
#define LDW(n) "buffer_load_dwordx4 %" #n ", %[v0], %[rm], %[so] offen nt\n\ts_add_u32 %[so], %[so], %[ld]\n\t"
    asm volatile("s_cmp_eq_u32 %[ld], 0\n\ts_cbranch_scc1 1f\n\ts_mov_b32 %[so], 0\n\t"
                 "buffer_load_dword %9, off, %[r0], 0\n\tbuffer_load_dwordx4 %10, %[vi], %[ri], 0 offen\n\t"
                 "buffer_load_ushort %11, %[vz], %[ri], 0 offen\n\t" LDW(0) LDW(1) LDW(2) LDW(3) LDW(4) LDW(5) LDW(6) LDW(7)
                 "buffer_load_dwordx4 %8, %[v8], %[rm], 0 offen nt\n\tbuffer_store_dword %[t0], %[vt], %[r0], 0 offen sc1\n"
                 "1:\n\ts_waitcnt lgkmcnt(0)\n\ts_barrier"
                 : "=&v"(w[0]), "=&v"(w[1]), "=&v"(w[2]), "=&v"(w[3]), "=&v"(w[4]), "=&v"(w[5]), "=&v"(w[6]), "=&v"(w[7]), "=&v"(w[8]),
                   "=&v"(ep), "=&v"(ci), "=&v"(cz), [so] "=&s"(so)
                 : [r0] "s"(r0), [ri] "s"(ri), [rm] "s"(rm), [v0] "v"(v0), [v8] "v"(v8), [vi] "v"(vi), [vz] "v"(vz), [ld] "s"(ld),
                   [vt] "v"(vt), [t0] "v"(t0)
                 : "scc", "memory");
  }

  if (wave >= 4) {
    const int s = wave - 4;
    const args_t A = args_ptr();
    const u16 *Kc = A->Kc, *Vc = A->Vc, *wg = A->wg, *wu = A->wu; char* ws = A->ws;
    const rsrc_t rws = mk(ws, WS_END);
    const u64 ka = (u64)(Kc + ((size_t)head << 20)), va = (u64)(Vc + ((size_t)head << 20)), wa = (u64)ws;
    const v4u rk = {(unsigned)ka, (unsigned)(ka >> 32) & 0xffffu, 1u << 21, 0x00020000u};
    const v4u rv = {(unsigned)va, (unsigned)(va >> 32) & 0xffffu, 1u << 21, 0x00020000u};
    const v4u rt = {(unsigned)wa, (unsigned)(wa >> 32) & 0xffffu, OFF_P, 0x00020000u};
    const unsigned vo = lane * 16, vt = lane == 0 ? OFF_T + head * 256 : OOB;
    const unsigned rot = ROTX, p0 = rot + k * 8 + s;
    v4u Kr[3][4], Vr[3][4];
    asm volatile("s_barrier" :: "s"(rk), "s"(rv), "s"(p0), "v"(vo) : "memory");
    STAMP(10);
#define S_(x) #x
#define S(x) S_(x)
#define LD1(n, off, T) "s_waitcnt vmcnt(" S(T) "-1)\n\tbuffer_load_dwordx4 %" #n ", %[vo], %[rs], %[so] offen offset:" #off " nt\n\t"
#define VOS [vo] "v"(vo)
#define CHUNK(c) (((c) & 511) << 12)
#define LD4(T) LD1(0, 0, T) LD1(1, 1024, T) LD1(2, 2048, T) LD1(3, 3072, T)
#define LD4E(T) "s_cmp_ge_i32 %[tn], 256-" S(ENDX) "\n\ts_cbranch_scc1 2f\n\t" LD4(T) "s_branch 1f\n2:\n\t" LD4(40)
#define LL1(off) "s_waitcnt vmcnt(" S(TF) "-1)\n\tbuffer_load_dwordx4 %[vo], %[rs], %[so] offen offset:" #off " nt lds\n\t"
#pragma unroll
    for (int i = 0; i < NL; ++i) {
      const unsigned so = CHUNK(p0 + 64 * i), lb = (unsigned)(size_t)(__attribute__((address_space(3))) void*)L.stg[s][i];
      asm volatile("s_mov_b32 m0, %[lb]\n\t" LL1(0) LL1(1024) LL1(2048) LL1(3072) :: VOS, [rs] "s"(rk), [so] "s"(so), [lb] "s"(lb) : "m0", "memory");
      PROF();
      asm volatile("s_mov_b32 m0, %[lb]\n\t" LL1(0) LL1(1024) LL1(2048) LL1(3072) :: VOS, [rs] "s"(rv), [so] "s"(so), [lb] "s"(lb + 4096) : "m0", "memory");
      PROF();
    }
#pragma unroll
    for (int i = 0; i < 3; ++i) {
      const unsigned so = CHUNK(p0 + 64 * (NL + i));
      asm volatile(LD4(TF) : "=&v"(Kr[i][0]), "=&v"(Kr[i][1]), "=&v"(Kr[i][2]), "=&v"(Kr[i][3]) : VOS, [rs] "s"(rk), [so] "s"(so));
      PROF();
      asm volatile(LD4(TF) : "=&v"(Vr[i][0]), "=&v"(Vr[i][1]), "=&v"(Vr[i][2]), "=&v"(Vr[i][3]) : VOS, [rs] "s"(rv), [so] "s"(so));
      PROF();
      if (i == 0) STAMP(11);
    }
    STAMP(6);
    const int pi = (s & 3) * 41 + lane;
    const unsigned pv = (lane < 41 && pi < 162) ? (18 * r + pi / 9) * 9216 + x * 1152 + (pi % 9) * 128 : OOB;
    const u64 pa = (u64)(s < 4 ? wg : wu);
    const v4u rp = {(unsigned)pa, (unsigned)(pa >> 32) & 0xffffu, 576u * 9216, 0x00020000u};
    lds_wait(&L.cnt[4], 1);
    const v4u qv = *(const v4u*)&L.qs[(lane & 15) * 4];
    const unsigned q0 = qv[0], q1 = qv[1], q2 = qv[2], q3 = qv[3];
    STAMP(7);
    float m = -INFINITY, l = 0.f;
    v2f acc[4] = {{0.f, 0.f}, {0.f, 0.f}, {0.f, 0.f}, {0.f, 0.f}};
    unsigned tk = k * 8 + s - 64 + 64 * NL;
    int rem = 3;
    const int lim = s >= 4 ? SLIM : 255;
#define TICKET "v_mov_b32 %[tk], 0x400\n\ts_cmp_ge_i32 %[tn], %[lim]\n\ts_cbranch_scc1 4f\n\tv_mov_b32 %[tk], 1\n\tbuffer_atomic_add %[tk], %[vt], %[rt], 0 offen sc0 sc1\n4:\n\t"
#define PREF "s_cmp_eq_u32 %[rem], " S(PAT) "\n\ts_cbranch_scc0 3f\n\ts_mov_b32 m0, %[sink]\n\ts_nop 1\n\tbuffer_load_dword %[pv], %[rp], 0 offen nt lds\n\tbuffer_load_dword %[pv], %[rp], 0 offen sc1 lds\n3:"
#define LASTW 0
#define SOFT \
        SB();                                                                                       \
        _Pragma("unroll") for (int t = 0; t < 4; ++t) sc[t] += dppf<X1>(sc[t]);                     \
        SB();                                                                                       \
        _Pragma("unroll") for (int t = 0; t < 4; ++t) sc[t] += dppf<X2>(sc[t]);                     \
        SB();                                                                                       \
        _Pragma("unroll") for (int t = 0; t < 4; ++t) sc[t] += dppf<R4>(sc[t]);                     \
        SB();                                                                                       \
        _Pragma("unroll") for (int t = 0; t < 4; ++t) sc[t] += dppf<R8>(sc[t]);                     \
        SB();                                                                                       \
        _Pragma("unroll") for (int t = 0; t < 4; ++t) sc[t] = rbf(rbf(sc[t]) * 0.08838834764831845f); \
        const float mn = fmaxf(fmaxf(fmaxf(sc[0], sc[1]), fmaxf(sc[2], sc[3])), m);                 \
        const float al = ex2((m - mn) * LOG2E);                                                     \
        m = mn;                                                                                     \
        float p[4];                                                                                 \
        _Pragma("unroll") for (int t = 0; t < 4; ++t) p[t] = ex2((sc[t] - mn) * LOG2E);             \
        l = l * al + ((p[0] + p[1]) + (p[2] + p[3]));                                               \
        _Pragma("unroll") for (int j = 0; j < 4; ++j) acc[j] *= al;
#define STEP(i)                                                                                     \
      {                                                                                             \
        float sc[4];                                                                                \
        asm volatile("" : "+v"(Kr[i][0]), "+v"(Kr[i][1]), "+v"(Kr[i][2]), "+v"(Kr[i][3]));          \
        _Pragma("unroll") for (int t = 0; t < 4; ++t) {                                             \
          const v4u kk = Kr[i][t];                                                                  \
          sc[t] = dot2(kk[3], q3, dot2(kk[2], q2, dot2(kk[1], q1, dot2(kk[0], q0, 0.f))));          \
        }                                                                                           \
        asm volatile("s_waitcnt vmcnt(4)" : "+v"(tk), "+v"(sc[0]), "+v"(sc[1]), "+v"(sc[2]), "+v"(sc[3])); \
        const int tn = __builtin_amdgcn_readfirstlane(tk);                                          \
        rem = tn < 256 ? 3 : rem - 1;                                                               \
        const unsigned so = CHUNK(tn + 256 + rot);                                         \
        asm volatile("s_cmp_eq_u32 %[rem], 3\n\ts_cbranch_scc0 1f\n\ts_nop 1\n\t" LD4E(TS) "1:"      \
                     : "+v"(Kr[i][0]), "+v"(Kr[i][1]), "+v"(Kr[i][2]), "+v"(Kr[i][3])               \
                     : VOS, [rs] "s"(rk), [so] "s"(so), [rem] "s"(rem), [tn] "s"(tn) : "scc"); \
        if (rem == 3) PROF();                                                                       \
        if (rem == 2) {                                                                             \
          if (lane == 0) __hip_atomic_fetch_add(&L.cnt[5], 1u, __ATOMIC_RELAXED, __HIP_MEMORY_SCOPE_WORKGROUP); \
          __builtin_amdgcn_s_setprio(3);                                                            \
          STAMP(8);                                                                                 \
        }                                                                                           \
        SOFT                                                                                        \
        asm volatile("s_cmp_eq_u32 %[rem], 0\n\ts_cbranch_scc0 1f\n\ts_waitcnt vmcnt(" S(LASTW) ")\n1:\n\t" PREF \
                     : "+v"(Vr[i][0]), "+v"(Vr[i][1]), "+v"(Vr[i][2]), "+v"(Vr[i][3]), "+v"(l),     \
                       "+v"(acc[0]), "+v"(acc[1]), "+v"(acc[2]), "+v"(acc[3])                       \
                     : [rem] "s"(rem), [pv] "v"(pv), [rp] "s"(rp), [sink] "s"(sink) : "scc", "m0");  \
        _Pragma("unroll") for (int t = 0; t < 4; ++t) {                                             \
          const v4u vv = Vr[i][t];                                                                  \
          const v2f pp = {p[t], p[t]};                                                              \
          _Pragma("unroll") for (int j = 0; j < 4; ++j) {                                           \
            const v2f e = {lo16(vv[j]), hi16(vv[j])};                                               \
            acc[j] = __builtin_elementwise_fma(pp, e, acc[j]);                                      \
          }                                                                                         \
        }                                                                                           \
        asm volatile("s_cmp_eq_u32 %[rem], 3\n\ts_cbranch_scc0 1f\n\t" TICKET LD4E(TS) "1:"          \
                     : "+v"(Vr[i][0]), "+v"(Vr[i][1]), "+v"(Vr[i][2]), "+v"(Vr[i][3]), [tk] "+v"(tk), \
                       "+v"(acc[0]), "+v"(acc[1]), "+v"(acc[2]), "+v"(acc[3])                       \
                     : VOS, [rs] "s"(rv), [so] "s"(so), [rem] "s"(rem), [vt] "v"(vt), [rt] "s"(rt), [tn] "s"(tn), [lim] "s"(lim) : "scc", "memory"); \
        if (rem == 3 || rem == 0) PROF(); \
        if (rem == 0) break;                                                                        \
      }
    _Pragma("unroll 1") for (int c = 0; c < NL; ++c) {
      float sc[4];
#pragma unroll
      for (int t = 0; t < 4; ++t) {
        const v4u kk = L.stg[s][c][t][lane];
        sc[t] = dot2(kk[3], q3, dot2(kk[2], q2, dot2(kk[1], q1, dot2(kk[0], q0, 0.f))));
      }
      SOFT
      SB();
#pragma unroll
      for (int t = 0; t < 4; ++t) {
        const v4u vv = L.stg[s][c][4 + t][lane];
        const v2f pp = {p[t], p[t]};
#pragma unroll
        for (int j = 0; j < 4; ++j) { const v2f e = {lo16(vv[j]), hi16(vv[j])}; acc[j] = __builtin_elementwise_fma(pp, e, acc[j]); }
      }
    }
    SB();
    _Pragma("unroll 1") for (int it = 0; it < 200; ++it) { STEP(0) STEP(1) STEP(2) }
#undef STEP
    STAMP(4);
    const float M = mx32(mx16(m));
    const float w = ex2((m - M) * LOG2E);
    l *= w; l = rs16(l, l); l = rs32(l, l);
    float a[8];
#pragma unroll
    for (int j = 0; j < 4; ++j) { a[2 * j] = acc[j].x * w; a[2 * j + 1] = acc[j].y * w; }
    float y[4];
#pragma unroll
    for (int j = 0; j < 4; ++j) y[j] = rs32(a[j], a[j + 4]);
    v2f z = {rs16(y[0], y[2]), rs16(y[1], y[3])};
    *(v2f*)&L.rec_o[s][(lane & 15) * 8 + (lane >> 4) * 2] = z;
    if (lane == 0) { v2f ml = {M, l}; *(v2f*)&L.rec_ml[s][0] = ml; }
    WAIT_LGKM0();
    unsigned old = 0;
    if (lane == 0) old = __hip_atomic_fetch_add(&L.cnt[3], 1u, __ATOMIC_RELAXED, __HIP_MEMORY_SCOPE_WORKGROUP);
    old = __builtin_amdgcn_readfirstlane(old);
    MEMFENCE();
    if (old == 7) {
      float ms[8], ls[8];
      const unsigned tgs = L.cnt[7];
#pragma unroll
      for (int j = 0; j < 4; ++j) { v4f t = *(const v4f*)&L.rec_ml[2 * j][0]; ms[2 * j] = t.x; ls[2 * j] = t.y; ms[2 * j + 1] = t.z; ls[2 * j + 1] = t.w; }
      v4f o[8];
#pragma unroll
      for (int j = 0; j < 8; ++j) o[j] = *(const v4f*)&L.rec_o[j][(lane & 31) * 4];
      const float MM = fmaxf(fmaxf(fmaxf(ms[0], ms[1]), fmaxf(ms[2], ms[3])), fmaxf(fmaxf(ms[4], ms[5]), fmaxf(ms[6], ms[7])));
      float LL = 0.f; v4f oo = {0.f, 0.f, 0.f, 0.f};
#pragma unroll
      for (int j = 0; j < 8; ++j) { const float e = ex2((ms[j] - MM) * LOG2E); LL += e * ls[j]; oo += o[j] * e; }
      if (lane >= 32) { oo.x = MM; oo.y = LL; oo.z = 0.f; oo.w = 0.f; }
      const v4u pv = seal(oo, tgs);
      const unsigned off = lane < 32 ? OFF_AO + b * 512 + lane * 16 : (lane == 32 ? OFF_AM + b * 16 : OOB);
      __builtin_amdgcn_raw_buffer_store_b128(pv, rws, off, 0, AUX_SC1);
      if (lane == 0) __hip_atomic_fetch_add(&L.cnt[3], 1u, __ATOMIC_RELAXED, __HIP_MEMORY_SCOPE_WORKGROUP);
      STAMP(5);
    }
    if (PAT == -2 && s >= 4) {
      __builtin_amdgcn_s_setprio(0);
      const u64 ga = (u64)wg, ua = (u64)wu;
      const v4u rg = {(unsigned)ga, (unsigned)(ga >> 32) & 0xffffu, 576u * 9216, 0x00020000u};
      const v4u ru = {(unsigned)ua, (unsigned)(ua >> 32) & 0xffffu, 576u * 9216, 0x00020000u};
      lds_wait(&L.cnt[PTRIG > 8 ? 3 : 5], (PTRIG > 8 ? PTRIG - 8 : PTRIG) - PSTEP * (7 - s) - (r < 9 ? WOE : 0));
      if (PDEL) __builtin_amdgcn_s_sleep(PDEL);
      STAMP(9);
      asm volatile("s_mov_b32 m0, %2\n\ts_nop 1\n\tbuffer_load_dword %0, %1, 0 offen nt lds\n\tbuffer_load_dword %0, %1, 0 offen sc1 lds\n\ts_waitcnt vmcnt(0)"
                   :: "v"(pv), "s"(rg), "s"(sink) : "m0");
      asm volatile("s_mov_b32 m0, %2\n\ts_nop 1\n\tbuffer_load_dword %0, %1, 0 offen nt lds\n\tbuffer_load_dword %0, %1, 0 offen sc1 lds"
                   :: "v"(pv), "s"(ru), "s"(sink) : "m0");
    }
    WAIT_VM0();
    FLUSH();
    return;
  }

  const rsrc_t rws = mk(ws, WS_END);
  if (wave == 3) {
    const int hrow = (lane & 8) ? 64 + 3 * (lane & 3) + ((lane & 16) ? 2 : lane >> 5) : 8 * c8 + ((lane >> 3) & 6);
    const rsrc_t rin = mk(xin, 1152);
    const unsigned e3p = LD32(rws, 0, 0);
    const v4u xa = LD128(rin, lane * 16, 0), xb = LD128(rin, lane * 16 + 1024, 0);
    const unsigned xp = LD32(rin, (72 * x + (hrow & ~1)) * 2, 0);
    const unsigned tg = __builtin_amdgcn_readfirstlane(e3p) + TAG_STEP;
    float s = 0.f, q = 0.f;
#pragma unroll
    for (int d = 0; d < 4; ++d) {
      float e0 = lo16(xa[d]), e1 = hi16(xa[d]), e2 = lo16(xb[d]), e3 = hi16(xb[d]);
      s += (e0 + e1) + (e2 + e3); q += (e0 * e0 + e1 * e1) + (e2 * e2 + e3 * e3);
    }
    s = wave_sum(s); q = wave_sum(q);
    float m, rsd; ln_stats(s, q, m, rsd);
    float n0 = rbf(rbf(lo16(xp >> ((hrow & 1) * 16)) - m) * rsd), n1 = rbf(rbf(hi16(xp) - m) * rsd);
    PIN(n0); PIN(n1);
    if (PD1) __builtin_amdgcn_s_sleep(PD1);
    STAMP(1);
    const unsigned vp = OFF_P + (x * 8 + r8) * 896 + c8 * 16;
    v4u g[7];
    _Pragma("unroll 1") for (int it = 0; it < GCAP; ++it) {
      unsigned bad = 0;
#pragma unroll
      for (int t = 0; t < 6; ++t) g[t] = LD128(rws, vp + t * 128, AUX_SC1);
      g[6] = LD128(rws, vp + ((lane & 3) == 3 ? 752 : 768), AUX_SC1);
#pragma unroll
      for (int t = 0; t < 7; ++t) bad |= g[t][3] ^ tg;
      if (all_true(bad == 0)) break;
      MEMFENCE();
    }
    STAMP(2);
#define G3(t, j) asf(g[(t) + (j) / 3][(j) % 3])
    float sa[2], ca[2];
#pragma unroll
    for (int e = 0; e < 2; ++e) {
      sa[e] = rs16(rs32(G3(0, e), G3(0, e + 4)), rs32(G3(0, e + 2), G3(0, e + 6)));
      ca[e] = rs16(rs32(G3(3, e), G3(3, e + 4)), rs32(G3(3, e + 2), G3(3, e + 6)));
      sa[e] += dppf<R8>(sa[e]); ca[e] += dppf<R8>(ca[e]);
    }
    float z = rs16(rs32(G3(6, 0), G3(6, 1)), rs32(G3(6, 2), G3(6, 2)));
    z += dppf<R8>(z);
    const float zc = dppf<R4>(z);
    if (lane & 8) { sa[0] = z; ca[0] = zc; }
    const unsigned hh = pack2(modulate(n0, rbf(ca[0]), rbf(sa[0])), modulate(n1, rbf(ca[1]), rbf(sa[1])));
    if ((lane & 8) == 0) L.hs[hrow >> 1] = hh;
    else if ((lane & 4) == 0) ((u16*)L.hs)[hrow] = (u16)hh;
    WAIT_LGKM0();
    if (lane == 0) __hip_atomic_store(&L.cnt[0], 1u, __ATOMIC_RELAXED, __HIP_MEMORY_SCOPE_WORKGROUP);
    if (PD2) __builtin_amdgcn_s_sleep(PD2);
    STAMP(3);
    const unsigned vq = OFF_Q + (head * 8 + r8) * 768 + c8 * 16;
    _Pragma("unroll 1") for (int it = 0; it < GCAP; ++it) {
      unsigned bad = 0;
#pragma unroll
      for (int t = 0; t < 6; ++t) g[t] = LD128(rws, vq + t * 128, AUX_SC1);
#pragma unroll
      for (int t = 0; t < 6; ++t) bad |= g[t][3] ^ tg;
      if (all_true(bad == 0)) break;
      MEMFENCE();
    }
#define Q3(j) rs32(asf(g[(j) / 3 * 2][(j) % 3]), asf(g[(j) / 3 * 2 + 1][(j) % 3]))
    float qq[4];
#pragma unroll
    for (int e = 0; e < 4; ++e) { const float t = rs16(Q3(e), Q3(e + 4)); qq[e] = t + dppf<R8>(t); }
    const v2u qp = {pack2(qq[0], qq[1]), pack2(qq[2], qq[3])};
    *(v2u*)&L.qs[((lane >> 5) * 8 + c8) * 4 + ((lane >> 3) & 2)] = qp;
    L.cnt[7] = tg;
    WAIT_LGKM0();
    if (lane == 0) __hip_atomic_store(&L.cnt[4], 1u, __ATOMIC_RELAXED, __HIP_MEMORY_SCOPE_WORKGROUP);
    STAMP(4);
  } else if (!is_mod || ffn) {
    asm volatile("s_waitcnt vmcnt(9)" : "+v"(ep), "+v"(ci), "+v"(cz) :: "memory");
    const unsigned tg0 = __builtin_amdgcn_readfirstlane(ep) + TAG_STEP;
    const int u = lane >> 4;
    unsigned vo;
    Hv H;
    if (is_mod) {
      const int o = (r - 9) * 8 + c8, kind = o >= 72, oc = o - 72 * kind, gr = oc / 9, i = oc - 9 * gr;
      vo = OFF_P + (gr * 8 + x) * 896 + (i < 8 ? (3 * kind + u) * 128 + i * 16 : 768 + (4 * kind + u) * 16);
      hv_silu(H, ci, cz);
    } else {
      vo = OFF_Q + b * 768 + u * 256 + ((wave - 1) * 8 + c8) * 16;
    }
    if ((lane & 8) || lane >= 48) vo = OOB;
    PIN(vo);
    asm volatile("s_waitcnt vmcnt(0)" : "+v"(w[0]), "+v"(w[1]), "+v"(w[2]), "+v"(w[3]), "+v"(w[4]), "+v"(w[5]), "+v"(w[6]), "+v"(w[7]), "+v"(w[8]) :: "memory");
    Slab S;
    slab_make(S, w);
    STAMP(1);
    if (!is_mod) {
      lds_wait(&L.cnt[0], 1);
      const v4u hp = *(const v4u*)&L.hs[4 * r8];
      H.za = ((const u16*)L.hs)[64 + r8]; H.zb = H.za << 16;
#pragma unroll
      for (int p = 0; p < 4; ++p) H.p[p] = hp[p];
    }
    STAMP(2);
    float a[8]; slab_dot(a, S, H);
    float e0 = rs16(rs32(a[0], a[6]), rs32(a[3], a[3])), e1 = rs16(rs32(a[1], a[7]), rs32(a[4], a[4])), e2 = rs16(rs32(a[2], a[2]), rs32(a[5], a[5]));
    e0 += dppf<R8>(e0); e1 += dppf<R8>(e1); e2 += dppf<R8>(e2);
    const v4u pv = {asu(e0), asu(e1), asu(e2), tg0};
    __builtin_amdgcn_raw_buffer_store_b128(pv, rws, vo, 0, AUX_SC1);
    STAMP(3);
  }
  const int qr = lane >> 2, qc = lane & 3;
  const unsigned mcopy = OFF_M + x * M_COPY;
  if (r < 9) {
    const rsrc_t rw = mk(wo + (size_t)(512 * x + 128 * wave) * 576 + 64 * r, 127 * 1152 + 128);
    v4u wo_w[16];
    if (WTRIG) lds_wait(&L.cnt[5], WTRIG);
    else if (LATEQ) lds_wait(&L.cnt[4], 1);
#pragma unroll
    for (int t = 0; t < 16; ++t) wo_w[t] = LD128(rw, (16 * r8 + t) * 1152 + c8 * 16, AUX_NT);
    unsigned P[8][8];
#pragma unroll
    for (int p = 0; p < 8; ++p) {
      interleave(P[p], wo_w[2 * p], wo_w[2 * p + 1]);
#pragma unroll
      for (int d = 0; d < 8; ++d) PIN(P[p][d]);
    }
    lds_wait(&L.cnt[4], 1);
    const unsigned tg = __builtin_amdgcn_readfirstlane(L.cnt[7]);
    lds_wait(&L.cnt[3], 9);
    STAMP(5);
    const int rec = (4 * x + wave) * 8 + (lane >> 5);
    v4u o[4], ml;
    _Pragma("unroll 1") for (int it = 0; it < GCAP; ++it) {
      ml = LD128(rws, OFF_AM + (rec + 2 * qc) * 16, AUX_SC1);
#pragma unroll
      for (int j = 0; j < 4; ++j) o[j] = LD128(rws, OFF_AO + (rec + 2 * j) * 512 + (lane & 31) * 16, AUX_SC1);
      const unsigned bad = (stale(o[0], tg) | stale(o[1], tg)) | (stale(o[2], tg) | stale(o[3], tg)) | stale(ml, tg);
      if (all_true((bad & 0xff) == 0)) break;
      MEMFENCE();
    }
    STAMP(6);
    const float mi = hi16(ml[0]);
    float M = fmaxf(mi, dppf<X1>(mi)); M = fmaxf(M, dppf<X2>(M)); M = mx32(M);
    const float e = ex2((mi - M) * LOG2E);
    float Ls = e * asf(ml[1]); Ls += dppf<X1>(Ls); Ls += dppf<X2>(Ls); Ls = rs32(Ls, Ls);
    const float e0 = dppf<0x00>(e), e1 = dppf<0x55>(e), e2 = dppf<0xAA>(e), e3 = dppf<0xFF>(e);
    float oo[4];
#pragma unroll
    for (int d = 0; d < 4; ++d) oo[d] = fmaf(e3, asf(o[3][d]), fmaf(e2, asf(o[2][d]), fmaf(e1, asf(o[1][d]), e0 * asf(o[0][d]))));
    const float rl = rcp(Ls);
    L.ob[wave][(lane & 31) * 2 + (lane >> 5)] = pack2(rs32(oo[0], oo[2]) * rl, rs32(oo[1], oo[3]) * rl);
    WAIT_LGKM0();
    const v4u oa = *(const v4u*)&L.ob[wave][r8 * 8], ob = *(const v4u*)&L.ob[wave][r8 * 8 + 4];
    float acc[8] = {0.f, 0.f, 0.f, 0.f, 0.f, 0.f, 0.f, 0.f};
#pragma unroll
    for (int p = 0; p < 8; ++p) {
      const unsigned hp = p < 4 ? oa[p & 3] : ob[p & 3];
#pragma unroll
      for (int d = 0; d < 8; ++d) acc[d] = dot2(P[p][d], hp, acc[d]);
    }
    float y[4];
#pragma unroll
    for (int j = 0; j < 4; ++j) y[j] = rs32(acc[j], acc[j + 4]);
    float z0 = rs16(y[0], y[2]), z1 = rs16(y[1], y[3]);
    z0 += dppf<R8>(z0); z1 += dppf<R8>(z1);
    if ((lane & 8) == 0) { v2f z = {z0, z1}; *(v2f*)&L.hp[wave][c8 * 8 + (lane >> 4) * 2] = z; }
    wg_barrier();
    if (wave == 0) {
      const int c4 = (lane & 15) * 4;
      const v4f t = (*(const v4f*)&L.hp[0][c4] + *(const v4f*)&L.hp[1][c4]) + (*(const v4f*)&L.hp[2][c4] + *(const v4f*)&L.hp[3][c4]);
      __builtin_amdgcn_raw_buffer_store_b128(seal(t, tg), rws, OFF_B + (lane >> 4) * 18432 + x * 2304 + r * 256 + c4 * 4, 0, AUX_SC1);
      STAMP(7);
    }
  } else if (r < 27) {
    const int f = 18 * x + (r - 9);
    const bool act3 = qc < 3;
    const int un4 = 144 * wave + (qc < 2 ? 8 * qr + 4 * qc : 128 + (qr & 12));
    const v2u xi = LD64(mk(xin, 1152), act3 ? un4 * 2 : OOB, 0);
    float xv[4], ga[4], sb[4], cb[4];
    lds_wait(&L.cnt[4], 1);
    const unsigned tg = __builtin_amdgcn_readfirstlane(L.cnt[7]);
    {
      for (int i = 0; i < MDEL; ++i) __builtin_amdgcn_s_sleep(127);
      const unsigned vm = act3 ? mcopy + un4 * 4 : OOB;
      v4u g[3];
      _Pragma("unroll 1") for (int it = 0; it < GCAP; ++it) {
        unsigned bad = 0;
#pragma unroll
        for (int j = 0; j < 3; ++j) g[j] = LD128(rws, vm + j * 2304, AUX_SC1);
#pragma unroll
        for (int j = 0; j < 3; ++j) bad |= (g[j][1] ^ tg) | (g[j][3] ^ tg);
        if (all_true(bad == 0 || !act3)) break;
        __builtin_amdgcn_s_sleep(32);
        MEMFENCE();
      }
      STAMP(4);
#pragma unroll
      for (int e = 0; e < 4; ++e) {
        const bool on = qc < 2 || (qc == 2 && (qr & 3) == e);
        const float a = (e & 1) ? hi16(xi[e >> 1]) : lo16(xi[e >> 1]), g0 = (e & 1) ? hi16(g[0][e & 2]) : lo16(g[0][e & 2]);
        const float g1 = (e & 1) ? hi16(g[1][e & 2]) : lo16(g[1][e & 2]), g2 = (e & 1) ? hi16(g[2][e & 2]) : lo16(g[2][e & 2]);
        xv[e] = on ? a : 0.f; ga[e] = on ? g0 : 0.f; sb[e] = on ? g1 : 0.f; cb[e] = on ? rbf(1.f + g2) : 0.f;
        PIN(xv[e]); PIN(ga[e]); PIN(sb[e]); PIN(cb[e]);
      }
    }
    __builtin_amdgcn_s_setprio(0);
    const rsrc_t rg = mk(wg + f * 32, 575 * 9216 + 64), ru = mk(wu + f * 32, 575 * 9216 + 64);
    const rsrc_t rd = mk(wd + (size_t)(32 * f + 8 * wave) * 576, 8 * 1152);
    v4u w1[9], w2[9], w3[8]; unsigned w4[8];
    SB(); lds_wait(&L.cnt[DTRIG > 8 ? 3 : 5], DTRIG > 8 ? DTRIG - 8 : DTRIG); if (DDEL) __builtin_amdgcn_s_sleep(DDEL); STAMP(5); SB();
#pragma unroll
    for (int t = 0; t < 8; ++t) { w3[t] = LD128(rd, t * 1152 + lane * 16, AUX_NT); w4[t] = LD16(rd, t * 1152 + 1024 + lane * 2, AUX_NT); }
    SB(); lds_wait(&L.cnt[3], GTRIG);
    if (GDEL) __builtin_amdgcn_s_sleep(GDEL);
    STAMP(9); SB();
    const unsigned vg = (144 * wave + 8 * qr) * 9216 + qc * 16, vz = (144 * wave + 128 + qr) * 9216 + qc * 16;
#pragma unroll
    for (int t = 0; t < 9; ++t) w1[t] = LD128(rg, t < 8 ? vg + t * 9216 : vz, AUX_NT);
#pragma unroll
    for (int t = 0; t < 9; ++t) w2[t] = LD128(ru, t < 8 ? vg + t * 9216 : vz, AUX_NT);
    SB();
    unsigned Dp[4][8], Dt[4];
#pragma unroll
    for (int p = 0; p < 4; ++p) {
      interleave(Dp[p], w3[2 * p], w3[2 * p + 1]); Dt[p] = w4[2 * p] | (w4[2 * p + 1] << 16);
#pragma unroll
      for (int d = 0; d < 8; ++d) PIN(Dp[p][d]);
      PIN(Dt[p]);
    }
    Slab G, U; slab_make(G, w1); slab_make(U, w2);
    STAMP(6);
    const unsigned vb = act3 ? OFF_B + (x & 3) * 18432 + un4 * 4 : OOB;
    v4u yb[8];
    _Pragma("unroll 1") for (int it = 0; it < GCAP; ++it) {
#pragma unroll
      for (int j = 0; j < 8; ++j) yb[j] = LD128(rws, vb + j * 2304, AUX_SC1);
      unsigned bad = 0;
#pragma unroll
      for (int j = 0; j < 8; ++j) bad |= stale(yb[j], tg);
      if (all_true((bad & 0xff) == 0 || !act3)) break;
      MEMFENCE();
    }
    STAMP(7);
    float xr[4];
#pragma unroll
    for (int j = 0; j < 4; ++j) {
      const float t = ((asf(yb[0][j]) + asf(yb[1][j])) + (asf(yb[2][j]) + asf(yb[3][j]))) + ((asf(yb[4][j]) + asf(yb[5][j])) + (asf(yb[6][j]) + asf(yb[7][j])));
      xr[j] = rbf(xv[j] + rbf(ga[j] * rbf(t)));
    }
    float s = (xr[0] + xr[1]) + (xr[2] + xr[3]), q = (xr[0] * xr[0] + xr[1] * xr[1]) + (xr[2] * xr[2] + xr[3] * xr[3]);
    s = wave_sum(s); q = wave_sum(q);
    if (lane == 0) { v2f t = {s, q}; *(v2f*)&L.st[wave][0] = t; }
    wg_barrier();
    {
      const v4f t0 = *(const v4f*)&L.st[0][0], t1 = *(const v4f*)&L.st[2][0];
      s = (t0.x + t0.z) + (t1.x + t1.z); q = (t0.y + t0.w) + (t1.y + t1.w);
    }
    float m, rsd; ln_stats(s, q, m, rsd);
#pragma unroll
    for (int j = 0; j < 4; ++j) xr[j] = rbf(rbf(rbf(xr[j] - m) * rsd) * cb[j]) + sb[j];
    const unsigned h01 = pack2(xr[0], xr[1]), h23 = pack2(xr[2], xr[3]);
    Hv H;
    H.p[0] = dppu<0x00>(h01); H.p[1] = dppu<0x00>(h23); H.p[2] = dppu<0x55>(h01); H.p[3] = dppu<0x55>(h23);
    { const unsigned hz = dppu<0xAA>(h01 | h23); H.zb = (hz | (hz << 16)) & 0xffff0000u; H.za = H.zb >> 16; }
    float ag[8], au[8]; slab_dot(ag, G, H); slab_dot(au, U, H);
    float y[8];
#pragma unroll
    for (int j = 0; j < 8; ++j) y[j] = rs32(ag[j], au[j]);
    v4f z;
#pragma unroll
    for (int j = 0; j < 4; ++j) { float t = rs16(y[j], y[j + 4]); t += dppf<R4>(t); t += dppf<R8>(t); z[j] = t; }
    if ((lane & 12) == 0) *(v4f*)&L.hp[wave][(lane >> 5) * 32 + qc * 8 + ((lane >> 4) & 1) * 4] = z;
    wg_barrier();
    const int un = 8 * wave + (lane & 7);
    const float g = rbf((L.hp[0][un] + L.hp[1][un]) + (L.hp[2][un] + L.hp[3][un]));
    const float up = rbf((L.hp[0][32 + un] + L.hp[1][32 + un]) + (L.hp[2][32 + un] + L.hp[3][32 + un]));
    const float av = rbf(silu(g)) * up;
    const unsigned ap = pack2(av, dppf<X1>(av));
    float d8[8] = {0.f, 0.f, 0.f, 0.f, 0.f, 0.f, 0.f, 0.f}, dt = 0.f;
#pragma unroll
    for (int p = 0; p < 4; ++p) {
      const unsigned a2 = __builtin_amdgcn_readlane(ap, 2 * p);
#pragma unroll
      for (int d = 0; d < 8; ++d) d8[d] = dot2(Dp[p][d], a2, d8[d]);
      dt = dot2(Dt[p], a2, dt);
    }
    { v4f t0 = {d8[0], d8[1], d8[2], d8[3]}, t1 = {d8[4], d8[5], d8[6], d8[7]};
      *(v4f*)&L.dn[wave][lane * 8] = t0; *(v4f*)&L.dn[wave][lane * 8 + 4] = t1; L.dn[wave][512 + lane] = dt; }
    wg_barrier();
    {
      const int c4 = 144 * wave + (lane < 36 ? lane : 0) * 4;
      const v4f t = (*(const v4f*)&L.dn[0][c4] + *(const v4f*)&L.dn[1][c4]) + (*(const v4f*)&L.dn[2][c4] + *(const v4f*)&L.dn[3][c4]);
      __builtin_amdgcn_raw_buffer_store_b128(seal(t, tg), rws, lane < 36 ? OFF_C + f * 2304 + c4 * 4 : OOB, 0, AUX_SC1);
    }
    STAMP(8);
  } else {
    const int j = (r - 27) * 8 + x;
    if (j >= 36) { FLUSH(); return; }
    unsigned tg;
    {
      const rsrc_t rw = mk(wmod + 144 * wave * 3456 + 1152 + 64 * j, 143 * 6912 + 128), rin = mk(cin + 144 * wave, 288);
      if (LATEQ) lds_wait(&L.cnt[4], 1);
      v4u ci[2], w[2][9]; unsigned cz[2];
#pragma unroll
      for (int u = 0; u < 2; ++u) { ci[u] = LD128(rin, u * 144 + r8 * 16, 0); cz[u] = LD16(rin, u * 144 + 128 + r8 * 2, 0); }
#pragma unroll
      for (int u = 0; u < 2; ++u)
#pragma unroll
        for (int t = 0; t < 9; ++t) w[u][t] = LD128(rw, (72 * u + (t < 8 ? 8 * r8 + t : 64 + r8)) * 6912 + c8 * 16, AUX_NT);
      SB();
      v4f y = {0.f, 0.f, 0.f, 0.f};
#pragma unroll
      for (int u = 0; u < 2; ++u) {
        Hv H; Slab S; float acc[8];
        hv_silu(H, ci[u], cz[u]); slab_make(S, w[u]); slab_dot(acc, S, H);
        y += fold8(acc);
      }
      if ((lane & 24) == 0) *(v4f*)&L.hp[wave][c8 * 8 + (lane >> 5) * 4] = y;
      WAIT_LGKM0();
      if (lane == 0) __hip_atomic_fetch_add(&L.cnt[1], 1u, __ATOMIC_RELAXED, __HIP_MEMORY_SCOPE_WORKGROUP);
      if (wave != 0) { FLUSH(); return; }
      lds_wait(&L.cnt[1], 4);
      tg = __builtin_amdgcn_readfirstlane(L.cnt[7]);
      const int c4 = (lane & 15) * 4;
      const v4f t = (*(const v4f*)&L.hp[0][c4] + *(const v4f*)&L.hp[1][c4]) + (*(const v4f*)&L.hp[2][c4] + *(const v4f*)&L.hp[3][c4]);
      const v4u g = {pack2(t.x, t.y), tg, pack2(t.z, t.w), tg};
#pragma unroll
      for (int u = 0; u < 2; ++u)
        __builtin_amdgcn_raw_buffer_store_b128(g, rws, OFF_M + (4 * u + (lane >> 4)) * M_COPY + j * 256 + c4 * 4, 0, AUX_SC1);
      STAMP(4);
    }
    const int col = 16 * j + 4 * qc + (lane >> 4);
    const float xv = lo16(LD16(mk(xin, 1152), col * 2, 0));
    __builtin_amdgcn_s_sleep(127);
    v2u ga, gb;
    _Pragma("unroll 1") for (int it = 0; it < GCAP; ++it) {
      ga = LD64(rws, mcopy + (col >> 1) * 8, AUX_SC1); gb = LD64(rws, mcopy + 3 * 2304 + (col >> 1) * 8, AUX_SC1);
      unsigned bad = (ga[1] ^ tg) | (gb[1] ^ tg);
      if (all_true(bad == 0)) break;
      __builtin_amdgcn_s_sleep(32);
      MEMFENCE();
    }
    const float gav = lo16(ga[0] >> ((col & 1) * 16)), gbv = lo16(gb[0] >> ((col & 1) * 16));
    lds_wait(&L.cnt[3], 9);
    STAMP(5);
    v4u yb;
    _Pragma("unroll 1") for (int it = 0; it < GCAP; ++it) {
      yb = LD128(rws, lane < 32 ? OFF_B + (x & 3) * 18432 + qr * 2304 + j * 64 + qc * 16 : OOB, AUX_SC1);
      if (all_true((stale(yb, tg) & 0xff) == 0 || lane >= 32)) break;
      MEMFENCE();
    }
    float ys = rs16(rs32(asf(yb[0]), asf(yb[2])), rs32(asf(yb[1]), asf(yb[3])));
    ys += dppf<R4>(ys); ys += dppf<R8>(ys);
    float x1 = rbf(xv + rbf(gav * rbf(ys)));
    PIN(x1);
    STAMP(6);
    v4u cc[9];
    _Pragma("unroll 1") for (int it = 0; it < GCAP; ++it) {
#pragma unroll
      for (int t = 0; t < 9; ++t) cc[t] = LD128(rws, OFF_C + (16 * t + qr) * 2304 + j * 64 + qc * 16, AUX_SC1);
      unsigned bad = 0;
#pragma unroll
      for (int t = 0; t < 9; ++t) bad |= stale(cc[t], tg);
      if (all_true((bad & 0xff) == 0)) break;
      MEMFENCE();
    }
    STAMP(7);
    float a[4];
#pragma unroll
    for (int e = 0; e < 4; ++e)
      a[e] = (((asf(cc[0][e]) + asf(cc[1][e])) + (asf(cc[2][e]) + asf(cc[3][e]))) + ((asf(cc[4][e]) + asf(cc[5][e])) + (asf(cc[6][e]) + asf(cc[7][e])))) + asf(cc[8][e]);
    float t = rs16(rs32(a[0], a[2]), rs32(a[1], a[3]));
    t += dppf<R4>(t); t += dppf<R8>(t);
    const float res = x1 + rbf(gbv * rbf(t));
    __builtin_amdgcn_raw_buffer_store_b16((u16)pack2(res, 0.f), mk(out, 1152), (lane & 12) == 0 ? col * 2 : OOB, 0, 0);
    if (j == 0) __builtin_amdgcn_raw_buffer_store_b32(tg, rws, lane == 0 ? 0u : OOB, 0, AUX_SC1);
    STAMP(8);
  }
  FLUSH();
}

extern "C" void kernel_launch(void* const* d_in, const int* in_sizes, int n_in, void* d_out, int out_size, void* d_ws,
                              size_t ws_size, hipStream_t stream) {
  const Args a = {(const u16*)d_in[0], (const u16*)d_in[3], (const u16*)d_in[9], (const u16*)d_in[10], (char*)d_ws, (u16*)d_out,
                  (const u16*)d_in[4], (const u16*)d_in[8], (const u16*)d_in[6], (const u16*)d_in[7], (const u16*)d_in[1], (const u16*)d_in[2]};
  fused_block<<<256, 768, 0, stream>>>(a);
}
```

```cpp
#include <hip/hip_runtime.h>
#include <hip/hip_bf16.h>
#include <stdint.h>

typedef unsigned short u16;
typedef unsigned long long u64;
typedef unsigned v4u __attribute__((ext_vector_type(4)));
typedef unsigned v3u __attribute__((ext_vector_type(3)));
typedef unsigned v2u __attribute__((ext_vector_type(2)));
typedef float v2f __attribute__((ext_vector_type(2)));
typedef float v4f __attribute__((ext_vector_type(4)));
typedef __bf16 bf2 __attribute__((ext_vector_type(2)));
typedef __amdgpu_buffer_rsrc_t rsrc_t;

#define TF 4
#define TS 5
#define STAMPS 0
#define WTRIG 0
#define ENDX 0
#define PAT 0
#define DTRIG 17
#define GTRIG 9
#define GDEL 10
#define SLIM 255
#define PTRIG 8
#define PDEL 0
#define PSTEP 1
#define GAPK 0
#define WOE 4
#define DDEL 0
#define ROTX ((head >> 2) * 64)
#define NL 1
#define CPRE 5
#define MDEL 2
#define PD1 30
#define PD2 40
#define LATEQ 1

constexpr int AUX_NT = 2, AUX_SC1 = 16;
constexpr unsigned TAG_STEP = 0x01010101u;
constexpr unsigned OOB = 0x40000000u;
constexpr unsigned OFF_T = 4096;
constexpr unsigned OFF_P = 16384;
constexpr unsigned OFF_Q = OFF_P + 64 * 896;
constexpr unsigned OFF_M = OFF_Q + 256 * 768;
constexpr unsigned M_COPY = 4 * 2304;
constexpr unsigned OFF_AO = OFF_M + 8 * M_COPY;
constexpr unsigned OFF_AM = OFF_AO + 131072;
constexpr unsigned OFF_B = OFF_AM + 4096;
constexpr unsigned OFF_C = OFF_B + 4 * 18432;
constexpr unsigned WS_END = OFF_C + 331776;
constexpr unsigned OFF_STAMP = 1u << 20;
constexpr int GCAP = 1 << 15;
constexpr int LCAP = 1 << 20;
constexpr float LOG2E = 1.4426950408889634f;

__device__ __forceinline__ float asf(unsigned u) { return __builtin_bit_cast(float, u); }
__device__ __forceinline__ unsigned asu(float f) { return __builtin_bit_cast(unsigned, f); }
__device__ __forceinline__ float lo16(unsigned u) { return asf(u << 16); }
__device__ __forceinline__ float hi16(unsigned u) { return asf(u & 0xffff0000u); }
__device__ __forceinline__ float rbf(float f) { __bf16 b = (__bf16)f; return (float)b; }
__device__ __forceinline__ unsigned pack2(float a, float b) { bf2 v = {(__bf16)a, (__bf16)b}; return __builtin_bit_cast(unsigned, v); }
__device__ __forceinline__ float dot2(unsigned a, unsigned b, float c) {
  return __builtin_amdgcn_fdot2_f32_bf16(__builtin_bit_cast(bf2, a), __builtin_bit_cast(bf2, b), c, false);
}
template <int CTRL> __device__ __forceinline__ unsigned dppu(unsigned v) { return __builtin_amdgcn_update_dpp(0u, v, CTRL, 0xf, 0xf, true); }
template <int CTRL> __device__ __forceinline__ float dppf(float v) { return asf(dppu<CTRL>(asu(v))); }
constexpr int X1 = 0xB1, X2 = 0x4E, HM = 0x141, R4 = 0x124, R8 = 0x128;
__device__ __forceinline__ float rs32(float a, float b) {
  auto r = __builtin_amdgcn_permlane32_swap(asu(a), asu(b), false, false);
  unsigned r0 = r[0], r1 = r[1]; return asf(r0) + asf(r1);
}
__device__ __forceinline__ float rs16(float a, float b) {
  auto r = __builtin_amdgcn_permlane16_swap(asu(a), asu(b), false, false);
  unsigned r0 = r[0], r1 = r[1]; return asf(r0) + asf(r1);
}
__device__ __forceinline__ float mx32(float a) {
  auto r = __builtin_amdgcn_permlane32_swap(asu(a), asu(a), false, false);
  unsigned r0 = r[0], r1 = r[1]; return fmaxf(asf(r0), asf(r1));
}
__device__ __forceinline__ float mx16(float a) {
  auto r = __builtin_amdgcn_permlane16_swap(asu(a), asu(a), false, false);
  unsigned r0 = r[0], r1 = r[1]; return fmaxf(asf(r0), asf(r1));
}
__device__ __forceinline__ float wave_sum(float v) {
  v += dppf<X1>(v); v += dppf<X2>(v); v += dppf<R4>(v); v += dppf<R8>(v);
  v = rs16(v, v); v = rs32(v, v); return v;
}
__device__ __forceinline__ float ex2(float x) { return __builtin_amdgcn_exp2f(x); }
__device__ __forceinline__ float rcp(float x) { return __builtin_amdgcn_rcpf(x); }
__device__ __forceinline__ float silu(float g) { return g * rcp(1.f + ex2(-g * LOG2E)); }
__device__ __forceinline__ v4u seal(v4f t, unsigned tg) {
  v4u o;
#pragma unroll
  for (int j = 0; j < 4; ++j) o[j] = ((asu(t[j]) + 0x80u) & ~0xffu) | ((tg >> (8 * j)) & 0xffu);
  return o;
}
__device__ __forceinline__ unsigned unseal(v4u& v, unsigned tg) {
#pragma unroll
  for (int j = 0; j < 4; ++j) v[j] ^= (tg >> (8 * j)) & 0xffu;
  asm volatile("" : "+v"(v));
  return (v[0] | v[1]) | (v[2] | v[3]);
}
__device__ __forceinline__ bool all_true(bool ok) { return __builtin_amdgcn_ballot_w64(!ok) == 0; }
__device__ __forceinline__ rsrc_t mk(const void* p, unsigned n) { return __builtin_amdgcn_make_buffer_rsrc((void*)p, 0, n, 0x00020000); }
#define LD128(r, v, aux) __builtin_amdgcn_raw_buffer_load_b128(r, v, 0, aux)
#define LD96(r, v, aux) __builtin_amdgcn_raw_buffer_load_b96(r, v, 0, aux)
#define LD64(r, v, aux) __builtin_amdgcn_raw_buffer_load_b64(r, v, 0, aux)
#define LD32(r, v, aux) __builtin_amdgcn_raw_buffer_load_b32(r, v, 0, aux)
#define LD16(r, v, aux) __builtin_amdgcn_raw_buffer_load_b16(r, v, 0, aux)
#define SB() __builtin_amdgcn_sched_barrier(0)
#define MEMFENCE() asm volatile("" ::: "memory")
#define WAIT_LGKM0() asm volatile("s_waitcnt lgkmcnt(0)" ::: "memory")
#define WAIT_VM0() asm volatile("s_waitcnt vmcnt(0)" ::: "memory")
#define PIN(x) asm volatile("" : "+v"(x))
__device__ __forceinline__ void wg_barrier() { WAIT_LGKM0(); __builtin_amdgcn_s_barrier(); MEMFENCE(); }

struct __attribute__((aligned(16))) Lds {
  float rec_o[8][128];
  float rec_ml[8][2];
  float dn[4][576];
  float hp[4][64];
  unsigned ob[4][64];
  unsigned qs[64];
  unsigned hs[40];
  unsigned sink[64];
  float st[4][2];
  v4u stg[8][NL ? NL : 1][8][64];
  unsigned cnt[8];
};

__device__ __forceinline__ void lds_wait(const unsigned* w, unsigned target) {
  _Pragma("unroll 1") for (int it = 0; it < LCAP; ++it) {
    unsigned v = __hip_atomic_load(w, __ATOMIC_RELAXED, __HIP_MEMORY_SCOPE_WORKGROUP);
    if (__builtin_amdgcn_readfirstlane(v) >= target) break;
    __builtin_amdgcn_s_sleep(1);
  }
  MEMFENCE();
}

struct Slab { unsigned p[4][8]; unsigned z[4]; };
__device__ __forceinline__ void interleave(unsigned (&o)[8], v4u a, v4u b) {
#pragma unroll
  for (int d = 0; d < 4; ++d) {
    unsigned x = a[d], y = b[d];
    o[2 * d] = __builtin_amdgcn_perm(y, x, 0x05040100u);
    o[2 * d + 1] = __builtin_amdgcn_perm(y, x, 0x07060302u);
  }
}
__device__ __forceinline__ void slab_make(Slab& s, const v4u (&w)[9]) {
#pragma unroll
  for (int p = 0; p < 4; ++p) {
    interleave(s.p[p], w[2 * p], w[2 * p + 1]); s.z[p] = w[8][p];
#pragma unroll
    for (int d = 0; d < 8; ++d) PIN(s.p[p][d]);
    PIN(s.z[p]);
  }
}
struct Hv { unsigned p[4], za, zb; };
__device__ __forceinline__ void hv_silu(Hv& h, v4u a, unsigned z) {
#pragma unroll
  for (int p = 0; p < 4; ++p) h.p[p] = pack2(silu(lo16(a[p])), silu(hi16(a[p])));
  h.za = pack2(silu(lo16(z)), 0.f); h.zb = h.za << 16;
}
__device__ __forceinline__ void slab_dot(float (&acc)[8], const Slab& s, const Hv& h) {
#pragma unroll
  for (int d = 0; d < 4; ++d) { acc[2 * d] = dot2(s.z[d], h.za, 0.f); acc[2 * d + 1] = dot2(s.z[d], h.zb, 0.f); }
#pragma unroll
  for (int p = 0; p < 4; ++p)
#pragma unroll
    for (int d = 0; d < 8; ++d) acc[d] = dot2(s.p[p][d], h.p[p], acc[d]);
}
__device__ __forceinline__ v4f fold8(const float (&acc)[8]) {
  v4f y;
#pragma unroll
  for (int j = 0; j < 4; ++j) { float t = rs32(acc[j], acc[j + 4]); t = rs16(t, t); y[j] = t + dppf<R8>(t); }
  return y;
}
__device__ __forceinline__ void ln_stats(float s, float q, float& m, float& rsd) {
  float mean = s * (1.f / 576.f);
  float var = fmaxf(q * (1.f / 576.f) - mean * mean, 0.f);
  m = rbf(mean);
  float sd = rbf(__builtin_sqrtf(rbf(rbf(var) + 1e-5f)));
  rsd = 1.f / sd;
}
__device__ __forceinline__ float modulate(float t, float scale, float shift) { return rbf(rbf(t * rbf(1.f + scale)) + shift); }

#define PROF() do {} while (0)
#define STAMP(k) do {} while (0)
#define FLUSH() do {} while (0)

struct Args { const u16 *xin, *wq, *cin, *wmod; char* ws; u16* out; const u16 *wo, *wd, *wg, *wu, *Kc, *Vc; };
typedef const Args __attribute__((address_space(4))) * args_t;
__device__ __forceinline__ args_t args_ptr() {
  args_t p = (args_t)__builtin_amdgcn_kernarg_segment_ptr();
  asm volatile("" : "+s"(p));
  return p;
}

__global__ void __launch_bounds__(768) fused_block(Args) {
  __shared__ Lds L;
  const int tid = threadIdx.x, lane = tid & 63, wave = __builtin_amdgcn_readfirstlane(tid >> 6);
  const int b = blockIdx.x, x = b & 7, head = b >> 3, r = (head + 4 * x) & 31;
  STAMP(0);
  const int k = r >> 2;
  const int r8 = lane >> 3, c8 = lane & 7;
  const unsigned sink = (unsigned)(size_t)(__attribute__((address_space(3))) void*)L.sink;

  const bool is_mod = wave == 0, ffn = r >= 9 && r < 27;
  unsigned ep, cz;
  v4u ci, w[9];
  const u16 *xin, *cin, *wmod, *wo, *wd, *wg, *wu; u16* out; char* ws;
  if (wave < 4) {
    __builtin_amdgcn_s_setprio(3);
    const args_t A = args_ptr();
    const u16* wq = A->wq;
    xin = A->xin; cin = A->cin; wmod = A->wmod; ws = A->ws; out = A->out; wo = A->wo; wd = A->wd; wg = A->wg; wu = A->wu;
    if (tid < 8) L.cnt[tid] = 0;
    if (CPRE && wave == 3) {
      u64 pc; unsigned junk;
      asm volatile("s_getpc_b64 %0" : "=s"(pc));
      const v4u rc = {(unsigned)pc & ~255u, (unsigned)(pc >> 32) & 0xffffu, CPRE * 4096u, 0x00020000u};
      asm volatile("s_mov_b32 m0, %2\n\ts_nop 1\n\tbuffer_load_dword %0, %1, 0 offen lds" :: "v"(lane < CPRE ? (lane * 32 + r) * 128 : OOB), "s"(rc), "s"(sink) : "m0");
    }
    unsigned ld = is_mod ? (ffn ? 6912 : 0) : (wave == 3 ? 0 : 8192);
    unsigned moff = 72 * x * ld + (is_mod ? r - 9 : 2 * head + wave - 1) * 128;
    unsigned v0 = 8 * r8 * ld + c8 * 16, v8 = (64 + r8) * ld + c8 * 16, vi = r8 * 16, vz = 128 + r8 * 2;
    unsigned vt = (lane == 0 && wave == 1 && x == 0) ? OFF_T + head * 256 : OOB, t0 = 64 * NL;
    asm volatile("" : "+s"(ld), "+s"(moff));
    asm volatile("" : "+v"(v0), "+v"(v8), "+v"(vz), "+v"(vi), "+v"(vt), "+v"(t0));
    SB();
    asm volatile("" :: "s"(xin), "s"(wq), "s"(wo), "s"(wg), "s"(wu), "s"(wd), "s"(cin), "s"(wmod), "s"(out), "s"(ws));
    const u64 wa = (u64)ws, ia = (u64)cin + 144 * x, ma = (u64)(is_mod ? wmod : wq) + moff;
    const v4u r0 = {(unsigned)wa, (unsigned)(wa >> 32) & 0xffffu, WS_END, 0x00020000u};
    const v4u ri = {(unsigned)ia, (unsigned)(ia >> 32) & 0xffffu, 144u, 0x00020000u};
    const v4u rm = {(unsigned)ma, (unsigned)(ma >> 32) & 0xffffu, 71 * ld + 128, 0x00020000u};
    unsigned so;
#define LDW(n) "buffer_load_dwordx4 %" #n ", %[v0], %[rm], %[so] offen nt\n\ts_add_u32 %[so], %[so], %[ld]\n\t"
    asm volatile("s_cmp_eq_u32 %[ld], 0\n\ts_cbranch_scc1 1f\n\ts_mov_b32 %[so], 0\n\t"
                 "buffer_load_dword %9, off, %[r0], 0\n\tbuffer_load_dwordx4 %10, %[vi], %[ri], 0 offen\n\t"
                 "buffer_load_ushort %11, %[vz], %[ri], 0 offen\n\t" LDW(0) LDW(1) LDW(2) LDW(3) LDW(4) LDW(5) LDW(6) LDW(7)
                 "buffer_load_dwordx4 %8, %[v8], %[rm], 0 offen nt\n\tbuffer_store_dword %[t0], %[vt], %[r0], 0 offen sc1\n"
                 "1:\n\ts_waitcnt lgkmcnt(0)\n\ts_barrier"
                 : "=&v"(w[0]), "=&v"(w[1]), "=&v"(w[2]), "=&v"(w[3]), "=&v"(w[4]), "=&v"(w[5]), "=&v"(w[6]), "=&v"(w[7]), "=&v"(w[8]),
                   "=&v"(ep), "=&v"(ci), "=&v"(cz), [so] "=&s"(so)
                 : [r0] "s"(r0), [ri] "s"(ri), [rm] "s"(rm), [v0] "v"(v0), [v8] "v"(v8), [vi] "v"(vi), [vz] "v"(vz), [ld] "s"(ld),
                   [vt] "v"(vt), [t0] "v"(t0)
                 : "scc", "memory");
  }

  if (wave >= 4) {
    const int s = wave - 4;
    const args_t A = args_ptr();
    const u16 *Kc = A->Kc, *Vc = A->Vc, *wg = A->wg, *wu = A->wu; char* ws = A->ws;
    const rsrc_t rws = mk(ws, WS_END);
    const u64 ka = (u64)(Kc + ((size_t)head << 20)), va = (u64)(Vc + ((size_t)head << 20)), wa = (u64)ws;
    const v4u rk = {(unsigned)ka, (unsigned)(ka >> 32) & 0xffffu, 1u << 21, 0x00020000u};
    const v4u rv = {(unsigned)va, (unsigned)(va >> 32) & 0xffffu, 1u << 21, 0x00020000u};
    const v4u rt = {(unsigned)wa, (unsigned)(wa >> 32) & 0xffffu, OFF_P, 0x00020000u};
    const unsigned vo = lane * 16, vt = lane == 0 ? OFF_T + head * 256 : OOB;
    const unsigned rot = ROTX, p0 = rot + k * 8 + s;
    v4u Kr[3][4], Vr[3][4];
    asm volatile("s_barrier" :: "s"(rk), "s"(rv), "s"(p0), "v"(vo) : "memory");
    STAMP(10);
#define S_(x) #x
#define S(x) S_(x)
#define LD1(n, off, T) "s_waitcnt vmcnt(" S(T) "-1)\n\tbuffer_load_dwordx4 %" #n ", %[vo], %[rs], %[so] offen offset:" #off " nt\n\t"
#define VOS [vo] "v"(vo)
#define CHUNK(c) (((c) & 511) << 12)
#define LD4(T) LD1(0, 0, T) LD1(1, 1024, T) LD1(2, 2048, T) LD1(3, 3072, T)
#define LD4E(T) "s_cmp_ge_i32 %[tn], 256-" S(ENDX) "\n\ts_cbranch_scc1 2f\n\t" LD4(T) "s_branch 1f\n2:\n\t" LD4(40)
#define LL1(off) "s_waitcnt vmcnt(" S(TF) "-1)\n\tbuffer_load_dwordx4 %[vo], %[rs], %[so] offen offset:" #off " nt lds\n\t"
#pragma unroll
    for (int i = 0; i < NL; ++i) {
      const unsigned so = CHUNK(p0 + 64 * i), lb = (unsigned)(size_t)(__attribute__((address_space(3))) void*)L.stg[s][i];
      asm volatile("s_mov_b32 m0, %[lb]\n\t" LL1(0) LL1(1024) LL1(2048) LL1(3072) :: VOS, [rs] "s"(rk), [so] "s"(so), [lb] "s"(lb) : "m0", "memory");
      PROF();
      asm volatile("s_mov_b32 m0, %[lb]\n\t" LL1(0) LL1(1024) LL1(2048) LL1(3072) :: VOS, [rs] "s"(rv), [so] "s"(so), [lb] "s"(lb + 4096) : "m0", "memory");
      PROF();
    }
#pragma unroll
    for (int i = 0; i < 3; ++i) {
      const unsigned so = CHUNK(p0 + 64 * (NL + i));
      asm volatile(LD4(TF) : "=&v"(Kr[i][0]), "=&v"(Kr[i][1]), "=&v"(Kr[i][2]), "=&v"(Kr[i][3]) : VOS, [rs] "s"(rk), [so] "s"(so));
      PROF();
      asm volatile(LD4(TF) : "=&v"(Vr[i][0]), "=&v"(Vr[i][1]), "=&v"(Vr[i][2]), "=&v"(Vr[i][3]) : VOS, [rs] "s"(rv), [so] "s"(so));
      PROF();
      if (i == 0) STAMP(11);
    }
    STAMP(6);
    const int pi = (s & 3) * 41 + lane;
    const unsigned pv = (lane < 41 && pi < 162) ? (18 * r + pi / 9) * 9216 + x * 1152 + (pi % 9) * 128 : OOB;
    const u64 pa = (u64)(s < 4 ? wg : wu);
    const v4u rp = {(unsigned)pa, (unsigned)(pa >> 32) & 0xffffu, 576u * 9216, 0x00020000u};
    lds_wait(&L.cnt[4], 1);
    const v4u qv = *(const v4u*)&L.qs[(lane & 15) * 4];
    const unsigned q0 = qv[0], q1 = qv[1], q2 = qv[2], q3 = qv[3];
    STAMP(7);
    float m = -INFINITY, l = 0.f;
    v2f acc[4] = {{0.f, 0.f}, {0.f, 0.f}, {0.f, 0.f}, {0.f, 0.f}};
    unsigned tk = k * 8 + s - 64 + 64 * NL;
    int rem = 3;
    const int lim = s >= 4 ? SLIM : 255;
#define TICKET "v_mov_b32 %[tk], 0x400\n\ts_cmp_ge_i32 %[tn], %[lim]\n\ts_cbranch_scc1 4f\n\tv_mov_b32 %[tk], 1\n\tbuffer_atomic_add %[tk], %[vt], %[rt], 0 offen sc0 sc1\n4:\n\t"
#define PREF "s_cmp_eq_u32 %[rem], " S(PAT) "\n\ts_cbranch_scc0 3f\n\ts_mov_b32 m0, %[sink]\n\ts_nop 1\n\tbuffer_load_dword %[pv], %[rp], 0 offen nt lds\n\tbuffer_load_dword %[pv], %[rp], 0 offen sc1 lds\n3:"
#define LASTW 0
#define SOFT \
        SB();                                                                                       \
        _Pragma("unroll") for (int t = 0; t < 4; ++t) sc[t] += dppf<X1>(sc[t]);                     \
        SB();                                                                                       \
        _Pragma("unroll") for (int t = 0; t < 4; ++t) sc[t] += dppf<X2>(sc[t]);                     \
        SB();                                                                                       \
        _Pragma("unroll") for (int t = 0; t < 4; ++t) sc[t] += dppf<R4>(sc[t]);                     \
        SB();                                                                                       \
        _Pragma("unroll") for (int t = 0; t < 4; ++t) sc[t] += dppf<R8>(sc[t]);                     \
        SB();                                                                                       \
        _Pragma("unroll") for (int t = 0; t < 4; ++t) sc[t] = rbf(rbf(sc[t]) * 0.08838834764831845f); \
        const float mn = fmaxf(fmaxf(fmaxf(sc[0], sc[1]), fmaxf(sc[2], sc[3])), m);                 \
        const float al = ex2((m - mn) * LOG2E);                                                     \
        m = mn;                                                                                     \
        float p[4];                                                                                 \
        _Pragma("unroll") for (int t = 0; t < 4; ++t) p[t] = ex2((sc[t] - mn) * LOG2E);             \
        l = l * al + ((p[0] + p[1]) + (p[2] + p[3]));                                               \
        _Pragma("unroll") for (int j = 0; j < 4; ++j) acc[j] *= al;
#define STEP(i)                                                                                     \
      {                                                                                             \
        float sc[4];                                                                                \
        asm volatile("" : "+v"(Kr[i][0]), "+v"(Kr[i][1]), "+v"(Kr[i][2]), "+v"(Kr[i][3]));          \
        _Pragma("unroll") for (int t = 0; t < 4; ++t) {                                             \
          const v4u kk = Kr[i][t];                                                                  \
          sc[t] = dot2(kk[3], q3, dot2(kk[2], q2, dot2(kk[1], q1, dot2(kk[0], q0, 0.f))));          \
        }                                                                                           \
        asm volatile("s_waitcnt vmcnt(4)" : "+v"(tk), "+v"(sc[0]), "+v"(sc[1]), "+v"(sc[2]), "+v"(sc[3])); \
        const int tn = __builtin_amdgcn_readfirstlane(tk);                                          \
        rem = tn < 256 ? 3 : rem - 1;                                                               \
        const unsigned so = CHUNK(tn + 256 + rot);                                         \
        asm volatile("s_cmp_eq_u32 %[rem], 3\n\ts_cbranch_scc0 1f\n\ts_nop 1\n\t" LD4E(TS) "1:"      \
                     : "+v"(Kr[i][0]), "+v"(Kr[i][1]), "+v"(Kr[i][2]), "+v"(Kr[i][3])               \
                     : VOS, [rs] "s"(rk), [so] "s"(so), [rem] "s"(rem), [tn] "s"(tn) : "scc"); \
        if (rem == 3) PROF();                                                                       \
        if (rem == 2) {                                                                             \
          if (lane == 0) __hip_atomic_fetch_add(&L.cnt[5], 1u, __ATOMIC_RELAXED, __HIP_MEMORY_SCOPE_WORKGROUP); \
          __builtin_amdgcn_s_setprio(3);                                                            \
          STAMP(8);                                                                                 \
        }                                                                                           \
        SOFT                                                                                        \
        asm volatile("s_cmp_eq_u32 %[rem], 0\n\ts_cbranch_scc0 1f\n\ts_waitcnt vmcnt(" S(LASTW) ")\n1:\n\t" PREF \
                     : "+v"(Vr[i][0]), "+v"(Vr[i][1]), "+v"(Vr[i][2]), "+v"(Vr[i][3]), "+v"(l),     \
                       "+v"(acc[0]), "+v"(acc[1]), "+v"(acc[2]), "+v"(acc[3])                       \
                     : [rem] "s"(rem), [pv] "v"(pv), [rp] "s"(rp), [sink] "s"(sink) : "scc", "m0");  \
        _Pragma("unroll") for (int t = 0; t < 4; ++t) {                                             \
          const v4u vv = Vr[i][t];                                                                  \
          const v2f pp = {p[t], p[t]};                                                              \
          _Pragma("unroll") for (int j = 0; j < 4; ++j) {                                           \
            const v2f e = {lo16(vv[j]), hi16(vv[j])};                                               \
            acc[j] = __builtin_elementwise_fma(pp, e, acc[j]);                                      \
          }                                                                                         \
        }                                                                                           \
        asm volatile("s_cmp_eq_u32 %[rem], 3\n\ts_cbranch_scc0 1f\n\t" TICKET LD4E(TS) "1:"          \
                     : "+v"(Vr[i][0]), "+v"(Vr[i][1]), "+v"(Vr[i][2]), "+v"(Vr[i][3]), [tk] "+v"(tk), \
                       "+v"(acc[0]), "+v"(acc[1]), "+v"(acc[2]), "+v"(acc[3])                       \
                     : VOS, [rs] "s"(rv), [so] "s"(so), [rem] "s"(rem), [vt] "v"(vt), [rt] "s"(rt), [tn] "s"(tn), [lim] "s"(lim) : "scc", "memory"); \
        if (rem == 3 || rem == 0) PROF(); \
        if (rem == 0) break;                                                                        \
      }
    _Pragma("unroll 1") for (int c = 0; c < NL; ++c) {
      float sc[4];
#pragma unroll
      for (int t = 0; t < 4; ++t) {
        const v4u kk = L.stg[s][c][t][lane];
        sc[t] = dot2(kk[3], q3, dot2(kk[2], q2, dot2(kk[1], q1, dot2(kk[0], q0, 0.f))));
      }
      SOFT
      SB();
#pragma unroll
      for (int t = 0; t < 4; ++t) {
        const v4u vv = L.stg[s][c][4 + t][lane];
        const v2f pp = {p[t], p[t]};
#pragma unroll
        for (int j = 0; j < 4; ++j) { const v2f e = {lo16(vv[j]), hi16(vv[j])}; acc[j] = __builtin_elementwise_fma(pp, e, acc[j]); }
      }
    }
    SB();
    _Pragma("unroll 1") for (int it = 0; it < 200; ++it) { STEP(0) STEP(1) STEP(2) }
#undef STEP
    STAMP(4);
    const float M = mx32(mx16(m));
    const float w = ex2((m - M) * LOG2E);
    l *= w; l = rs16(l, l); l = rs32(l, l);
    float a[8];
#pragma unroll
    for (int j = 0; j < 4; ++j) { a[2 * j] = acc[j].x * w; a[2 * j + 1] = acc[j].y * w; }
    float y[4];
#pragma unroll
    for (int j = 0; j < 4; ++j) y[j] = rs32(a[j], a[j + 4]);
    v2f z = {rs16(y[0], y[2]), rs16(y[1], y[3])};
    *(v2f*)&L.rec_o[s][(lane & 15) * 8 + (lane >> 4) * 2] = z;
    if (lane == 0) { v2f ml = {M, l}; *(v2f*)&L.rec_ml[s][0] = ml; }
    WAIT_LGKM0();
    unsigned old = 0;
    if (lane == 0) old = __hip_atomic_fetch_add(&L.cnt[3], 1u, __ATOMIC_RELAXED, __HIP_MEMORY_SCOPE_WORKGROUP);
    old = __builtin_amdgcn_readfirstlane(old);
    MEMFENCE();
    if (old == 7) {
      float ms[8], ls[8];
      const unsigned tgs = L.cnt[7];
#pragma unroll
      for (int j = 0; j < 4; ++j) { v4f t = *(const v4f*)&L.rec_ml[2 * j][0]; ms[2 * j] = t.x; ls[2 * j] = t.y; ms[2 * j + 1] = t.z; ls[2 * j + 1] = t.w; }
      v4f o[8];
#pragma unroll
      for (int j = 0; j < 8; ++j) o[j] = *(const v4f*)&L.rec_o[j][(lane & 31) * 4];
      const float MM = fmaxf(fmaxf(fmaxf(ms[0], ms[1]), fmaxf(ms[2], ms[3])), fmaxf(fmaxf(ms[4], ms[5]), fmaxf(ms[6], ms[7])));
      float LL = 0.f; v4f oo = {0.f, 0.f, 0.f, 0.f};
#pragma unroll
      for (int j = 0; j < 8; ++j) { const float e = ex2((ms[j] - MM) * LOG2E); LL += e * ls[j]; oo += o[j] * e; }
      if (lane >= 32) { oo.x = MM; oo.y = LL; oo.z = 0.f; oo.w = 0.f; }
      const v4u pv = seal(oo, tgs);
      const unsigned off = lane < 32 ? OFF_AO + b * 512 + lane * 16 : (lane == 32 ? OFF_AM + b * 16 : OOB);
      __builtin_amdgcn_raw_buffer_store_b128(pv, rws, off, 0, AUX_SC1);
      if (lane == 0) __hip_atomic_fetch_add(&L.cnt[3], 1u, __ATOMIC_RELAXED, __HIP_MEMORY_SCOPE_WORKGROUP);
      STAMP(5);
    }
    if (PAT == -2 && s >= 4) {
      __builtin_amdgcn_s_setprio(0);
      const u64 ga = (u64)wg, ua = (u64)wu;
      const v4u rg = {(unsigned)ga, (unsigned)(ga >> 32) & 0xffffu, 576u * 9216, 0x00020000u};
      const v4u ru = {(unsigned)ua, (unsigned)(ua >> 32) & 0xffffu, 576u * 9216, 0x00020000u};
      lds_wait(&L.cnt[PTRIG > 8 ? 3 : 5], (PTRIG > 8 ? PTRIG - 8 : PTRIG) - PSTEP * (7 - s) - (r < 9 ? WOE : 0));
      if (PDEL) __builtin_amdgcn_s_sleep(PDEL);
      STAMP(9);
      asm volatile("s_mov_b32 m0, %2\n\ts_nop 1\n\tbuffer_load_dword %0, %1, 0 offen nt lds\n\tbuffer_load_dword %0, %1, 0 offen sc1 lds\n\ts_waitcnt vmcnt(0)"
                   :: "v"(pv), "s"(rg), "s"(sink) : "m0");
      asm volatile("s_mov_b32 m0, %2\n\ts_nop 1\n\tbuffer_load_dword %0, %1, 0 offen nt lds\n\tbuffer_load_dword %0, %1, 0 offen sc1 lds"
                   :: "v"(pv), "s"(ru), "s"(sink) : "m0");
    }
    WAIT_VM0();
    FLUSH();
    return;
  }

  const rsrc_t rws = mk(ws, WS_END);
  if (wave == 3) {
    const int hrow = (lane & 8) ? 64 + 3 * (lane & 3) + ((lane & 16) ? 2 : lane >> 5) : 8 * c8 + ((lane >> 3) & 6);
    const rsrc_t rin = mk(xin, 1152);
    const unsigned e3p = LD32(rws, 0, 0);
    const v4u xa = LD128(rin, lane * 16, 0), xb = LD128(rin, lane * 16 + 1024, 0);
    const unsigned xp = LD32(rin, (72 * x + (hrow & ~1)) * 2, 0);
    const unsigned tg = __builtin_amdgcn_readfirstlane(e3p) + TAG_STEP;
    float s = 0.f, q = 0.f;
#pragma unroll
    for (int d = 0; d < 4; ++d) {
      float e0 = lo16(xa[d]), e1 = hi16(xa[d]), e2 = lo16(xb[d]), e3 = hi16(xb[d]);
      s += (e0 + e1) + (e2 + e3); q += (e0 * e0 + e1 * e1) + (e2 * e2 + e3 * e3);
    }
    s = wave_sum(s); q = wave_sum(q);
    float m, rsd; ln_stats(s, q, m, rsd);
    float n0 = rbf(rbf(lo16(xp >> ((hrow & 1) * 16)) - m) * rsd), n1 = rbf(rbf(hi16(xp) - m) * rsd);
    PIN(n0); PIN(n1);
    if (PD1) __builtin_amdgcn_s_sleep(PD1);
    STAMP(1);
    const unsigned vp = OFF_P + (x * 8 + r8) * 896 + c8 * 16;
    v4u g[7];
    _Pragma("unroll 1") for (int it = 0; it < GCAP; ++it) {
      unsigned bad = 0;
#pragma unroll
      for (int t = 0; t < 6; ++t) g[t] = LD128(rws, vp + t * 128, AUX_SC1);
      g[6] = LD128(rws, vp + ((lane & 3) == 3 ? 752 : 768), AUX_SC1);
#pragma unroll
      for (int t = 0; t < 7; ++t) bad |= g[t][3] ^ tg;
      if (all_true(bad == 0)) break;
      MEMFENCE();
    }
    STAMP(2);
#define G3(t, j) asf(g[(t) + (j) / 3][(j) % 3])
    float sa[2], ca[2];
#pragma unroll
    for (int e = 0; e < 2; ++e) {
      sa[e] = rs16(rs32(G3(0, e), G3(0, e + 4)), rs32(G3(0, e + 2), G3(0, e + 6)));
      ca[e] = rs16(rs32(G3(3, e), G3(3, e + 4)), rs32(G3(3, e + 2), G3(3, e + 6)));
      sa[e] += dppf<R8>(sa[e]); ca[e] += dppf<R8>(ca[e]);
    }
    float z = rs16(rs32(G3(6, 0), G3(6, 1)), rs32(G3(6, 2), G3(6, 2)));
    z += dppf<R8>(z);
    const float zc = dppf<R4>(z);
    if (lane & 8) { sa[0] = z; ca[0] = zc; }
    const unsigned hh = pack2(modulate(n0, rbf(ca[0]), rbf(sa[0])), modulate(n1, rbf(ca[1]), rbf(sa[1])));
    if ((lane & 8) == 0) L.hs[hrow >> 1] = hh;
    else if ((lane & 4) == 0) ((u16*)L.hs)[hrow] = (u16)hh;
    WAIT_LGKM0();
    if (lane == 0) __hip_atomic_store(&L.cnt[0], 1u, __ATOMIC_RELAXED, __HIP_MEMORY_SCOPE_WORKGROUP);
    if (PD2) __builtin_amdgcn_s_sleep(PD2);
    STAMP(3);
    const unsigned vq = OFF_Q + (head * 8 + r8) * 768 + c8 * 16;
    _Pragma("unroll 1") for (int it = 0; it < GCAP; ++it) {
      unsigned bad = 0;
#pragma unroll
      for (int t = 0; t < 6; ++t) g[t] = LD128(rws, vq + t * 128, AUX_SC1);
#pragma unroll
      for (int t = 0; t < 6; ++t) bad |= g[t][3] ^ tg;
      if (all_true(bad == 0)) break;
      MEMFENCE();
    }
#define Q3(j) rs32(asf(g[(j) / 3 * 2][(j) % 3]), asf(g[(j) / 3 * 2 + 1][(j) % 3]))
    float qq[4];
#pragma unroll
    for (int e = 0; e < 4; ++e) { const float t = rs16(Q3(e), Q3(e + 4)); qq[e] = t + dppf<R8>(t); }
    const v2u qp = {pack2(qq[0], qq[1]), pack2(qq[2], qq[3])};
    *(v2u*)&L.qs[((lane >> 5) * 8 + c8) * 4 + ((lane >> 3) & 2)] = qp;
    L.cnt[7] = tg;
    WAIT_LGKM0();
    if (lane == 0) __hip_atomic_store(&L.cnt[4], 1u, __ATOMIC_RELAXED, __HIP_MEMORY_SCOPE_WORKGROUP);
    STAMP(4);
  } else if (!is_mod || ffn) {
    asm volatile("s_waitcnt vmcnt(9)" : "+v"(ep), "+v"(ci), "+v"(cz) :: "memory");
    const unsigned tg0 = __builtin_amdgcn_readfirstlane(ep) + TAG_STEP;
    const int u = lane >> 4;
    unsigned vo;
    Hv H;
    if (is_mod) {
      const int o = (r - 9) * 8 + c8, kind = o >= 72, oc = o - 72 * kind, gr = oc / 9, i = oc - 9 * gr;
      vo = OFF_P + (gr * 8 + x) * 896 + (i < 8 ? (3 * kind + u) * 128 + i * 16 : 768 + (4 * kind + u) * 16);
      hv_silu(H, ci, cz);
    } else {
      vo = OFF_Q + b * 768 + u * 256 + ((wave - 1) * 8 + c8) * 16;
    }
    if ((lane & 8) || lane >= 48) vo = OOB;
    PIN(vo);
    asm volatile("s_waitcnt vmcnt(0)" : "+v"(w[0]), "+v"(w[1]), "+v"(w[2]), "+v"(w[3]), "+v"(w[4]), "+v"(w[5]), "+v"(w[6]), "+v"(w[7]), "+v"(w[8]) :: "memory");
    Slab S;
    slab_make(S, w);
    STAMP(1);
    if (!is_mod) {
      lds_wait(&L.cnt[0], 1);
      const v4u hp = *(const v4u*)&L.hs[4 * r8];
      H.za = ((const u16*)L.hs)[64 + r8]; H.zb = H.za << 16;
#pragma unroll
      for (int p = 0; p < 4; ++p) H.p[p] = hp[p];
    }
    STAMP(2);
    float a[8]; slab_dot(a, S, H);
    float e0 = rs16(rs32(a[0], a[6]), rs32(a[3], a[3])), e1 = rs16(rs32(a[1], a[7]), rs32(a[4], a[4])), e2 = rs16(rs32(a[2], a[2]), rs32(a[5], a[5]));
    e0 += dppf<R8>(e0); e1 += dppf<R8>(e1); e2 += dppf<R8>(e2);
    const v4u pv = {asu(e0), asu(e1), asu(e2), tg0};
    __builtin_amdgcn_raw_buffer_store_b128(pv, rws, vo, 0, AUX_SC1);
    STAMP(3);
  }
  const int qr = lane >> 2, qc = lane & 3;
  const unsigned mcopy = OFF_M + x * M_COPY;
  if (r < 9) {
    const rsrc_t rw = mk(wo + (size_t)(512 * x + 128 * wave) * 576 + 64 * r, 127 * 1152 + 128);
    v4u wo_w[16];
    if (WTRIG) lds_wait(&L.cnt[5], WTRIG);
    else if (LATEQ) lds_wait(&L.cnt[4], 1);
#pragma unroll
    for (int t = 0; t < 16; ++t) wo_w[t] = LD128(rw, (16 * r8 + t) * 1152 + c8 * 16, AUX_NT);
    unsigned P[8][8];
#pragma unroll
    for (int p = 0; p < 8; ++p) {
      interleave(P[p], wo_w[2 * p], wo_w[2 * p + 1]);
#pragma unroll
      for (int d = 0; d < 8; ++d) PIN(P[p][d]);
    }
    lds_wait(&L.cnt[4], 1);
    const unsigned tg = __builtin_amdgcn_readfirstlane(L.cnt[7]);
    lds_wait(&L.cnt[3], 9);
    STAMP(5);
    const int rec = (4 * x + wave) * 8 + (lane >> 5);
    v4u o[4], ml;
    _Pragma("unroll 1") for (int it = 0; it < GCAP; ++it) {
      ml = LD128(rws, OFF_AM + (rec + 2 * qc) * 16, AUX_SC1);
#pragma unroll
      for (int j = 0; j < 4; ++j) o[j] = LD128(rws, OFF_AO + (rec + 2 * j) * 512 + (lane & 31) * 16, AUX_SC1);
      const unsigned bad = (unseal(o[0], tg) | unseal(o[1], tg)) | (unseal(o[2], tg) | unseal(o[3], tg)) | unseal(ml, tg);
      if (all_true((bad & 0xff) == 0)) break;
      MEMFENCE();
    }
    STAMP(6);
    const float mi = hi16(ml[0]);
    float M = fmaxf(mi, dppf<X1>(mi)); M = fmaxf(M, dppf<X2>(M)); M = mx32(M);
    const float e = ex2((mi - M) * LOG2E);
    float Ls = e * asf(ml[1]); Ls += dppf<X1>(Ls); Ls += dppf<X2>(Ls); Ls = rs32(Ls, Ls);
    const float e0 = dppf<0x00>(e), e1 = dppf<0x55>(e), e2 = dppf<0xAA>(e), e3 = dppf<0xFF>(e);
    float oo[4];
#pragma unroll
    for (int d = 0; d < 4; ++d) oo[d] = fmaf(e3, asf(o[3][d]), fmaf(e2, asf(o[2][d]), fmaf(e1, asf(o[1][d]), e0 * asf(o[0][d]))));
    const float rl = rcp(Ls);
    L.ob[wave][(lane & 31) * 2 + (lane >> 5)] = pack2(rs32(oo[0], oo[2]) * rl, rs32(oo[1], oo[3]) * rl);
    WAIT_LGKM0();
    const v4u oa = *(const v4u*)&L.ob[wave][r8 * 8], ob = *(const v4u*)&L.ob[wave][r8 * 8 + 4];
    float acc[8] = {0.f, 0.f, 0.f, 0.f, 0.f, 0.f, 0.f, 0.f};
#pragma unroll
    for (int p = 0; p < 8; ++p) {
      const unsigned hp = p < 4 ? oa[p & 3] : ob[p & 3];
#pragma unroll
      for (int d = 0; d < 8; ++d) acc[d] = dot2(P[p][d], hp, acc[d]);
    }
    float y[4];
#pragma unroll
    for (int j = 0; j < 4; ++j) y[j] = rs32(acc[j], acc[j + 4]);
    float z0 = rs16(y[0], y[2]), z1 = rs16(y[1], y[3]);
    z0 += dppf<R8>(z0); z1 += dppf<R8>(z1);
    if ((lane & 8) == 0) { v2f z = {z0, z1}; *(v2f*)&L.hp[wave][c8 * 8 + (lane >> 4) * 2] = z; }
    wg_barrier();
    if (wave == 0) {
      const int c4 = (lane & 15) * 4;
      const v4f t = (*(const v4f*)&L.hp[0][c4] + *(const v4f*)&L.hp[1][c4]) + (*(const v4f*)&L.hp[2][c4] + *(const v4f*)&L.hp[3][c4]);
      __builtin_amdgcn_raw_buffer_store_b128(seal(t, tg), rws, OFF_B + (lane >> 4) * 18432 + x * 2304 + r * 256 + c4 * 4, 0, AUX_SC1);
      STAMP(7);
    }
  } else if (r < 27) {
    const int f = 18 * x + (r - 9);
    const bool act3 = qc < 3;
    const int un4 = 144 * wave + (qc < 2 ? 8 * qr + 4 * qc : 128 + (qr & 12));
    const v2u xi = LD64(mk(xin, 1152), act3 ? un4 * 2 : OOB, 0);
    float xv[4], ga[4], sb[4], cb[4];
    lds_wait(&L.cnt[4], 1);
    const unsigned tg = __builtin_amdgcn_readfirstlane(L.cnt[7]);
    {
      for (int i = 0; i < MDEL; ++i) __builtin_amdgcn_s_sleep(127);
      const unsigned vm = act3 ? mcopy + un4 * 4 : OOB;
      v4u g[3];
      _Pragma("unroll 1") for (int it = 0; it < GCAP; ++it) {
        unsigned bad = 0;
#pragma unroll
        for (int j = 0; j < 3; ++j) g[j] = LD128(rws, vm + j * 2304, AUX_SC1);
#pragma unroll
        for (int j = 0; j < 3; ++j) bad |= (g[j][1] ^ tg) | (g[j][3] ^ tg);
        if (all_true(bad == 0 || !act3)) break;
        __builtin_amdgcn_s_sleep(32);
        MEMFENCE();
      }
      STAMP(4);
#pragma unroll
      for (int e = 0; e < 4; ++e) {
        const bool on = qc < 2 || (qc == 2 && (qr & 3) == e);
        const float a = (e & 1) ? hi16(xi[e >> 1]) : lo16(xi[e >> 1]), g0 = (e & 1) ? hi16(g[0][e & 2]) : lo16(g[0][e & 2]);
        const float g1 = (e & 1) ? hi16(g[1][e & 2]) : lo16(g[1][e & 2]), g2 = (e & 1) ? hi16(g[2][e & 2]) : lo16(g[2][e & 2]);
        xv[e] = on ? a : 0.f; ga[e] = on ? g0 : 0.f; sb[e] = on ? g1 : 0.f; cb[e] = on ? rbf(1.f + g2) : 0.f;
        PIN(xv[e]); PIN(ga[e]); PIN(sb[e]); PIN(cb[e]);
      }
    }
    __builtin_amdgcn_s_setprio(0);
    const rsrc_t rg = mk(wg + f * 32, 575 * 9216 + 64), ru = mk(wu + f * 32, 575 * 9216 + 64);
    const rsrc_t rd = mk(wd + (size_t)(32 * f + 8 * wave) * 576, 8 * 1152);
    v4u w1[9], w2[9], w3[8]; unsigned w4[8];
    SB(); lds_wait(&L.cnt[DTRIG > 8 ? 3 : 5], DTRIG > 8 ? DTRIG - 8 : DTRIG); if (DDEL) __builtin_amdgcn_s_sleep(DDEL); STAMP(5); SB();
#pragma unroll
    for (int t = 0; t < 8; ++t) { w3[t] = LD128(rd, t * 1152 + lane * 16, AUX_NT); w4[t] = LD16(rd, t * 1152 + 1024 + lane * 2, AUX_NT); }
    SB(); lds_wait(&L.cnt[3], GTRIG);
    if (GDEL) __builtin_amdgcn_s_sleep(GDEL);
    STAMP(9); SB();
    const unsigned vg = (144 * wave + 8 * qr) * 9216 + qc * 16, vz = (144 * wave + 128 + qr) * 9216 + qc * 16;
#pragma unroll
    for (int t = 0; t < 9; ++t) w1[t] = LD128(rg, t < 8 ? vg + t * 9216 : vz, AUX_NT);
#pragma unroll
    for (int t = 0; t < 9; ++t) w2[t] = LD128(ru, t < 8 ? vg + t * 9216 : vz, AUX_NT);
    SB();
    unsigned Dp[4][8], Dt[4];
#pragma unroll
    for (int p = 0; p < 4; ++p) {
      interleave(Dp[p], w3[2 * p], w3[2 * p + 1]); Dt[p] = w4[2 * p] | (w4[2 * p + 1] << 16);
#pragma unroll
      for (int d = 0; d < 8; ++d) PIN(Dp[p][d]);
      PIN(Dt[p]);
    }
    Slab G, U; slab_make(G, w1); slab_make(U, w2);
    STAMP(6);
    const unsigned vb = act3 ? OFF_B + (x & 3) * 18432 + un4 * 4 : OOB;
    v4u yb[8];
    _Pragma("unroll 1") for (int it = 0; it < GCAP; ++it) {
#pragma unroll
      for (int j = 0; j < 8; ++j) yb[j] = LD128(rws, vb + j * 2304, AUX_SC1);
      unsigned bad = 0;
#pragma unroll
      for (int j = 0; j < 8; ++j) bad |= unseal(yb[j], tg);
      if (all_true((bad & 0xff) == 0 || !act3)) break;
      MEMFENCE();
    }
    STAMP(7);
    float xr[4];
#pragma unroll
    for (int j = 0; j < 4; ++j) {
      const float t = ((asf(yb[0][j]) + asf(yb[1][j])) + (asf(yb[2][j]) + asf(yb[3][j]))) + ((asf(yb[4][j]) + asf(yb[5][j])) + (asf(yb[6][j]) + asf(yb[7][j])));
      xr[j] = rbf(xv[j] + rbf(ga[j] * rbf(t)));
    }
    float s = (xr[0] + xr[1]) + (xr[2] + xr[3]), q = (xr[0] * xr[0] + xr[1] * xr[1]) + (xr[2] * xr[2] + xr[3] * xr[3]);
    s = wave_sum(s); q = wave_sum(q);
    if (lane == 0) { v2f t = {s, q}; *(v2f*)&L.st[wave][0] = t; }
    wg_barrier();
    {
      const v4f t0 = *(const v4f*)&L.st[0][0], t1 = *(const v4f*)&L.st[2][0];
      s = (t0.x + t0.z) + (t1.x + t1.z); q = (t0.y + t0.w) + (t1.y + t1.w);
    }
    float m, rsd; ln_stats(s, q, m, rsd);
#pragma unroll
    for (int j = 0; j < 4; ++j) xr[j] = rbf(rbf(rbf(xr[j] - m) * rsd) * cb[j]) + sb[j];
    const unsigned h01 = pack2(xr[0], xr[1]), h23 = pack2(xr[2], xr[3]);
    Hv H;
    H.p[0] = dppu<0x00>(h01); H.p[1] = dppu<0x00>(h23); H.p[2] = dppu<0x55>(h01); H.p[3] = dppu<0x55>(h23);
    { const unsigned hz = dppu<0xAA>(h01 | h23); H.zb = (hz | (hz << 16)) & 0xffff0000u; H.za = H.zb >> 16; }
    float ag[8], au[8]; slab_dot(ag, G, H); slab_dot(au, U, H);
    float y[8];
#pragma unroll
    for (int j = 0; j < 8; ++j) y[j] = rs32(ag[j], au[j]);
    v4f z;
#pragma unroll
    for (int j = 0; j < 4; ++j) { float t = rs16(y[j], y[j + 4]); t += dppf<R4>(t); t += dppf<R8>(t); z[j] = t; }
    if ((lane & 12) == 0) *(v4f*)&L.hp[wave][(lane >> 5) * 32 + qc * 8 + ((lane >> 4) & 1) * 4] = z;
    wg_barrier();
    const int un = 8 * wave + (lane & 7);
    const float g = rbf((L.hp[0][un] + L.hp[1][un]) + (L.hp[2][un] + L.hp[3][un]));
    const float up = rbf((L.hp[0][32 + un] + L.hp[1][32 + un]) + (L.hp[2][32 + un] + L.hp[3][32 + un]));
    const float av = rbf(silu(g)) * up;
    const unsigned ap = pack2(av, dppf<X1>(av));
    float d8[8] = {0.f, 0.f, 0.f, 0.f, 0.f, 0.f, 0.f, 0.f}, dt = 0.f;
#pragma unroll
    for (int p = 0; p < 4; ++p) {
      const unsigned a2 = __builtin_amdgcn_readlane(ap, 2 * p);
#pragma unroll
      for (int d = 0; d < 8; ++d) d8[d] = dot2(Dp[p][d], a2, d8[d]);
      dt = dot2(Dt[p], a2, dt);
    }
    { v4f t0 = {d8[0], d8[1], d8[2], d8[3]}, t1 = {d8[4], d8[5], d8[6], d8[7]};
      *(v4f*)&L.dn[wave][lane * 8] = t0; *(v4f*)&L.dn[wave][lane * 8 + 4] = t1; L.dn[wave][512 + lane] = dt; }
    wg_barrier();
    {
      const int c4 = 144 * wave + (lane < 36 ? lane : 0) * 4;
      const v4f t = (*(const v4f*)&L.dn[0][c4] + *(const v4f*)&L.dn[1][c4]) + (*(const v4f*)&L.dn[2][c4] + *(const v4f*)&L.dn[3][c4]);
      __builtin_amdgcn_raw_buffer_store_b128(seal(t, tg), rws, lane < 36 ? OFF_C + f * 2304 + c4 * 4 : OOB, 0, AUX_SC1);
    }
    STAMP(8);
  } else {
    const int j = (r - 27) * 8 + x;
    if (j >= 36) { FLUSH(); return; }
    unsigned tg;
    {
      const rsrc_t rw = mk(wmod + 144 * wave * 3456 + 1152 + 64 * j, 143 * 6912 + 128), rin = mk(cin + 144 * wave, 288);
      if (LATEQ) lds_wait(&L.cnt[4], 1);
      v4u ci[2], w[2][9]; unsigned cz[2];
#pragma unroll
      for (int u = 0; u < 2; ++u) { ci[u] = LD128(rin, u * 144 + r8 * 16, 0); cz[u] = LD16(rin, u * 144 + 128 + r8 * 2, 0); }
#pragma unroll
      for (int u = 0; u < 2; ++u)
#pragma unroll
        for (int t = 0; t < 9; ++t) w[u][t] = LD128(rw, (72 * u + (t < 8 ? 8 * r8 + t : 64 + r8)) * 6912 + c8 * 16, AUX_NT);
      SB();
      v4f y = {0.f, 0.f, 0.f, 0.f};
#pragma unroll
      for (int u = 0; u < 2; ++u) {
        Hv H; Slab S; float acc[8];
        hv_silu(H, ci[u], cz[u]); slab_make(S, w[u]); slab_dot(acc, S, H);
        y += fold8(acc);
      }
      if ((lane & 24) == 0) *(v4f*)&L.hp[wave][c8 * 8 + (lane >> 5) * 4] = y;
      WAIT_LGKM0();
      if (lane == 0) __hip_atomic_fetch_add(&L.cnt[1], 1u, __ATOMIC_RELAXED, __HIP_MEMORY_SCOPE_WORKGROUP);
      if (wave != 0) { FLUSH(); return; }
      lds_wait(&L.cnt[1], 4);
      tg = __builtin_amdgcn_readfirstlane(L.cnt[7]);
      const int c4 = (lane & 15) * 4;
      const v4f t = (*(const v4f*)&L.hp[0][c4] + *(const v4f*)&L.hp[1][c4]) + (*(const v4f*)&L.hp[2][c4] + *(const v4f*)&L.hp[3][c4]);
      const v4u g = {pack2(t.x, t.y), tg, pack2(t.z, t.w), tg};
#pragma unroll
      for (int u = 0; u < 2; ++u)
        __builtin_amdgcn_raw_buffer_store_b128(g, rws, OFF_M + (4 * u + (lane >> 4)) * M_COPY + j * 256 + c4 * 4, 0, AUX_SC1);
      STAMP(4);
    }
    const int col = 16 * j + 4 * qc + (lane >> 4);
    const float xv = lo16(LD16(mk(xin, 1152), col * 2, 0));
    __builtin_amdgcn_s_sleep(127);
    v2u ga, gb;
    _Pragma("unroll 1") for (int it = 0; it < GCAP; ++it) {
      ga = LD64(rws, mcopy + (col >> 1) * 8, AUX_SC1); gb = LD64(rws, mcopy + 3 * 2304 + (col >> 1) * 8, AUX_SC1);
      unsigned bad = (ga[1] ^ tg) | (gb[1] ^ tg);
      if (all_true(bad == 0)) break;
      __builtin_amdgcn_s_sleep(32);
      MEMFENCE();
    }
    const float gav = lo16(ga[0] >> ((col & 1) * 16)), gbv = lo16(gb[0] >> ((col & 1) * 16));
    lds_wait(&L.cnt[3], 9);
    STAMP(5);
    v4u yb;
    _Pragma("unroll 1") for (int it = 0; it < GCAP; ++it) {
      yb = LD128(rws, lane < 32 ? OFF_B + (x & 3) * 18432 + qr * 2304 + j * 64 + qc * 16 : OOB, AUX_SC1);
      if (all_true((unseal(yb, tg) & 0xff) == 0 || lane >= 32)) break;
      MEMFENCE();
    }
    if (lane >= 32) yb = (v4u){0u, 0u, 0u, 0u};
    float ys = rs16(rs32(asf(yb[0]), asf(yb[2])), rs32(asf(yb[1]), asf(yb[3])));
    ys += dppf<R4>(ys); ys += dppf<R8>(ys);
    float x1 = rbf(xv + rbf(gav * rbf(ys)));
    PIN(x1);
    STAMP(6);
    v4u cc[9];
    _Pragma("unroll 1") for (int it = 0; it < GCAP; ++it) {
#pragma unroll
      for (int t = 0; t < 9; ++t) cc[t] = LD128(rws, OFF_C + (16 * t + qr) * 2304 + j * 64 + qc * 16, AUX_SC1);
      unsigned bad = 0;
#pragma unroll
      for (int t = 0; t < 9; ++t) bad |= unseal(cc[t], tg);
      if (all_true((bad & 0xff) == 0)) break;
      MEMFENCE();
    }
    STAMP(7);
    float a[4];
#pragma unroll
    for (int e = 0; e < 4; ++e)
      a[e] = (((asf(cc[0][e]) + asf(cc[1][e])) + (asf(cc[2][e]) + asf(cc[3][e]))) + ((asf(cc[4][e]) + asf(cc[5][e])) + (asf(cc[6][e]) + asf(cc[7][e])))) + asf(cc[8][e]);
    float t = rs16(rs32(a[0], a[2]), rs32(a[1], a[3]));
    t += dppf<R4>(t); t += dppf<R8>(t);
    const float res = x1 + rbf(gbv * rbf(t));
    __builtin_amdgcn_raw_buffer_store_b16((u16)pack2(res, 0.f), mk(out, 1152), (lane & 12) == 0 ? col * 2 : OOB, 0, 0);
    if (j == 0) __builtin_amdgcn_raw_buffer_store_b32(tg, rws, lane == 0 ? 0u : OOB, 0, AUX_SC1);
    STAMP(8);
  }
  FLUSH();
}

extern "C" void kernel_launch(void* const* d_in, const int* in_sizes, int n_in, void* d_out, int out_size, void* d_ws,
                              size_t ws_size, hipStream_t stream) {
  const Args a = {(const u16*)d_in[0], (const u16*)d_in[3], (const u16*)d_in[9], (const u16*)d_in[10], (char*)d_ws, (u16*)d_out,
                  (const u16*)d_in[4], (const u16*)d_in[8], (const u16*)d_in[6], (const u16*)d_in[7], (const u16*)d_in[1], (const u16*)d_in[2]};
  fused_block<<<256, 768, 0, stream>>>(a);
}
```

```cpp
#include <hip/hip_runtime.h>
#include <hip/hip_bf16.h>
#include <stdint.h>

typedef unsigned short u16;
typedef unsigned long long u64;
typedef unsigned v4u __attribute__((ext_vector_type(4)));
typedef unsigned v3u __attribute__((ext_vector_type(3)));
typedef unsigned v2u __attribute__((ext_vector_type(2)));
typedef float v2f __attribute__((ext_vector_type(2)));
typedef float v4f __attribute__((ext_vector_type(4)));
typedef __bf16 bf2 __attribute__((ext_vector_type(2)));
typedef __amdgpu_buffer_rsrc_t rsrc_t;

#define TF 4
#define TS 5
#define STAMPS 0
#define WTRIG 0
#define ENDX 0
#define PAT 0
#define DTRIG 17
#define GTRIG 9
#define GDEL 0
#define SLIM 255
#define PTRIG 8
#define PDEL 0
#define PSTEP 1
#define GAPK 0
#define WOE 4
#define DDEL 0
#define ROTX ((head >> 2) * 64)
#define NL 1
#define CPRE 6
#define CSZ 21504u
#define PR 32
#define PC 9
#define DE 0
#define DL 8
#define DB 8
#define CN 2
#define CD 8
#define XP 200
#define XD 0
#define XG 400
#define POUT 1
#define MDEL 2
#define PD1 30
#define PD2 40
#define LATEQ 1

constexpr int AUX_NT = 2, AUX_SC1 = 16;
constexpr unsigned TAG_STEP = 0x01010101u;
constexpr unsigned OOB = 0x40000000u;
constexpr unsigned OFF_T = 4096;
constexpr unsigned OFF_P = 16384;
constexpr unsigned OFF_Q = OFF_P + 64 * 896;
constexpr unsigned OFF_M = OFF_Q + 256 * 768;
constexpr unsigned M_COPY = 4 * 2304;
constexpr unsigned OFF_AO = OFF_M + 8 * M_COPY;
constexpr unsigned OFF_B = OFF_AO + 256 * 768;
constexpr unsigned OFF_C = OFF_B + 4 * 18432;
constexpr unsigned WS_END = OFF_C + 144 * 3072;
constexpr unsigned OFF_STAMP = 2u << 20;
constexpr int GCAP = 1 << 15;
constexpr int LCAP = 1 << 20;
constexpr float LOG2E = 1.4426950408889634f;

__device__ __forceinline__ float asf(unsigned u) { return __builtin_bit_cast(float, u); }
__device__ __forceinline__ unsigned asu(float f) { return __builtin_bit_cast(unsigned, f); }
__device__ __forceinline__ float lo16(unsigned u) { return asf(u << 16); }
__device__ __forceinline__ float hi16(unsigned u) { return asf(u & 0xffff0000u); }
__device__ __forceinline__ float rbf(float f) { __bf16 b = (__bf16)f; return (float)b; }
__device__ __forceinline__ unsigned pack2(float a, float b) { bf2 v = {(__bf16)a, (__bf16)b}; return __builtin_bit_cast(unsigned, v); }
__device__ __forceinline__ float dot2(unsigned a, unsigned b, float c) {
  return __builtin_amdgcn_fdot2_f32_bf16(__builtin_bit_cast(bf2, a), __builtin_bit_cast(bf2, b), c, false);
}
template <int CTRL> __device__ __forceinline__ unsigned dppu(unsigned v) { return __builtin_amdgcn_update_dpp(0u, v, CTRL, 0xf, 0xf, true); }
template <int CTRL> __device__ __forceinline__ float dppf(float v) { return asf(dppu<CTRL>(asu(v))); }
constexpr int X1 = 0xB1, X2 = 0x4E, HM = 0x141, R4 = 0x124, R8 = 0x128;
__device__ __forceinline__ float rs32(float a, float b) {
  auto r = __builtin_amdgcn_permlane32_swap(asu(a), asu(b), false, false);
  unsigned r0 = r[0], r1 = r[1]; return asf(r0) + asf(r1);
}
__device__ __forceinline__ float rs16(float a, float b) {
  auto r = __builtin_amdgcn_permlane16_swap(asu(a), asu(b), false, false);
  unsigned r0 = r[0], r1 = r[1]; return asf(r0) + asf(r1);
}
__device__ __forceinline__ float mx32(float a) {
  auto r = __builtin_amdgcn_permlane32_swap(asu(a), asu(a), false, false);
  unsigned r0 = r[0], r1 = r[1]; return fmaxf(asf(r0), asf(r1));
}
__device__ __forceinline__ float mx16(float a) {
  auto r = __builtin_amdgcn_permlane16_swap(asu(a), asu(a), false, false);
  unsigned r0 = r[0], r1 = r[1]; return fmaxf(asf(r0), asf(r1));
}
__device__ __forceinline__ float wave_sum(float v) {
  v += dppf<X1>(v); v += dppf<X2>(v); v += dppf<R4>(v); v += dppf<R8>(v);
  v = rs16(v, v); v = rs32(v, v); return v;
}
__device__ __forceinline__ float ex2(float x) { return __builtin_amdgcn_exp2f(x); }
__device__ __forceinline__ float rcp(float x) { return __builtin_amdgcn_rcpf(x); }
__device__ __forceinline__ float silu(float g) { return g * rcp(1.f + ex2(-g * LOG2E)); }
__device__ __forceinline__ v4u seal(v4f t, unsigned tg) {
  v4u o;
#pragma unroll
  for (int j = 0; j < 4; ++j) o[j] = ((asu(t[j]) + 0x80u) & ~0xffu) | ((tg >> (8 * j)) & 0xffu);
  return o;
}
__device__ __forceinline__ unsigned unseal(v4u& v, unsigned tg) {
#pragma unroll
  for (int j = 0; j < 4; ++j) v[j] ^= (tg >> (8 * j)) & 0xffu;
  asm volatile("" : "+v"(v));
  return (v[0] | v[1]) | (v[2] | v[3]);
}
__device__ __forceinline__ bool all_true(bool ok) { return __builtin_amdgcn_ballot_w64(!ok) == 0; }
__device__ __forceinline__ rsrc_t mk(const void* p, unsigned n) { return __builtin_amdgcn_make_buffer_rsrc((void*)p, 0, n, 0x00020000); }
#define LD128(r, v, aux) __builtin_amdgcn_raw_buffer_load_b128(r, v, 0, aux)
#define LD96(r, v, aux) __builtin_amdgcn_raw_buffer_load_b96(r, v, 0, aux)
#define LD64(r, v, aux) __builtin_amdgcn_raw_buffer_load_b64(r, v, 0, aux)
#define LD32(r, v, aux) __builtin_amdgcn_raw_buffer_load_b32(r, v, 0, aux)
#define LD16(r, v, aux) __builtin_amdgcn_raw_buffer_load_b16(r, v, 0, aux)
#define SB() __builtin_amdgcn_sched_barrier(0)
#define MEMFENCE() asm volatile("" ::: "memory")
#define WAIT_LGKM0() asm volatile("s_waitcnt lgkmcnt(0)" ::: "memory")
#define WAIT_VM0() asm volatile("s_waitcnt vmcnt(0)" ::: "memory")
#define PIN(x) asm volatile("" : "+v"(x))
__device__ __forceinline__ void wg_barrier() { WAIT_LGKM0(); __builtin_amdgcn_s_barrier(); MEMFENCE(); }

struct __attribute__((aligned(16))) Lds {
  float rec_o[8][176];
  float rec_ml[8][2];
  float dn[4][576];
  float hp[4][64];
  unsigned ob[4][96];
  unsigned qs[64];
  unsigned hs[40];
  unsigned sink[64];
  float st[4][2];
  v4u stg[8][NL ? NL : 1][8][64];
  unsigned cnt[8];
};

__device__ __forceinline__ void lds_wait(const unsigned* w, unsigned target) {
  _Pragma("unroll 1") for (int it = 0; it < LCAP; ++it) {
    unsigned v = __hip_atomic_load(w, __ATOMIC_RELAXED, __HIP_MEMORY_SCOPE_WORKGROUP);
    if (__builtin_amdgcn_readfirstlane(v) >= target) break;
    __builtin_amdgcn_s_sleep(1);
  }
  MEMFENCE();
}

__device__ __forceinline__ unsigned now32() { return (unsigned)__builtin_amdgcn_s_memrealtime(); }
__device__ __forceinline__ void wait_until(unsigned t) {
  _Pragma("unroll 1") for (int it = 0; it < 200 && (int)(now32() - t) < 0; ++it) __builtin_amdgcn_s_sleep(1);
}

struct Slab { unsigned p[4][8]; unsigned z[4]; };
__device__ __forceinline__ void interleave(unsigned (&o)[8], v4u a, v4u b) {
#pragma unroll
  for (int d = 0; d < 4; ++d) {
    unsigned x = a[d], y = b[d];
    o[2 * d] = __builtin_amdgcn_perm(y, x, 0x05040100u);
    o[2 * d + 1] = __builtin_amdgcn_perm(y, x, 0x07060302u);
  }
}
__device__ __forceinline__ void slab_make(Slab& s, const v4u (&w)[9]) {
#pragma unroll
  for (int p = 0; p < 4; ++p) {
    interleave(s.p[p], w[2 * p], w[2 * p + 1]); s.z[p] = w[8][p];
#pragma unroll
    for (int d = 0; d < 8; ++d) PIN(s.p[p][d]);
    PIN(s.z[p]);
  }
}
struct Hv { unsigned p[4], za, zb; };
__device__ __forceinline__ void hv_silu(Hv& h, v4u a, unsigned z) {
#pragma unroll
  for (int p = 0; p < 4; ++p) h.p[p] = pack2(silu(lo16(a[p])), silu(hi16(a[p])));
  h.za = pack2(silu(lo16(z)), 0.f); h.zb = h.za << 16;
}
__device__ __forceinline__ void slab_dot(float (&acc)[8], const Slab& s, const Hv& h) {
#pragma unroll
  for (int d = 0; d < 4; ++d) { acc[2 * d] = dot2(s.z[d], h.za, 0.f); acc[2 * d + 1] = dot2(s.z[d], h.zb, 0.f); }
#pragma unroll
  for (int p = 0; p < 4; ++p)
#pragma unroll
    for (int d = 0; d < 8; ++d) acc[d] = dot2(s.p[p][d], h.p[p], acc[d]);
}
__device__ __forceinline__ v4f fold8(const float (&acc)[8]) {
  v4f y;
#pragma unroll
  for (int j = 0; j < 4; ++j) { float t = rs32(acc[j], acc[j + 4]); t = rs16(t, t); y[j] = t + dppf<R8>(t); }
  return y;
}
__device__ __forceinline__ void ln_stats(float s, float q, float& m, float& rsd) {
  float mean = s * (1.f / 576.f);
  float var = fmaxf(q * (1.f / 576.f) - mean * mean, 0.f);
  m = rbf(mean);
  float sd = rbf(__builtin_sqrtf(rbf(rbf(var) + 1e-5f)));
  rsd = 1.f / sd;
}
__device__ __forceinline__ float modulate(float t, float scale, float shift) { return rbf(rbf(t * rbf(1.f + scale)) + shift); }

#define PROF() do {} while (0)
#define STAMP(k) do {} while (0)
#define FLUSH() do {} while (0)

struct Args { const u16 *xin, *wq, *cin, *wmod; char* ws; u16* out; const u16 *wo, *wd, *wg, *wu, *Kc, *Vc; };
typedef const Args __attribute__((address_space(4))) * args_t;
__device__ __forceinline__ args_t args_ptr() {
  args_t p = (args_t)__builtin_amdgcn_kernarg_segment_ptr();
  asm volatile("" : "+s"(p));
  return p;
}

__global__ void __launch_bounds__(768) fused_block(Args) {
  __shared__ Lds L;
  const int tid = threadIdx.x, lane = tid & 63, wave = __builtin_amdgcn_readfirstlane(tid >> 6);
  const int b = blockIdx.x, x = b & 7, head = b >> 3, r = (head + 4 * x) & 31;
  STAMP(0);
  const int k = r >> 2;
  const int r8 = lane >> 3, c8 = lane & 7;
  const unsigned sink = (unsigned)(size_t)(__attribute__((address_space(3))) void*)L.sink;

  const bool is_mod = wave == 0, ffn = r >= 9 && r < 27;
  unsigned ep, cz;
  v4u ci, w[9];
  const u16 *xin, *cin, *wmod, *wo, *wd, *wg, *wu; u16* out; char* ws;
  if (wave < 4) {
    __builtin_amdgcn_s_setprio(3);
    const args_t A = args_ptr();
    const u16* wq = A->wq;
    xin = A->xin; cin = A->cin; wmod = A->wmod; ws = A->ws; out = A->out; wo = A->wo; wd = A->wd; wg = A->wg; wu = A->wu;
    if (tid < 8) L.cnt[tid] = 0;
    if (CPRE && wave == 3) {
      u64 pc; unsigned junk;
      asm volatile("s_getpc_b64 %0" : "=s"(pc));
      const v4u rc = {(unsigned)pc & ~255u, (unsigned)(pc >> 32) & 0xffffu, CSZ, 0x00020000u};
      asm volatile("s_mov_b32 m0, %2\n\ts_nop 1\n\tbuffer_load_dword %0, %1, 0 offen lds" :: "v"(lane < CPRE ? (lane * 32 + r) * 128 : OOB), "s"(rc), "s"(sink) : "m0");
    }
    unsigned ld = is_mod ? (ffn ? 6912 : 0) : (wave == 3 ? 0 : 8192);
    unsigned moff = 72 * x * ld + (is_mod ? r - 9 : 2 * head + wave - 1) * 128;
    unsigned v0 = 8 * r8 * ld + c8 * 16, v8 = (64 + r8) * ld + c8 * 16, vi = r8 * 16, vz = 128 + r8 * 2;
    unsigned vt = (lane == 0 && wave == 1 && x == 0) ? OFF_T + head * 256 : OOB, t0 = 64 * NL;
    asm volatile("" : "+s"(ld), "+s"(moff));
    asm volatile("" : "+v"(v0), "+v"(v8), "+v"(vz), "+v"(vi), "+v"(vt), "+v"(t0));
    SB();
    asm volatile("" :: "s"(xin), "s"(wq), "s"(wo), "s"(wg), "s"(wu), "s"(wd), "s"(cin), "s"(wmod), "s"(out), "s"(ws));
    const u64 wa = (u64)ws, ia = (u64)cin + 144 * x, ma = (u64)(is_mod ? wmod : wq) + moff;
    const v4u r0 = {(unsigned)wa, (unsigned)(wa >> 32) & 0xffffu, WS_END, 0x00020000u};
    const v4u ri = {(unsigned)ia, (unsigned)(ia >> 32) & 0xffffu, 144u, 0x00020000u};
    const v4u rm = {(unsigned)ma, (unsigned)(ma >> 32) & 0xffffu, 71 * ld + 128, 0x00020000u};
    unsigned so;
#define LDW(n) "buffer_load_dwordx4 %" #n ", %[v0], %[rm], %[so] offen nt\n\ts_add_u32 %[so], %[so], %[ld]\n\t"
    asm volatile("s_cmp_eq_u32 %[ld], 0\n\ts_cbranch_scc1 1f\n\ts_mov_b32 %[so], 0\n\t"
                 "buffer_load_dword %9, off, %[r0], 0\n\tbuffer_load_dwordx4 %10, %[vi], %[ri], 0 offen\n\t"
                 "buffer_load_ushort %11, %[vz], %[ri], 0 offen\n\t" LDW(0) LDW(1) LDW(2) LDW(3) LDW(4) LDW(5) LDW(6) LDW(7)
                 "buffer_load_dwordx4 %8, %[v8], %[rm], 0 offen nt\n\tbuffer_store_dword %[t0], %[vt], %[r0], 0 offen sc1\n"
                 "1:\n\ts_waitcnt lgkmcnt(0)\n\ts_barrier"
                 : "=&v"(w[0]), "=&v"(w[1]), "=&v"(w[2]), "=&v"(w[3]), "=&v"(w[4]), "=&v"(w[5]), "=&v"(w[6]), "=&v"(w[7]), "=&v"(w[8]),
                   "=&v"(ep), "=&v"(ci), "=&v"(cz), [so] "=&s"(so)
                 : [r0] "s"(r0), [ri] "s"(ri), [rm] "s"(rm), [v0] "v"(v0), [v8] "v"(v8), [vi] "v"(vi), [vz] "v"(vz), [ld] "s"(ld),
                   [vt] "v"(vt), [t0] "v"(t0)
                 : "scc", "memory");
  }

  if (wave >= 4) {
    const int s = wave - 4;
    const args_t A = args_ptr();
    const u16 *Kc = A->Kc, *Vc = A->Vc, *wg = A->wg, *wu = A->wu; char* ws = A->ws;
    const rsrc_t rws = mk(ws, WS_END);
    const u64 ka = (u64)(Kc + ((size_t)head << 20)), va = (u64)(Vc + ((size_t)head << 20)), wa = (u64)ws;
    const v4u rk = {(unsigned)ka, (unsigned)(ka >> 32) & 0xffffu, 1u << 21, 0x00020000u};
    const v4u rv = {(unsigned)va, (unsigned)(va >> 32) & 0xffffu, 1u << 21, 0x00020000u};
    const v4u rt = {(unsigned)wa, (unsigned)(wa >> 32) & 0xffffu, OFF_P, 0x00020000u};
    const unsigned vo = lane * 16, vt = lane == 0 ? OFF_T + head * 256 : OOB;
    const unsigned rot = ROTX, p0 = rot + k * 8 + s;
    v4u Kr[3][4], Vr[3][4];
    asm volatile("s_barrier" :: "s"(rk), "s"(rv), "s"(p0), "v"(vo) : "memory");
    STAMP(10);
#define S_(x) #x
#define S(x) S_(x)
#define LD1(n, off, T) "s_waitcnt vmcnt(" S(T) "-1)\n\tbuffer_load_dwordx4 %" #n ", %[vo], %[rs], %[so] offen offset:" #off " nt\n\t"
#define VOS [vo] "v"(vo)
#define CHUNK(c) (((c) & 511) << 12)
#define LD4(T) LD1(0, 0, T) LD1(1, 1024, T) LD1(2, 2048, T) LD1(3, 3072, T)
#define LD4E(T) "s_cmp_ge_i32 %[tn], 256-" S(ENDX) "\n\ts_cbranch_scc1 2f\n\t" LD4(T) "s_branch 1f\n2:\n\t" LD4(40)
#define LL1(off) "s_waitcnt vmcnt(" S(TF) "-1)\n\tbuffer_load_dwordx4 %[vo], %[rs], %[so] offen offset:" #off " nt lds\n\t"
#pragma unroll
    for (int i = 0; i < NL; ++i) {
      const unsigned so = CHUNK(p0 + 64 * i), lb = (unsigned)(size_t)(__attribute__((address_space(3))) void*)L.stg[s][i];
      asm volatile("s_mov_b32 m0, %[lb]\n\t" LL1(0) LL1(1024) LL1(2048) LL1(3072) :: VOS, [rs] "s"(rk), [so] "s"(so), [lb] "s"(lb) : "m0", "memory");
      PROF();
      asm volatile("s_mov_b32 m0, %[lb]\n\t" LL1(0) LL1(1024) LL1(2048) LL1(3072) :: VOS, [rs] "s"(rv), [so] "s"(so), [lb] "s"(lb + 4096) : "m0", "memory");
      PROF();
    }
#pragma unroll
    for (int i = 0; i < 3; ++i) {
      const unsigned so = CHUNK(p0 + 64 * (NL + i));
      asm volatile(LD4(TF) : "=&v"(Kr[i][0]), "=&v"(Kr[i][1]), "=&v"(Kr[i][2]), "=&v"(Kr[i][3]) : VOS, [rs] "s"(rk), [so] "s"(so));
      PROF();
      asm volatile(LD4(TF) : "=&v"(Vr[i][0]), "=&v"(Vr[i][1]), "=&v"(Vr[i][2]), "=&v"(Vr[i][3]) : VOS, [rs] "s"(rv), [so] "s"(so));
      PROF();
      if (i == 0) STAMP(11);
    }
    STAMP(6);
    const int pi = (s & 3) * 41 + lane;
    const unsigned pv = (lane < 41 && pi < 162) ? (18 * r + pi / 9) * 9216 + x * 1152 + (pi % 9) * 128 : OOB;
    const int prk = (POUT && r < 9) ? 256 : 256 + PR;
    unsigned tz = 0;
    const u64 pa = (u64)(s < 4 ? wg : wu);
    const v4u rp = {(unsigned)pa, (unsigned)(pa >> 32) & 0xffffu, 576u * 9216, 0x00020000u};
    lds_wait(&L.cnt[4], 1);
    const v4u qv = *(const v4u*)&L.qs[(lane & 15) * 4];
    unsigned q0 = qv[0], q1 = qv[1], q2 = qv[2], q3 = qv[3];
    PIN(q0); PIN(q1); PIN(q2); PIN(q3);
    STAMP(7);
    float m = -INFINITY, l = 0.f;
    v2f acc[4] = {{0.f, 0.f}, {0.f, 0.f}, {0.f, 0.f}, {0.f, 0.f}};
    unsigned tk = k * 8 + s - 64 + 64 * NL;
#define TICKET "v_mov_b32 %[tk], 1\n\tbuffer_atomic_add %[tk], %[vt], %[rt], 0 offen sc0 sc1\n\t"
#define PFM 0
#define PFI "buffer_load_dword %[pv], %[rp], 0 offen nt lds\n\tbuffer_load_dword %[pv], %[rp], 0 offen sc1 lds"
#define PREF0 "s_mov_b32 m0, %[sink]\n\ts_nop 1\n\t" PFI "\n\t"
#define PREF "s_cmp_lt_i32 %[tn], %[prk]\n\ts_cbranch_scc1 7f\n\t" PREF0 "7:\n\t"
#define LASTW "s_waitcnt vmcnt(0)\n\t" PREF
#define DOTS(K) \
        _Pragma("unroll") for (int t = 0; t < 4; ++t)                                               \
          sc[t] = dot2(K[t][3], q3, dot2(K[t][2], q2, dot2(K[t][1], q1, dot2(K[t][0], q0, 0.f))));
#define PV(V) \
        _Pragma("unroll") for (int t = 0; t < 4; ++t) {                                             \
          const v2f pp = {p[t], p[t]};                                                              \
          _Pragma("unroll") for (int j = 0; j < 4; ++j) {                                           \
            const v2f e = {lo16(V[t][j]), hi16(V[t][j])};                                           \
            acc[j] = __builtin_elementwise_fma(pp, e, acc[j]);                                      \
          }                                                                                         \
        }
#define SOFT \
        SB();                                                                                       \
        _Pragma("unroll") for (int t = 0; t < 4; ++t) sc[t] += dppf<X1>(sc[t]);                     \
        SB();                                                                                       \
        _Pragma("unroll") for (int t = 0; t < 4; ++t) sc[t] += dppf<X2>(sc[t]);                     \
        SB();                                                                                       \
        _Pragma("unroll") for (int t = 0; t < 4; ++t) sc[t] += dppf<R4>(sc[t]);                     \
        SB();                                                                                       \
        _Pragma("unroll") for (int t = 0; t < 4; ++t) sc[t] += dppf<R8>(sc[t]);                     \
        SB();                                                                                       \
        _Pragma("unroll") for (int t = 0; t < 4; ++t) sc[t] = rbf(rbf(sc[t]) * 0.08838834764831845f); \
        const float mn = fmaxf(fmaxf(fmaxf(sc[0], sc[1]), fmaxf(sc[2], sc[3])), m);                 \
        const float al = ex2((m - mn) * LOG2E);                                                     \
        m = mn;                                                                                     \
        float p[4];                                                                                 \
        _Pragma("unroll") for (int t = 0; t < 4; ++t) p[t] = ex2((sc[t] - mn) * LOG2E);             \
        l = l * al + ((p[0] + p[1]) + (p[2] + p[3]));                                               \
        _Pragma("unroll") for (int j = 0; j < 4; ++j) acc[j] *= al;
    int nb = 3, tn = 0;
    const unsigned sa = (unsigned)(size_t)(__attribute__((address_space(3))) void*)&L.stg[s][0][0][lane];
    asm volatile("s_waitcnt vmcnt(4)" ::: "memory");
    _Pragma("unroll 1") for (int it = 0; it < 600; ++it) {
      if (tn < 256) {
        float sc[4];
        asm volatile("s_waitcnt vmcnt(5)" : "+v"(Kr[2][0]), "+v"(Kr[2][1]), "+v"(Kr[2][2]), "+v"(Kr[2][3]));
        DOTS(Kr[2])
        SOFT
        asm volatile("s_waitcnt vmcnt(4)" : "+v"(tk), "+v"(p[0]), "+v"(p[1]), "+v"(p[2]), "+v"(p[3]), "+v"(l),
                     "+v"(acc[0]), "+v"(acc[1]), "+v"(acc[2]), "+v"(acc[3]));
        tn = __builtin_amdgcn_readfirstlane(tk);
        const unsigned so = CHUNK(tn + 256 + rot);
        if (STAMPS && tn >= 256) STAMP(8);
        tz = now32();
        asm volatile("s_cmp_lt_i32 %[tn], 0x100\n\ts_cbranch_scc1 5f\n\ts_setprio 3\n\t" LASTW "s_branch 6f\n5:\n\t" LD4E(TS) "1:\n\ts_waitcnt vmcnt(4)\n6:"
                     : "+v"(Kr[2][0]), "+v"(Kr[2][1]), "+v"(Kr[2][2]), "+v"(Kr[2][3]), "+v"(Vr[2][0]), "+v"(Vr[2][1]), "+v"(Vr[2][2]), "+v"(Vr[2][3])
                     : VOS, [rs] "s"(rk), [so] "s"(so), [tn] "s"(tn), [pv] "v"(pv), [rp] "s"(rp), [sink] "s"(sink), [prk] "s"(prk) : "scc", "m0");
        if (tn < 256) PROF();
        PV(Vr[2])
        asm volatile("s_cmp_lt_i32 %[tn], 0x100\n\ts_cbranch_scc0 1f\n\t" TICKET LD4E(TS) "1:"
                     : "+v"(Vr[2][0]), "+v"(Vr[2][1]), "+v"(Vr[2][2]), "+v"(Vr[2][3]), [tk] "+v"(tk),
                       "+v"(acc[0]), "+v"(acc[1]), "+v"(acc[2]), "+v"(acc[3])
                     : VOS, [rs] "s"(rv), [so] "s"(so), [vt] "v"(vt), [rt] "s"(rt), [tn] "s"(tn) : "scc", "memory");
        PROF();
      }
      if (nb) {
        --nb;
        float sc[4];
        asm volatile("s_waitcnt lgkmcnt(0)" : "+v"(Kr[0][0]), "+v"(Kr[0][1]), "+v"(Kr[0][2]), "+v"(Kr[0][3]), "+v"(Vr[0][0]), "+v"(Vr[0][1]), "+v"(Vr[0][2]), "+v"(Vr[0][3]));
        DOTS(Kr[0])
        SOFT
        SB();
        PV(Vr[0])
#define DR(n, off) "ds_read_b128 %" #n ", %[sa] offset:" #off "\n\t"
#define DW(n, off) "ds_write_b128 %[sa], %" #n " offset:" #off "\n\t"
        asm volatile(DR(0, 0) DR(1, 1024) DR(2, 2048) DR(3, 3072) DR(4, 4096) DR(5, 5120) DR(6, 6144) DR(7, 7168)
                     DW(12, 0) DW(13, 1024) DW(14, 2048) DW(15, 3072) DW(16, 4096) DW(17, 5120) DW(18, 6144) DW(19, 7168)
                     : "+v"(Kr[0][0]), "+v"(Kr[0][1]), "+v"(Kr[0][2]), "+v"(Kr[0][3]), "+v"(Vr[0][0]), "+v"(Vr[0][1]), "+v"(Vr[0][2]), "+v"(Vr[0][3]),
                       "+v"(acc[0]), "+v"(acc[1]), "+v"(acc[2]), "+v"(acc[3])
                     : "v"(Kr[1][0]), "v"(Kr[1][1]), "v"(Kr[1][2]), "v"(Kr[1][3]), "v"(Vr[1][0]), "v"(Vr[1][1]), "v"(Vr[1][2]), "v"(Vr[1][3]), [sa] "v"(sa)
                     : "memory");
      }
      if (nb == 0 && tn >= 256) break;
    }
    asm volatile("s_waitcnt lgkmcnt(0)" :: "v"(Kr[0][0]), "v"(Kr[0][1]), "v"(Kr[0][2]), "v"(Kr[0][3]), "v"(Vr[0][0]), "v"(Vr[0][1]), "v"(Vr[0][2]), "v"(Vr[0][3]));
    tz -= ((tn - 256) * PC) >> 1;
    if (lane == 0) __hip_atomic_fetch_max(&L.cnt[5], tz, __ATOMIC_RELAXED, __HIP_MEMORY_SCOPE_WORKGROUP);
    STAMP(4);
    const float M = mx32(mx16(m));
    const float w = ex2((m - M) * LOG2E);
    l *= w; l = rs16(l, l); l = rs32(l, l);
    float a[8];
#pragma unroll
    for (int j = 0; j < 4; ++j) { a[2 * j] = acc[j].x * w; a[2 * j + 1] = acc[j].y * w; }
    float y[4];
#pragma unroll
    for (int j = 0; j < 4; ++j) y[j] = rs32(a[j], a[j + 4]);
    const int d0 = (lane & 15) * 8 + (lane >> 4) * 2;
    L.rec_o[s][d0 + (d0 * 171 >> 9)] = rs16(y[0], y[2]);
    L.rec_o[s][d0 + 1 + ((d0 + 1) * 171 >> 9)] = rs16(y[1], y[3]);
    if (lane == 0) { v2f ml = {M, l}; *(v2f*)&L.rec_ml[s][0] = ml; }
    WAIT_LGKM0();
    unsigned old = 0;
    if (lane == 0) old = __hip_atomic_fetch_add(&L.cnt[3], 1u, __ATOMIC_RELAXED, __HIP_MEMORY_SCOPE_WORKGROUP);
    old = __builtin_amdgcn_readfirstlane(old);
    MEMFENCE();
    if (old == 7) {
      float ms[8], ls[8];
      const unsigned tgs = L.cnt[7];
#pragma unroll
      for (int j = 0; j < 4; ++j) { v4f t = *(const v4f*)&L.rec_ml[2 * j][0]; ms[2 * j] = t.x; ls[2 * j] = t.y; ms[2 * j + 1] = t.z; ls[2 * j + 1] = t.w; }
      v4f o[8];
#pragma unroll
      for (int j = 0; j < 8; ++j) o[j] = *(const v4f*)&L.rec_o[j][(lane < 43 ? lane : 0) * 4];
      const float MM = fmaxf(fmaxf(fmaxf(ms[0], ms[1]), fmaxf(ms[2], ms[3])), fmaxf(fmaxf(ms[4], ms[5]), fmaxf(ms[6], ms[7])));
      float LL = 0.f; v4f oo = {0.f, 0.f, 0.f, 0.f};
#pragma unroll
      for (int j = 0; j < 8; ++j) { const float e = ex2((ms[j] - MM) * LOG2E); LL += e * ls[j]; oo += o[j] * e; }
      const v4u pv = {asu(lane < 43 ? oo.x : MM), asu(lane < 43 ? oo.y : LL), lane < 42 ? asu(oo.z) : 0u, tgs};
      const unsigned off = lane < 44 ? OFF_AO + b * 768 + lane * 16 : OOB;
      __builtin_amdgcn_raw_buffer_store_b128(pv, rws, off, 0, AUX_SC1);
      if (lane == 0) __hip_atomic_fetch_add(&L.cnt[3], 1u, __ATOMIC_RELAXED, __HIP_MEMORY_SCOPE_WORKGROUP);
      STAMP(5);
    }
    if (PR && tn < prk) {
      __builtin_amdgcn_s_setprio(0);
      wait_until(tz + XP);
      STAMP(9);
      asm volatile(PREF0 :: [pv] "v"(pv), [rp] "s"(rp), [sink] "s"(sink) : "m0");
    }
    WAIT_VM0();
    FLUSH();
    return;
  }

  const rsrc_t rws = mk(ws, WS_END);
  if (wave == 3) {
    const int hrow = (lane & 8) ? 64 + 3 * (lane & 3) + ((lane & 16) ? 2 : lane >> 5) : 8 * c8 + ((lane >> 3) & 6);
    const rsrc_t rin = mk(xin, 1152);
    const unsigned e3p = LD32(rws, 0, 0);
    const v4u xa = LD128(rin, lane * 16, 0), xb = LD128(rin, lane * 16 + 1024, 0);
    const unsigned xp = LD32(rin, (72 * x + (hrow & ~1)) * 2, 0);
    const unsigned tg = __builtin_amdgcn_readfirstlane(e3p) + TAG_STEP;
    float s = 0.f, q = 0.f;
#pragma unroll
    for (int d = 0; d < 4; ++d) {
      float e0 = lo16(xa[d]), e1 = hi16(xa[d]), e2 = lo16(xb[d]), e3 = hi16(xb[d]);
      s += (e0 + e1) + (e2 + e3); q += (e0 * e0 + e1 * e1) + (e2 * e2 + e3 * e3);
    }
    s = wave_sum(s); q = wave_sum(q);
    float m, rsd; ln_stats(s, q, m, rsd);
    float n0 = rbf(rbf(lo16(xp >> ((hrow & 1) * 16)) - m) * rsd), n1 = rbf(rbf(hi16(xp) - m) * rsd);
    PIN(n0); PIN(n1);
    if (PD1) __builtin_amdgcn_s_sleep(PD1);
    STAMP(1);
    const unsigned vp = OFF_P + (x * 8 + r8) * 896 + c8 * 16;
    v4u g[7];
    _Pragma("unroll 1") for (int it = 0; it < GCAP; ++it) {
      unsigned bad = 0;
#pragma unroll
      for (int t = 0; t < 6; ++t) g[t] = LD128(rws, vp + t * 128, AUX_SC1);
      g[6] = LD128(rws, vp + ((lane & 3) == 3 ? 752 : 768), AUX_SC1);
#pragma unroll
      for (int t = 0; t < 7; ++t) bad |= g[t][3] ^ tg;
      if (all_true(bad == 0)) break;
      MEMFENCE();
    }
    STAMP(2);
#define G3(t, j) asf(g[(t) + (j) / 3][(j) % 3])
    float sa[2], ca[2];
#pragma unroll
    for (int e = 0; e < 2; ++e) {
      sa[e] = rs16(rs32(G3(0, e), G3(0, e + 4)), rs32(G3(0, e + 2), G3(0, e + 6)));
      ca[e] = rs16(rs32(G3(3, e), G3(3, e + 4)), rs32(G3(3, e + 2), G3(3, e + 6)));
      sa[e] += dppf<R8>(sa[e]); ca[e] += dppf<R8>(ca[e]);
    }
    float z = rs16(rs32(G3(6, 0), G3(6, 1)), rs32(G3(6, 2), G3(6, 2)));
    z += dppf<R8>(z);
    const float zc = dppf<R4>(z);
    if (lane & 8) { sa[0] = z; ca[0] = zc; }
    const unsigned hh = pack2(modulate(n0, rbf(ca[0]), rbf(sa[0])), modulate(n1, rbf(ca[1]), rbf(sa[1])));
    if ((lane & 8) == 0) L.hs[hrow >> 1] = hh;
    else if ((lane & 4) == 0) ((u16*)L.hs)[hrow] = (u16)hh;
    WAIT_LGKM0();
    if (lane == 0) __hip_atomic_store(&L.cnt[0], 1u, __ATOMIC_RELAXED, __HIP_MEMORY_SCOPE_WORKGROUP);
    if (PD2) __builtin_amdgcn_s_sleep(PD2);
    STAMP(3);
    const unsigned vq = OFF_Q + (head * 8 + r8) * 768 + c8 * 16;
    _Pragma("unroll 1") for (int it = 0; it < GCAP; ++it) {
      unsigned bad = 0;
#pragma unroll
      for (int t = 0; t < 6; ++t) g[t] = LD128(rws, vq + t * 128, AUX_SC1);
#pragma unroll
      for (int t = 0; t < 6; ++t) bad |= g[t][3] ^ tg;
      if (all_true(bad == 0)) break;
      MEMFENCE();
    }
#define Q3(j) rs32(asf(g[(j) / 3 * 2][(j) % 3]), asf(g[(j) / 3 * 2 + 1][(j) % 3]))
    float qq[4];
#pragma unroll
    for (int e = 0; e < 4; ++e) { const float t = rs16(Q3(e), Q3(e + 4)); qq[e] = t + dppf<R8>(t); }
    const v2u qp = {pack2(qq[0], qq[1]), pack2(qq[2], qq[3])};
    *(v2u*)&L.qs[((lane >> 5) * 8 + c8) * 4 + ((lane >> 3) & 2)] = qp;
    L.cnt[7] = tg;
    WAIT_LGKM0();
    if (lane == 0) __hip_atomic_store(&L.cnt[4], 1u, __ATOMIC_RELAXED, __HIP_MEMORY_SCOPE_WORKGROUP);
    STAMP(4);
  } else if (!is_mod || ffn) {
    asm volatile("s_waitcnt vmcnt(9)" : "+v"(ep), "+v"(ci), "+v"(cz) :: "memory");
    const unsigned tg0 = __builtin_amdgcn_readfirstlane(ep) + TAG_STEP;
    const int u = lane >> 4;
    unsigned vo;
    Hv H;
    if (is_mod) {
      const int o = (r - 9) * 8 + c8, kind = o >= 72, oc = o - 72 * kind, gr = oc / 9, i = oc - 9 * gr;
      vo = OFF_P + (gr * 8 + x) * 896 + (i < 8 ? (3 * kind + u) * 128 + i * 16 : 768 + (4 * kind + u) * 16);
      hv_silu(H, ci, cz);
    } else {
      vo = OFF_Q + b * 768 + u * 256 + ((wave - 1) * 8 + c8) * 16;
    }
    if ((lane & 8) || lane >= 48) vo = OOB;
    PIN(vo);
    asm volatile("s_waitcnt vmcnt(0)" : "+v"(w[0]), "+v"(w[1]), "+v"(w[2]), "+v"(w[3]), "+v"(w[4]), "+v"(w[5]), "+v"(w[6]), "+v"(w[7]), "+v"(w[8]) :: "memory");
    Slab S;
    slab_make(S, w);
    STAMP(1);
    if (!is_mod) {
      lds_wait(&L.cnt[0], 1);
      const v4u hp = *(const v4u*)&L.hs[4 * r8];
      H.za = ((const u16*)L.hs)[64 + r8]; H.zb = H.za << 16;
#pragma unroll
      for (int p = 0; p < 4; ++p) H.p[p] = hp[p];
    }
    STAMP(2);
    float a[8]; slab_dot(a, S, H);
    float e0 = rs16(rs32(a[0], a[6]), rs32(a[3], a[3])), e1 = rs16(rs32(a[1], a[7]), rs32(a[4], a[4])), e2 = rs16(rs32(a[2], a[2]), rs32(a[5], a[5]));
    e0 += dppf<R8>(e0); e1 += dppf<R8>(e1); e2 += dppf<R8>(e2);
    const v4u pv = {asu(e0), asu(e1), asu(e2), tg0};
    __builtin_amdgcn_raw_buffer_store_b128(pv, rws, vo, 0, AUX_SC1);
    STAMP(3);
  }
  const int qr = lane >> 2, qc = lane & 3;
  const unsigned mcopy = OFF_M + x * M_COPY;
  if (r < 9) {
    const rsrc_t rw = mk(wo + (size_t)(512 * x + 128 * wave) * 576 + 64 * r, 127 * 1152 + 128);
    v4u wo_w[16];
    if (LATEQ) lds_wait(&L.cnt[4], 1);
#pragma unroll
    for (int t = 0; t < 16; ++t) wo_w[t] = LD128(rw, (16 * r8 + t) * 1152 + c8 * 16, AUX_NT);
    unsigned P[8][8];
#pragma unroll
    for (int p = 0; p < 8; ++p) {
      interleave(P[p], wo_w[2 * p], wo_w[2 * p + 1]);
#pragma unroll
      for (int d = 0; d < 8; ++d) PIN(P[p][d]);
    }
    lds_wait(&L.cnt[4], 1);
    const unsigned tg = __builtin_amdgcn_readfirstlane(L.cnt[7]);
    lds_wait(&L.cnt[3], 9);
    STAMP(5);
    const unsigned va = OFF_AO + (4 * x + wave) * 8 * 768 + (lane < 43 ? lane : 43) * 16, va0 = va + (lane < 48 ? 0 : c8 * 768);
    v4u o[8];
    _Pragma("unroll 1") for (int it = 0; it < GCAP; ++it) {
      unsigned bad = 0;
#pragma unroll
      for (int j = 0; j < 8; ++j) o[j] = LD128(rws, j ? va + j * 768 : va0, AUX_SC1);
#pragma unroll
      for (int j = 0; j < 8; ++j) bad |= o[j][3] ^ tg;
      if (all_true(bad == 0)) break;
      MEMFENCE();
    }
    STAMP(6);
    const float mi = asf(o[0][0]);
    float M = fmaxf(mi, dppf<X1>(mi)); M = fmaxf(M, dppf<X2>(M)); M = fmaxf(M, dppf<HM>(M));
    const float e = ex2((mi - M) * LOG2E);
    float Ls = e * asf(o[0][1]); Ls += dppf<X1>(Ls); Ls += dppf<X2>(Ls); Ls += dppf<HM>(Ls);
    float oo[3] = {0.f, 0.f, 0.f};
#pragma unroll
    for (int j = 0; j < 8; ++j) {
      const float ej = asf(__builtin_amdgcn_readlane(asu(e), 48 + j));
#pragma unroll
      for (int d = 0; d < 3; ++d) oo[d] = fmaf(ej, asf(o[j][d]), oo[d]);
    }
    const float rl = asf(__builtin_amdgcn_readlane(asu(rcp(Ls)), 48));
    {
      const unsigned p01 = pack2(oo[0] * rl, oo[1] * rl), p2 = pack2(oo[2] * rl, 0.f);
      u16* const ot = (u16*)L.ob[wave] + 3 * lane;
      ot[0] = (u16)p01; ot[1] = (u16)(p01 >> 16); ot[2] = (u16)p2;
    }
    WAIT_LGKM0();
    const v4u oa = *(const v4u*)&L.ob[wave][r8 * 8], ob = *(const v4u*)&L.ob[wave][r8 * 8 + 4];
    float acc[8] = {0.f, 0.f, 0.f, 0.f, 0.f, 0.f, 0.f, 0.f};
#pragma unroll
    for (int p = 0; p < 8; ++p) {
      const unsigned hp = p < 4 ? oa[p & 3] : ob[p & 3];
#pragma unroll
      for (int d = 0; d < 8; ++d) acc[d] = dot2(P[p][d], hp, acc[d]);
    }
    float y[4];
#pragma unroll
    for (int j = 0; j < 4; ++j) y[j] = rs32(acc[j], acc[j + 4]);
    float z0 = rs16(y[0], y[2]), z1 = rs16(y[1], y[3]);
    z0 += dppf<R8>(z0); z1 += dppf<R8>(z1);
    if ((lane & 8) == 0) { v2f z = {z0, z1}; *(v2f*)&L.hp[wave][c8 * 8 + (lane >> 4) * 2] = z; }
    wg_barrier();
    if (wave == 0) {
      const int c4 = (lane & 15) * 4;
      const v4f t = (*(const v4f*)&L.hp[0][c4] + *(const v4f*)&L.hp[1][c4]) + (*(const v4f*)&L.hp[2][c4] + *(const v4f*)&L.hp[3][c4]);
      __builtin_amdgcn_raw_buffer_store_b128(seal(t, tg), rws, OFF_B + (lane >> 4) * 18432 + x * 2304 + r * 256 + c4 * 4, 0, AUX_SC1);
      STAMP(7);
    }
  } else if (r < 27) {
    const int f = 18 * x + (r - 9);
    const bool act3 = qc < 3;
    const int un4 = 144 * wave + (qc < 2 ? 8 * qr + 4 * qc : 128 + (qr & 12));
    const v2u xi = LD64(mk(xin, 1152), act3 ? un4 * 2 : OOB, 0);
    float xv[4], ga[4], sb[4], cb[4];
    lds_wait(&L.cnt[4], 1);
    const unsigned tg = __builtin_amdgcn_readfirstlane(L.cnt[7]);
    {
      for (int i = 0; i < MDEL; ++i) __builtin_amdgcn_s_sleep(127);
      const unsigned vm = act3 ? mcopy + un4 * 4 : OOB;
      v4u g[3];
      _Pragma("unroll 1") for (int it = 0; it < GCAP; ++it) {
        unsigned bad = 0;
#pragma unroll
        for (int j = 0; j < 3; ++j) g[j] = LD128(rws, vm + j * 2304, AUX_SC1);
#pragma unroll
        for (int j = 0; j < 3; ++j) bad |= (g[j][1] ^ tg) | (g[j][3] ^ tg);
        if (all_true(bad == 0 || !act3)) break;
        __builtin_amdgcn_s_sleep(32);
        MEMFENCE();
      }
      STAMP(4);
#pragma unroll
      for (int e = 0; e < 4; ++e) {
        const bool on = qc < 2 || (qc == 2 && (qr & 3) == e);
        const float a = (e & 1) ? hi16(xi[e >> 1]) : lo16(xi[e >> 1]), g0 = (e & 1) ? hi16(g[0][e & 2]) : lo16(g[0][e & 2]);
        const float g1 = (e & 1) ? hi16(g[1][e & 2]) : lo16(g[1][e & 2]), g2 = (e & 1) ? hi16(g[2][e & 2]) : lo16(g[2][e & 2]);
        xv[e] = on ? a : 0.f; ga[e] = on ? g0 : 0.f; sb[e] = on ? g1 : 0.f; cb[e] = on ? rbf(1.f + g2) : 0.f;
        PIN(xv[e]); PIN(ga[e]); PIN(sb[e]); PIN(cb[e]);
      }
    }
    __builtin_amdgcn_s_setprio(0);
    const rsrc_t rg = mk(wg + f * 32, 575 * 9216 + 64), ru = mk(wu + f * 32, 575 * 9216 + 64);
    const rsrc_t rd = mk(wd + (size_t)(32 * f + 8 * wave) * 576, 8 * 1152);
    v4u w1[9], w2[9], w3[8]; unsigned w4[8];
#define LDD(t0, t1) _Pragma("unroll") for (int t = t0; t < t1; ++t) { w3[t] = LD128(rd, t * 1152 + lane * 16, AUX_NT); w4[t] = LD16(rd, t * 1152 + 1024 + lane * 2, AUX_NT); }
    LDD(0, DE)
    SB(); lds_wait(&L.cnt[3], DTRIG - 8); if (DDEL) __builtin_amdgcn_s_sleep(DDEL);
    const unsigned tz = __builtin_amdgcn_readfirstlane(L.cnt[5]);
    if (XD) wait_until(tz + XD);
    STAMP(5); SB();
    LDD(DE, DL)
    SB(); lds_wait(&L.cnt[3], GTRIG);
    if (GDEL) __builtin_amdgcn_s_sleep(GDEL);
    if (XG) wait_until(tz + XG);
    STAMP(9); SB();
    const unsigned vg = (144 * wave + 8 * qr) * 9216 + qc * 16, vz = (144 * wave + 128 + qr) * 9216 + qc * 16;
#pragma unroll
    for (int t = 0; t < 9; ++t) w1[t] = LD128(rg, t < 8 ? vg + t * 9216 : vz, AUX_NT);
#pragma unroll
    for (int t = 0; t < 9; ++t) w2[t] = LD128(ru, t < 8 ? vg + t * 9216 : vz, AUX_NT);
    SB();
    LDD(DL, DB)
    SB();
    unsigned Dp[4][8], Dt[4];
#define MKD(p0, p1) _Pragma("unroll") for (int p = p0; p < p1; ++p) { \
      interleave(Dp[p], w3[2 * p], w3[2 * p + 1]); Dt[p] = w4[2 * p] | (w4[2 * p + 1] << 16); \
      _Pragma("unroll") for (int d = 0; d < 8; ++d) PIN(Dp[p][d]); \
      PIN(Dt[p]); }
    MKD(0, DB / 2)
    STAMP(10);
    Slab G, U; slab_make(G, w1); slab_make(U, w2);
    STAMP(6);
    const unsigned vb = act3 ? OFF_B + (x & 3) * 18432 + un4 * 4 : OOB;
    v4u yb[8];
    _Pragma("unroll 1") for (int it = 0; it < GCAP; ++it) {
#pragma unroll
      for (int j = 0; j < 8; ++j) yb[j] = LD128(rws, vb + j * 2304, AUX_SC1);
      unsigned bad = 0;
#pragma unroll
      for (int j = 0; j < 8; ++j) bad |= unseal(yb[j], tg);
      if (all_true((bad & 0xff) == 0 || !act3)) break;
      MEMFENCE();
    }
    STAMP(7);
    float xr[4];
#pragma unroll
    for (int j = 0; j < 4; ++j) {
      const float t = ((asf(yb[0][j]) + asf(yb[1][j])) + (asf(yb[2][j]) + asf(yb[3][j]))) + ((asf(yb[4][j]) + asf(yb[5][j])) + (asf(yb[6][j]) + asf(yb[7][j])));
      xr[j] = rbf(xv[j] + rbf(ga[j] * rbf(t)));
    }
    float s = (xr[0] + xr[1]) + (xr[2] + xr[3]), q = (xr[0] * xr[0] + xr[1] * xr[1]) + (xr[2] * xr[2] + xr[3] * xr[3]);
    s = wave_sum(s); q = wave_sum(q);
    if (lane == 0) { v2f t = {s, q}; *(v2f*)&L.st[wave][0] = t; }
    wg_barrier();
    SB();
    LDD(DB, 8)
    SB();
    {
      const v4f t0 = *(const v4f*)&L.st[0][0], t1 = *(const v4f*)&L.st[2][0];
      s = (t0.x + t0.z) + (t1.x + t1.z); q = (t0.y + t0.w) + (t1.y + t1.w);
    }
    float m, rsd; ln_stats(s, q, m, rsd);
#pragma unroll
    for (int j = 0; j < 4; ++j) xr[j] = rbf(rbf(rbf(xr[j] - m) * rsd) * cb[j]) + sb[j];
    const unsigned h01 = pack2(xr[0], xr[1]), h23 = pack2(xr[2], xr[3]);
    Hv H;
    H.p[0] = dppu<0x00>(h01); H.p[1] = dppu<0x00>(h23); H.p[2] = dppu<0x55>(h01); H.p[3] = dppu<0x55>(h23);
    { const unsigned hz = dppu<0xAA>(h01 | h23); H.zb = (hz | (hz << 16)) & 0xffff0000u; H.za = H.zb >> 16; }
    float ag[8], au[8]; slab_dot(ag, G, H); slab_dot(au, U, H);
    float y[8];
#pragma unroll
    for (int j = 0; j < 8; ++j) y[j] = rs32(ag[j], au[j]);
    v4f z;
#pragma unroll
    for (int j = 0; j < 4; ++j) { float t = rs16(y[j], y[j + 4]); t += dppf<R4>(t); t += dppf<R8>(t); z[j] = t; }
    if ((lane & 12) == 0) *(v4f*)&L.hp[wave][(lane >> 5) * 32 + qc * 8 + ((lane >> 4) & 1) * 4] = z;
    wg_barrier();
    const int un = 8 * wave + (lane & 7);
    const float g = rbf((L.hp[0][un] + L.hp[1][un]) + (L.hp[2][un] + L.hp[3][un]));
    const float up = rbf((L.hp[0][32 + un] + L.hp[1][32 + un]) + (L.hp[2][32 + un] + L.hp[3][32 + un]));
    const float av = rbf(silu(g)) * up;
    unsigned ap = pack2(av, dppf<X1>(av));
    if (DB < 8) { PIN(ap); SB(); STAMP(11); MKD(DB / 2, 4) }
    float d8[8] = {0.f, 0.f, 0.f, 0.f, 0.f, 0.f, 0.f, 0.f}, dt = 0.f;
#pragma unroll
    for (int p = 0; p < 4; ++p) {
      const unsigned a2 = __builtin_amdgcn_readlane(ap, 2 * p);
#pragma unroll
      for (int d = 0; d < 8; ++d) d8[d] = dot2(Dp[p][d], a2, d8[d]);
      dt = dot2(Dt[p], a2, dt);
    }
    { v4f t0 = {d8[0], d8[1], d8[2], d8[3]}, t1 = {d8[4], d8[5], d8[6], d8[7]};
      *(v4f*)&L.dn[wave][lane * 8] = t0; *(v4f*)&L.dn[wave][lane * 8 + 4] = t1; L.dn[wave][512 + lane] = dt; }
    wg_barrier();
    {
      const int c3 = 144 * wave + (lane < 48 ? lane : 0) * 3;
      v4u t; t[3] = tg;
#pragma unroll
      for (int e = 0; e < 3; ++e) t[e] = asu((L.dn[0][c3 + e] + L.dn[1][c3 + e]) + (L.dn[2][c3 + e] + L.dn[3][c3 + e]));
      __builtin_amdgcn_raw_buffer_store_b128(t, rws, lane < 48 ? OFF_C + f * 3072 + (48 * wave + lane) * 16 : OOB, 0, AUX_SC1);
    }
    STAMP(8);
  } else {
    const int j = (r - 27) * 8 + x;
    if (j >= 36) { FLUSH(); return; }
    unsigned tg;
    {
      const rsrc_t rw = mk(wmod + 144 * wave * 3456 + 1152 + 64 * j, 143 * 6912 + 128), rin = mk(cin + 144 * wave, 288);
      if (LATEQ) lds_wait(&L.cnt[4], 1);
      v4u ci[2], w[2][9]; unsigned cz[2];
#pragma unroll
      for (int u = 0; u < 2; ++u) { ci[u] = LD128(rin, u * 144 + r8 * 16, 0); cz[u] = LD16(rin, u * 144 + 128 + r8 * 2, 0); }
#pragma unroll
      for (int u = 0; u < 2; ++u)
#pragma unroll
        for (int t = 0; t < 9; ++t) w[u][t] = LD128(rw, (72 * u + (t < 8 ? 8 * r8 + t : 64 + r8)) * 6912 + c8 * 16, AUX_NT);
      SB();
      v4f y = {0.f, 0.f, 0.f, 0.f};
#pragma unroll
      for (int u = 0; u < 2; ++u) {
        Hv H; Slab S; float acc[8];
        hv_silu(H, ci[u], cz[u]); slab_make(S, w[u]); slab_dot(acc, S, H);
        y += fold8(acc);
      }
      if ((lane & 24) == 0) *(v4f*)&L.hp[wave][c8 * 8 + (lane >> 5) * 4] = y;
      WAIT_LGKM0();
      if (lane == 0) __hip_atomic_fetch_add(&L.cnt[1], 1u, __ATOMIC_RELAXED, __HIP_MEMORY_SCOPE_WORKGROUP);
      if (wave > 1) { FLUSH(); return; }
      lds_wait(&L.cnt[1], 4);
      tg = __builtin_amdgcn_readfirstlane(L.cnt[7]);
      const int c4 = (lane & 15) * 4;
      const v4f t = (*(const v4f*)&L.hp[0][c4] + *(const v4f*)&L.hp[1][c4]) + (*(const v4f*)&L.hp[2][c4] + *(const v4f*)&L.hp[3][c4]);
      const v4u g = {pack2(t.x, t.y), tg, pack2(t.z, t.w), tg};
      __builtin_amdgcn_raw_buffer_store_b128(g, rws, OFF_M + (4 * wave + (lane >> 4)) * M_COPY + j * 256 + c4 * 4, 0, AUX_SC1);
      STAMP(4);
      if (j >= 24) { FLUSH(); return; }
    }
    lds_wait(&L.cnt[3], 9);
    const int jj = 2 * j + wave, col = 12 * jj + 3 * qc + ((lane & 16) ? 2 : lane >> 5);
    const float xv = lo16(LD16(mk(xin, 1152), col * 2, 0));
    v2u ga, gb;
    _Pragma("unroll 1") for (int it = 0; it < GCAP; ++it) {
      ga = LD64(rws, mcopy + (col >> 1) * 8, AUX_SC1); gb = LD64(rws, mcopy + 3 * 2304 + (col >> 1) * 8, AUX_SC1);
      unsigned bad = (ga[1] ^ tg) | (gb[1] ^ tg);
      if (all_true(bad == 0)) break;
      __builtin_amdgcn_s_sleep(32);
      MEMFENCE();
    }
    float gav = lo16(ga[0] >> ((col & 1) * 16)), gbv = lo16(gb[0] >> ((col & 1) * 16));
    STAMP(5);
    const unsigned vb = OFF_B + (x & 3) * 18432 + (qr & 3) * 4608 + col * 4, tb = (tg >> (8 * (col & 3))) & 0xff;
    unsigned y0, y1;
    _Pragma("unroll 1") for (int it = 0; it < GCAP; ++it) {
      y0 = LD32(rws, vb, AUX_SC1) ^ tb; y1 = LD32(rws, vb + 2304, AUX_SC1) ^ tb;
      if (all_true(((y0 | y1) & 0xff) == 0)) break;
      __builtin_amdgcn_s_sleep(4);
      MEMFENCE();
    }
    float ys = asf(y0) + asf(y1);
    ys += dppf<R4>(ys); ys += dppf<R8>(ys);
    float x1 = rbf(xv + rbf(gav * rbf(ys)));
    unsigned vout = ((lane & 12) == 0 && lane < 48) ? col * 2 : OOB;
    PIN(x1); PIN(gbv); PIN(vout);
    STAMP(6);
    const unsigned vc = OFF_C + qr * 3072 + jj * 64 + qc * 16;
    const u64 wa = (u64)ws;
    const v4u rc = {(unsigned)wa, (unsigned)(wa >> 32) & 0xffffu, WS_END, 0x00020000u};
    v4u cc[CN][9];
    v2f a01; float a2;
#define CL1(n) "buffer_load_dwordx4 %" #n ", %[vc], %[rc], %[so] offen sc1\n\ts_add_u32 %[so], %[so], 0xc000\n\t"
#define CREG(m, n) m(cc[n][0]), m(cc[n][1]), m(cc[n][2]), m(cc[n][3]), m(cc[n][4]), m(cc[n][5]), m(cc[n][6]), m(cc[n][7]), m(cc[n][8])
#define CLD(n) { unsigned so; asm volatile("s_mov_b32 %[so], 0\n\t" CL1(0) CL1(1) CL1(2) CL1(3) CL1(4) CL1(5) CL1(6) CL1(7) CL1(8) \
                              : CREG("=&v", n), [so] "=&s"(so) : [vc] "v"(vc), [rc] "s"(rc) : "scc", "memory"); }
#pragma unroll
    for (int n = 0; n < CN; ++n) { CLD(n) if (n < CN - 1) __builtin_amdgcn_s_sleep(CD); }
    _Pragma("unroll 1") for (int it = 0; it < GCAP; ++it) {
#pragma unroll
      for (int n = 0; n < CN; ++n) {
        unsigned bad = 0;
#pragma unroll
        for (int t = 0; t < 9; t += 3) {
          asm volatile("s_waitcnt vmcnt(%3)" : "+v"(cc[n][t]), "+v"(cc[n][t + 1]), "+v"(cc[n][t + 2]) : "n"(9 * CN - 3 - t));
#pragma unroll
          for (int u = t; u < t + 3; ++u) {
            const v2f e = {asf(cc[n][u][0]), asf(cc[n][u][1])};
            bad |= cc[n][u][3] ^ tg; a01 = u ? a01 + e : e; a2 = u ? a2 + asf(cc[n][u][2]) : asf(cc[n][u][2]);
          }
          PIN(a01); PIN(a2); PIN(bad);
        }
        if (all_true(bad == 0)) goto have_c;
        CLD(n)
      }
    }
  have_c:
    STAMP(7);
    float t = rs16(rs32(a01.x, a01.y), rs32(a2, a2));
    t += dppf<R4>(t); t += dppf<R8>(t);
    const float res = x1 + rbf(gbv * rbf(t));
    __builtin_amdgcn_raw_buffer_store_b16((u16)pack2(res, 0.f), mk(out, 1152), vout, 0, 0);
    if (jj == 0) __builtin_amdgcn_raw_buffer_store_b32(tg, rws, lane == 0 ? 0u : OOB, 0, AUX_SC1);
    STAMP(8);
#pragma unroll
    for (int n = 0; n < CN; ++n) asm volatile("s_waitcnt vmcnt(0)" :: CREG("v", n));
  }
  FLUSH();
}

extern "C" void kernel_launch(void* const* d_in, const int* in_sizes, int n_in, void* d_out, int out_size, void* d_ws,
                              size_t ws_size, hipStream_t stream) {
  const Args a = {(const u16*)d_in[0], (const u16*)d_in[3], (const u16*)d_in[9], (const u16*)d_in[10], (char*)d_ws, (u16*)d_out,
                  (const u16*)d_in[4], (const u16*)d_in[8], (const u16*)d_in[6], (const u16*)d_in[7], (const u16*)d_in[1], (const u16*)d_in[2]};
  fused_block<<<256, 768, 0, stream>>>(a);
}
```

```cpp
#include <hip/hip_runtime.h>
#include <hip/hip_bf16.h>
#include <stdint.h>

typedef unsigned short u16;
typedef unsigned long long u64;
typedef unsigned v4u __attribute__((ext_vector_type(4)));
typedef unsigned v3u __attribute__((ext_vector_type(3)));
typedef unsigned v2u __attribute__((ext_vector_type(2)));
typedef float v2f __attribute__((ext_vector_type(2)));
typedef float v4f __attribute__((ext_vector_type(4)));
typedef __bf16 bf2 __attribute__((ext_vector_type(2)));
typedef __amdgpu_buffer_rsrc_t rsrc_t;

#define TF 4
#define TS 5
#define STAMPS 0
#define WTRIG 0
#define ENDX 0
#define PAT 0
#define DTRIG 17
#define GTRIG 9
#define GDEL 10
#define SLIM 255
#define PTRIG 8
#define PDEL 0
#define PSTEP 1
#define GAPK 0
#define WOE 4
#define DDEL 0
#define ROTX ((head >> 2) * 64)
#define NL 1
#define CPRE 5
#define MDEL 2
#define PD1 30
#define PD2 40
#define LATEQ 1

constexpr int AUX_NT = 2, AUX_SC1 = 16;
constexpr unsigned TAG_STEP = 0x01010101u;
constexpr unsigned OOB = 0x40000000u;
constexpr unsigned OFF_T = 4096;
constexpr unsigned OFF_P = 16384;
constexpr unsigned OFF_Q = OFF_P + 64 * 896;
constexpr unsigned OFF_M = OFF_Q + 256 * 768;
constexpr unsigned M_COPY = 4 * 2304;
constexpr unsigned OFF_AO = OFF_M + 8 * M_COPY;
constexpr unsigned OFF_B = OFF_AO + 256 * 768;
constexpr unsigned OFF_C = OFF_B + 4 * 18432;
constexpr unsigned WS_END = OFF_C + 331776;
constexpr unsigned OFF_STAMP = 1u << 20;
constexpr int GCAP = 1 << 15;
constexpr int LCAP = 1 << 20;
constexpr float LOG2E = 1.4426950408889634f;

__device__ __forceinline__ float asf(unsigned u) { return __builtin_bit_cast(float, u); }
__device__ __forceinline__ unsigned asu(float f) { return __builtin_bit_cast(unsigned, f); }
__device__ __forceinline__ float lo16(unsigned u) { return asf(u << 16); }
__device__ __forceinline__ float hi16(unsigned u) { return asf(u & 0xffff0000u); }
__device__ __forceinline__ float rbf(float f) { __bf16 b = (__bf16)f; return (float)b; }
__device__ __forceinline__ unsigned pack2(float a, float b) { bf2 v = {(__bf16)a, (__bf16)b}; return __builtin_bit_cast(unsigned, v); }
__device__ __forceinline__ float dot2(unsigned a, unsigned b, float c) {
  return __builtin_amdgcn_fdot2_f32_bf16(__builtin_bit_cast(bf2, a), __builtin_bit_cast(bf2, b), c, false);
}
template <int CTRL> __device__ __forceinline__ unsigned dppu(unsigned v) { return __builtin_amdgcn_update_dpp(0u, v, CTRL, 0xf, 0xf, true); }
template <int CTRL> __device__ __forceinline__ float dppf(float v) { return asf(dppu<CTRL>(asu(v))); }
constexpr int X1 = 0xB1, X2 = 0x4E, HM = 0x141, R4 = 0x124, R8 = 0x128;
__device__ __forceinline__ float rs32(float a, float b) {
  auto r = __builtin_amdgcn_permlane32_swap(asu(a), asu(b), false, false);
  unsigned r0 = r[0], r1 = r[1]; return asf(r0) + asf(r1);
}
__device__ __forceinline__ float rs16(float a, float b) {
  auto r = __builtin_amdgcn_permlane16_swap(asu(a), asu(b), false, false);
  unsigned r0 = r[0], r1 = r[1]; return asf(r0) + asf(r1);
}
__device__ __forceinline__ float mx32(float a) {
  auto r = __builtin_amdgcn_permlane32_swap(asu(a), asu(a), false, false);
  unsigned r0 = r[0], r1 = r[1]; return fmaxf(asf(r0), asf(r1));
}
__device__ __forceinline__ float mx16(float a) {
  auto r = __builtin_amdgcn_permlane16_swap(asu(a), asu(a), false, false);
  unsigned r0 = r[0], r1 = r[1]; return fmaxf(asf(r0), asf(r1));
}
__device__ __forceinline__ float wave_sum(float v) {
  v += dppf<X1>(v); v += dppf<X2>(v); v += dppf<R4>(v); v += dppf<R8>(v);
  v = rs16(v, v); v = rs32(v, v); return v;
}
__device__ __forceinline__ float ex2(float x) { return __builtin_amdgcn_exp2f(x); }
__device__ __forceinline__ float rcp(float x) { return __builtin_amdgcn_rcpf(x); }
__device__ __forceinline__ float silu(float g) { return g * rcp(1.f + ex2(-g * LOG2E)); }
__device__ __forceinline__ v4u seal(v4f t, unsigned tg) {
  v4u o;
#pragma unroll
  for (int j = 0; j < 4; ++j) o[j] = ((asu(t[j]) + 0x80u) & ~0xffu) | ((tg >> (8 * j)) & 0xffu);
  return o;
}
__device__ __forceinline__ unsigned unseal(v4u& v, unsigned tg) {
#pragma unroll
  for (int j = 0; j < 4; ++j) v[j] ^= (tg >> (8 * j)) & 0xffu;
  asm volatile("" : "+v"(v));
  return (v[0] | v[1]) | (v[2] | v[3]);
}
__device__ __forceinline__ bool all_true(bool ok) { return __builtin_amdgcn_ballot_w64(!ok) == 0; }
__device__ __forceinline__ rsrc_t mk(const void* p, unsigned n) { return __builtin_amdgcn_make_buffer_rsrc((void*)p, 0, n, 0x00020000); }
#define LD128(r, v, aux) __builtin_amdgcn_raw_buffer_load_b128(r, v, 0, aux)
#define LD96(r, v, aux) __builtin_amdgcn_raw_buffer_load_b96(r, v, 0, aux)
#define LD64(r, v, aux) __builtin_amdgcn_raw_buffer_load_b64(r, v, 0, aux)
#define LD32(r, v, aux) __builtin_amdgcn_raw_buffer_load_b32(r, v, 0, aux)
#define LD16(r, v, aux) __builtin_amdgcn_raw_buffer_load_b16(r, v, 0, aux)
#define SB() __builtin_amdgcn_sched_barrier(0)
#define MEMFENCE() asm volatile("" ::: "memory")
#define WAIT_LGKM0() asm volatile("s_waitcnt lgkmcnt(0)" ::: "memory")
#define WAIT_VM0() asm volatile("s_waitcnt vmcnt(0)" ::: "memory")
#define PIN(x) asm volatile("" : "+v"(x))
__device__ __forceinline__ void wg_barrier() { WAIT_LGKM0(); __builtin_amdgcn_s_barrier(); MEMFENCE(); }

struct __attribute__((aligned(16))) Lds {
  float rec_o[8][176];
  float rec_ml[8][2];
  float dn[4][576];
  float hp[4][64];
  unsigned ob[4][96];
  unsigned qs[64];
  unsigned hs[40];
  unsigned sink[64];
  float st[4][2];
  v4u stg[8][NL ? NL : 1][8][64];
  unsigned cnt[8];
};

__device__ __forceinline__ void lds_wait(const unsigned* w, unsigned target) {
  _Pragma("unroll 1") for (int it = 0; it < LCAP; ++it) {
    unsigned v = __hip_atomic_load(w, __ATOMIC_RELAXED, __HIP_MEMORY_SCOPE_WORKGROUP);
    if (__builtin_amdgcn_readfirstlane(v) >= target) break;
    __builtin_amdgcn_s_sleep(1);
  }
  MEMFENCE();
}

struct Slab { unsigned p[4][8]; unsigned z[4]; };
__device__ __forceinline__ void interleave(unsigned (&o)[8], v4u a, v4u b) {
#pragma unroll
  for (int d = 0; d < 4; ++d) {
    unsigned x = a[d], y = b[d];
    o[2 * d] = __builtin_amdgcn_perm(y, x, 0x05040100u);
    o[2 * d + 1] = __builtin_amdgcn_perm(y, x, 0x07060302u);
  }
}
__device__ __forceinline__ void slab_make(Slab& s, const v4u (&w)[9]) {
#pragma unroll
  for (int p = 0; p < 4; ++p) {
    interleave(s.p[p], w[2 * p], w[2 * p + 1]); s.z[p] = w[8][p];
#pragma unroll
    for (int d = 0; d < 8; ++d) PIN(s.p[p][d]);
    PIN(s.z[p]);
  }
}
struct Hv { unsigned p[4], za, zb; };
__device__ __forceinline__ void hv_silu(Hv& h, v4u a, unsigned z) {
#pragma unroll
  for (int p = 0; p < 4; ++p) h.p[p] = pack2(silu(lo16(a[p])), silu(hi16(a[p])));
  h.za = pack2(silu(lo16(z)), 0.f); h.zb = h.za << 16;
}
__device__ __forceinline__ void slab_dot(float (&acc)[8], const Slab& s, const Hv& h) {
#pragma unroll
  for (int d = 0; d < 4; ++d) { acc[2 * d] = dot2(s.z[d], h.za, 0.f); acc[2 * d + 1] = dot2(s.z[d], h.zb, 0.f); }
#pragma unroll
  for (int p = 0; p < 4; ++p)
#pragma unroll
    for (int d = 0; d < 8; ++d) acc[d] = dot2(s.p[p][d], h.p[p], acc[d]);
}
__device__ __forceinline__ v4f fold8(const float (&acc)[8]) {
  v4f y;
#pragma unroll
  for (int j = 0; j < 4; ++j) { float t = rs32(acc[j], acc[j + 4]); t = rs16(t, t); y[j] = t + dppf<R8>(t); }
  return y;
}
__device__ __forceinline__ void ln_stats(float s, float q, float& m, float& rsd) {
  float mean = s * (1.f / 576.f);
  float var = fmaxf(q * (1.f / 576.f) - mean * mean, 0.f);
  m = rbf(mean);
  float sd = rbf(__builtin_sqrtf(rbf(rbf(var) + 1e-5f)));
  rsd = 1.f / sd;
}
__device__ __forceinline__ float modulate(float t, float scale, float shift) { return rbf(rbf(t * rbf(1.f + scale)) + shift); }

#define PROF() do {} while (0)
#define STAMP(k) do {} while (0)
#define FLUSH() do {} while (0)

struct Args { const u16 *xin, *wq, *cin, *wmod; char* ws; u16* out; const u16 *wo, *wd, *wg, *wu, *Kc, *Vc; };
typedef const Args __attribute__((address_space(4))) * args_t;
__device__ __forceinline__ args_t args_ptr() {
  args_t p = (args_t)__builtin_amdgcn_kernarg_segment_ptr();
  asm volatile("" : "+s"(p));
  return p;
}

__global__ void __launch_bounds__(768) fused_block(Args) {
  __shared__ Lds L;
  const int tid = threadIdx.x, lane = tid & 63, wave = __builtin_amdgcn_readfirstlane(tid >> 6);
  const int b = blockIdx.x, x = b & 7, head = b >> 3, r = (head + 4 * x) & 31;
  STAMP(0);
  const int k = r >> 2;
  const int r8 = lane >> 3, c8 = lane & 7;
  const unsigned sink = (unsigned)(size_t)(__attribute__((address_space(3))) void*)L.sink;

  const bool is_mod = wave == 0, ffn = r >= 9 && r < 27;
  unsigned ep, cz;
  v4u ci, w[9];
  const u16 *xin, *cin, *wmod, *wo, *wd, *wg, *wu; u16* out; char* ws;
  if (wave < 4) {
    __builtin_amdgcn_s_setprio(3);
    const args_t A = args_ptr();
    const u16* wq = A->wq;
    xin = A->xin; cin = A->cin; wmod = A->wmod; ws = A->ws; out = A->out; wo = A->wo; wd = A->wd; wg = A->wg; wu = A->wu;
    if (tid < 8) L.cnt[tid] = 0;
    if (CPRE && wave == 3) {
      u64 pc; unsigned junk;
      asm volatile("s_getpc_b64 %0" : "=s"(pc));
      const v4u rc = {(unsigned)pc & ~255u, (unsigned)(pc >> 32) & 0xffffu, CPRE * 4096u, 0x00020000u};
      asm volatile("s_mov_b32 m0, %2\n\ts_nop 1\n\tbuffer_load_dword %0, %1, 0 offen lds" :: "v"(lane < CPRE ? (lane * 32 + r) * 128 : OOB), "s"(rc), "s"(sink) : "m0");
    }
    unsigned ld = is_mod ? (ffn ? 6912 : 0) : (wave == 3 ? 0 : 8192);
    unsigned moff = 72 * x * ld + (is_mod ? r - 9 : 2 * head + wave - 1) * 128;
    unsigned v0 = 8 * r8 * ld + c8 * 16, v8 = (64 + r8) * ld + c8 * 16, vi = r8 * 16, vz = 128 + r8 * 2;
    unsigned vt = (lane == 0 && wave == 1 && x == 0) ? OFF_T + head * 256 : OOB, t0 = 64 * NL;
    asm volatile("" : "+s"(ld), "+s"(moff));
    asm volatile("" : "+v"(v0), "+v"(v8), "+v"(vz), "+v"(vi), "+v"(vt), "+v"(t0));
    SB();
    asm volatile("" :: "s"(xin), "s"(wq), "s"(wo), "s"(wg), "s"(wu), "s"(wd), "s"(cin), "s"(wmod), "s"(out), "s"(ws));
    const u64 wa = (u64)ws, ia = (u64)cin + 144 * x, ma = (u64)(is_mod ? wmod : wq) + moff;
    const v4u r0 = {(unsigned)wa, (unsigned)(wa >> 32) & 0xffffu, WS_END, 0x00020000u};
    const v4u ri = {(unsigned)ia, (unsigned)(ia >> 32) & 0xffffu, 144u, 0x00020000u};
    const v4u rm = {(unsigned)ma, (unsigned)(ma >> 32) & 0xffffu, 71 * ld + 128, 0x00020000u};
    unsigned so;
#define LDW(n) "buffer_load_dwordx4 %" #n ", %[v0], %[rm], %[so] offen nt\n\ts_add_u32 %[so], %[so], %[ld]\n\t"
    asm volatile("s_cmp_eq_u32 %[ld], 0\n\ts_cbranch_scc1 1f\n\ts_mov_b32 %[so], 0\n\t"
                 "buffer_load_dword %9, off, %[r0], 0\n\tbuffer_load_dwordx4 %10, %[vi], %[ri], 0 offen\n\t"
                 "buffer_load_ushort %11, %[vz], %[ri], 0 offen\n\t" LDW(0) LDW(1) LDW(2) LDW(3) LDW(4) LDW(5) LDW(6) LDW(7)
                 "buffer_load_dwordx4 %8, %[v8], %[rm], 0 offen nt\n\tbuffer_store_dword %[t0], %[vt], %[r0], 0 offen sc1\n"
                 "1:\n\ts_waitcnt lgkmcnt(0)\n\ts_barrier"
                 : "=&v"(w[0]), "=&v"(w[1]), "=&v"(w[2]), "=&v"(w[3]), "=&v"(w[4]), "=&v"(w[5]), "=&v"(w[6]), "=&v"(w[7]), "=&v"(w[8]),
                   "=&v"(ep), "=&v"(ci), "=&v"(cz), [so] "=&s"(so)
                 : [r0] "s"(r0), [ri] "s"(ri), [rm] "s"(rm), [v0] "v"(v0), [v8] "v"(v8), [vi] "v"(vi), [vz] "v"(vz), [ld] "s"(ld),
                   [vt] "v"(vt), [t0] "v"(t0)
                 : "scc", "memory");
  }

  if (wave >= 4) {
    const int s = wave - 4;
    const args_t A = args_ptr();
    const u16 *Kc = A->Kc, *Vc = A->Vc, *wg = A->wg, *wu = A->wu; char* ws = A->ws;
    const rsrc_t rws = mk(ws, WS_END);
    const u64 ka = (u64)(Kc + ((size_t)head << 20)), va = (u64)(Vc + ((size_t)head << 20)), wa = (u64)ws;
    const v4u rk = {(unsigned)ka, (unsigned)(ka >> 32) & 0xffffu, 1u << 21, 0x00020000u};
    const v4u rv = {(unsigned)va, (unsigned)(va >> 32) & 0xffffu, 1u << 21, 0x00020000u};
    const v4u rt = {(unsigned)wa, (unsigned)(wa >> 32) & 0xffffu, OFF_P, 0x00020000u};
    const unsigned vo = lane * 16, vt = lane == 0 ? OFF_T + head * 256 : OOB;
    const unsigned rot = ROTX, p0 = rot + k * 8 + s;
    v4u Kr[3][4], Vr[3][4];
    asm volatile("s_barrier" :: "s"(rk), "s"(rv), "s"(p0), "v"(vo) : "memory");
    STAMP(10);
#define S_(x) #x
#define S(x) S_(x)
#define LD1(n, off, T) "s_waitcnt vmcnt(" S(T) "-1)\n\tbuffer_load_dwordx4 %" #n ", %[vo], %[rs], %[so] offen offset:" #off " nt\n\t"
#define VOS [vo] "v"(vo)
#define CHUNK(c) (((c) & 511) << 12)
#define LD4(T) LD1(0, 0, T) LD1(1, 1024, T) LD1(2, 2048, T) LD1(3, 3072, T)
#define LD4E(T) "s_cmp_ge_i32 %[tn], 256-" S(ENDX) "\n\ts_cbranch_scc1 2f\n\t" LD4(T) "s_branch 1f\n2:\n\t" LD4(40)
#define LL1(off) "s_waitcnt vmcnt(" S(TF) "-1)\n\tbuffer_load_dwordx4 %[vo], %[rs], %[so] offen offset:" #off " nt lds\n\t"
#pragma unroll
    for (int i = 0; i < NL; ++i) {
      const unsigned so = CHUNK(p0 + 64 * i), lb = (unsigned)(size_t)(__attribute__((address_space(3))) void*)L.stg[s][i];
      asm volatile("s_mov_b32 m0, %[lb]\n\t" LL1(0) LL1(1024) LL1(2048) LL1(3072) :: VOS, [rs] "s"(rk), [so] "s"(so), [lb] "s"(lb) : "m0", "memory");
      PROF();
      asm volatile("s_mov_b32 m0, %[lb]\n\t" LL1(0) LL1(1024) LL1(2048) LL1(3072) :: VOS, [rs] "s"(rv), [so] "s"(so), [lb] "s"(lb + 4096) : "m0", "memory");
      PROF();
    }
#pragma unroll
    for (int i = 0; i < 3; ++i) {
      const unsigned so = CHUNK(p0 + 64 * (NL + i));
      asm volatile(LD4(TF) : "=&v"(Kr[i][0]), "=&v"(Kr[i][1]), "=&v"(Kr[i][2]), "=&v"(Kr[i][3]) : VOS, [rs] "s"(rk), [so] "s"(so));
      PROF();
      asm volatile(LD4(TF) : "=&v"(Vr[i][0]), "=&v"(Vr[i][1]), "=&v"(Vr[i][2]), "=&v"(Vr[i][3]) : VOS, [rs] "s"(rv), [so] "s"(so));
      PROF();
      if (i == 0) STAMP(11);
    }
    STAMP(6);
    const int pi = (s & 3) * 41 + lane;
    const unsigned pv = (lane < 41 && pi < 162) ? (18 * r + pi / 9) * 9216 + x * 1152 + (pi % 9) * 128 : OOB;
    const u64 pa = (u64)(s < 4 ? wg : wu);
    const v4u rp = {(unsigned)pa, (unsigned)(pa >> 32) & 0xffffu, 576u * 9216, 0x00020000u};
    lds_wait(&L.cnt[4], 1);
    const v4u qv = *(const v4u*)&L.qs[(lane & 15) * 4];
    const unsigned q0 = qv[0], q1 = qv[1], q2 = qv[2], q3 = qv[3];
    STAMP(7);
    float m = -INFINITY, l = 0.f;
    v2f acc[4] = {{0.f, 0.f}, {0.f, 0.f}, {0.f, 0.f}, {0.f, 0.f}};
    unsigned tk = k * 8 + s - 64 + 64 * NL;
    int rem = 3;
    const int lim = s >= 4 ? SLIM : 255;
#define TICKET "v_mov_b32 %[tk], 0x400\n\ts_cmp_ge_i32 %[tn], %[lim]\n\ts_cbranch_scc1 4f\n\tv_mov_b32 %[tk], 1\n\tbuffer_atomic_add %[tk], %[vt], %[rt], 0 offen sc0 sc1\n4:\n\t"
#define PREF "s_cmp_eq_u32 %[rem], " S(PAT) "\n\ts_cbranch_scc0 3f\n\ts_mov_b32 m0, %[sink]\n\ts_nop 1\n\tbuffer_load_dword %[pv], %[rp], 0 offen nt lds\n\tbuffer_load_dword %[pv], %[rp], 0 offen sc1 lds\n3:"
#define LASTW 0
#define SOFT \
        SB();                                                                                       \
        _Pragma("unroll") for (int t = 0; t < 4; ++t) sc[t] += dppf<X1>(sc[t]);                     \
        SB();                                                                                       \
        _Pragma("unroll") for (int t = 0; t < 4; ++t) sc[t] += dppf<X2>(sc[t]);                     \
        SB();                                                                                       \
        _Pragma("unroll") for (int t = 0; t < 4; ++t) sc[t] += dppf<R4>(sc[t]);                     \
        SB();                                                                                       \
        _Pragma("unroll") for (int t = 0; t < 4; ++t) sc[t] += dppf<R8>(sc[t]);                     \
        SB();                                                                                       \
        _Pragma("unroll") for (int t = 0; t < 4; ++t) sc[t] = rbf(rbf(sc[t]) * 0.08838834764831845f); \
        const float mn = fmaxf(fmaxf(fmaxf(sc[0], sc[1]), fmaxf(sc[2], sc[3])), m);                 \
        const float al = ex2((m - mn) * LOG2E);                                                     \
        m = mn;                                                                                     \
        float p[4];                                                                                 \
        _Pragma("unroll") for (int t = 0; t < 4; ++t) p[t] = ex2((sc[t] - mn) * LOG2E);             \
        l = l * al + ((p[0] + p[1]) + (p[2] + p[3]));                                               \
        _Pragma("unroll") for (int j = 0; j < 4; ++j) acc[j] *= al;
#define STEP(i)                                                                                     \
      {                                                                                             \
        float sc[4];                                                                                \
        asm volatile("" : "+v"(Kr[i][0]), "+v"(Kr[i][1]), "+v"(Kr[i][2]), "+v"(Kr[i][3]));          \
        _Pragma("unroll") for (int t = 0; t < 4; ++t) {                                             \
          const v4u kk = Kr[i][t];                                                                  \
          sc[t] = dot2(kk[3], q3, dot2(kk[2], q2, dot2(kk[1], q1, dot2(kk[0], q0, 0.f))));          \
        }                                                                                           \
        asm volatile("s_waitcnt vmcnt(4)" : "+v"(tk), "+v"(sc[0]), "+v"(sc[1]), "+v"(sc[2]), "+v"(sc[3])); \
        const int tn = __builtin_amdgcn_readfirstlane(tk);                                          \
        rem = tn < 256 ? 3 : rem - 1;                                                               \
        const unsigned so = CHUNK(tn + 256 + rot);                                         \
        asm volatile("s_cmp_eq_u32 %[rem], 3\n\ts_cbranch_scc0 1f\n\ts_nop 1\n\t" LD4E(TS) "1:"      \
                     : "+v"(Kr[i][0]), "+v"(Kr[i][1]), "+v"(Kr[i][2]), "+v"(Kr[i][3])               \
                     : VOS, [rs] "s"(rk), [so] "s"(so), [rem] "s"(rem), [tn] "s"(tn) : "scc"); \
        if (rem == 3) PROF();                                                                       \
        if (rem == 2) {                                                                             \
          if (lane == 0) __hip_atomic_fetch_add(&L.cnt[5], 1u, __ATOMIC_RELAXED, __HIP_MEMORY_SCOPE_WORKGROUP); \
          __builtin_amdgcn_s_setprio(3);                                                            \
          STAMP(8);                                                                                 \
        }                                                                                           \
        SOFT                                                                                        \
        asm volatile("s_cmp_eq_u32 %[rem], 0\n\ts_cbranch_scc0 1f\n\ts_waitcnt vmcnt(" S(LASTW) ")\n1:\n\t" PREF \
                     : "+v"(Vr[i][0]), "+v"(Vr[i][1]), "+v"(Vr[i][2]), "+v"(Vr[i][3]), "+v"(l),     \
                       "+v"(acc[0]), "+v"(acc[1]), "+v"(acc[2]), "+v"(acc[3])                       \
                     : [rem] "s"(rem), [pv] "v"(pv), [rp] "s"(rp), [sink] "s"(sink) : "scc", "m0");  \
        _Pragma("unroll") for (int t = 0; t < 4; ++t) {                                             \
          const v4u vv = Vr[i][t];                                                                  \
          const v2f pp = {p[t], p[t]};                                                              \
          _Pragma("unroll") for (int j = 0; j < 4; ++j) {                                           \
            const v2f e = {lo16(vv[j]), hi16(vv[j])};                                               \
            acc[j] = __builtin_elementwise_fma(pp, e, acc[j]);                                      \
          }                                                                                         \
        }                                                                                           \
        asm volatile("s_cmp_eq_u32 %[rem], 3\n\ts_cbranch_scc0 1f\n\t" TICKET LD4E(TS) "1:"          \
                     : "+v"(Vr[i][0]), "+v"(Vr[i][1]), "+v"(Vr[i][2]), "+v"(Vr[i][3]), [tk] "+v"(tk), \
                       "+v"(acc[0]), "+v"(acc[1]), "+v"(acc[2]), "+v"(acc[3])                       \
                     : VOS, [rs] "s"(rv), [so] "s"(so), [rem] "s"(rem), [vt] "v"(vt), [rt] "s"(rt), [tn] "s"(tn), [lim] "s"(lim) : "scc", "memory"); \
        if (rem == 3 || rem == 0) PROF(); \
        if (rem == 0) break;                                                                        \
      }
    _Pragma("unroll 1") for (int c = 0; c < NL; ++c) {
      float sc[4];
#pragma unroll
      for (int t = 0; t < 4; ++t) {
        const v4u kk = L.stg[s][c][t][lane];
        sc[t] = dot2(kk[3], q3, dot2(kk[2], q2, dot2(kk[1], q1, dot2(kk[0], q0, 0.f))));
      }
      SOFT
      SB();
#pragma unroll
      for (int t = 0; t < 4; ++t) {
        const v4u vv = L.stg[s][c][4 + t][lane];
        const v2f pp = {p[t], p[t]};
#pragma unroll
        for (int j = 0; j < 4; ++j) { const v2f e = {lo16(vv[j]), hi16(vv[j])}; acc[j] = __builtin_elementwise_fma(pp, e, acc[j]); }
      }
    }
    SB();
    _Pragma("unroll 1") for (int it = 0; it < 200; ++it) { STEP(0) STEP(1) STEP(2) }
#undef STEP
    STAMP(4);
    const float M = mx32(mx16(m));
    const float w = ex2((m - M) * LOG2E);
    l *= w; l = rs16(l, l); l = rs32(l, l);
    float a[8];
#pragma unroll
    for (int j = 0; j < 4; ++j) { a[2 * j] = acc[j].x * w; a[2 * j + 1] = acc[j].y * w; }
    float y[4];
#pragma unroll
    for (int j = 0; j < 4; ++j) y[j] = rs32(a[j], a[j + 4]);
    const int d0 = (lane & 15) * 8 + (lane >> 4) * 2;
    L.rec_o[s][d0 + (d0 * 171 >> 9)] = rs16(y[0], y[2]);
    L.rec_o[s][d0 + 1 + ((d0 + 1) * 171 >> 9)] = rs16(y[1], y[3]);
    if (lane == 0) { v2f ml = {M, l}; *(v2f*)&L.rec_ml[s][0] = ml; }
    WAIT_LGKM0();
    unsigned old = 0;
    if (lane == 0) old = __hip_atomic_fetch_add(&L.cnt[3], 1u, __ATOMIC_RELAXED, __HIP_MEMORY_SCOPE_WORKGROUP);
    old = __builtin_amdgcn_readfirstlane(old);
    MEMFENCE();
    if (old == 7) {
      float ms[8], ls[8];
      const unsigned tgs = L.cnt[7];
#pragma unroll
      for (int j = 0; j < 4; ++j) { v4f t = *(const v4f*)&L.rec_ml[2 * j][0]; ms[2 * j] = t.x; ls[2 * j] = t.y; ms[2 * j + 1] = t.z; ls[2 * j + 1] = t.w; }
      v4f o[8];
#pragma unroll
      for (int j = 0; j < 8; ++j) o[j] = *(const v4f*)&L.rec_o[j][(lane < 43 ? lane : 0) * 4];
      const float MM = fmaxf(fmaxf(fmaxf(ms[0], ms[1]), fmaxf(ms[2], ms[3])), fmaxf(fmaxf(ms[4], ms[5]), fmaxf(ms[6], ms[7])));
      float LL = 0.f; v4f oo = {0.f, 0.f, 0.f, 0.f};
#pragma unroll
      for (int j = 0; j < 8; ++j) { const float e = ex2((ms[j] - MM) * LOG2E); LL += e * ls[j]; oo += o[j] * e; }
      const v4u pv = {asu(lane < 43 ? oo.x : MM), asu(lane < 43 ? oo.y : LL), lane < 42 ? asu(oo.z) : 0u, tgs};
      const unsigned off = lane < 44 ? OFF_AO + b * 768 + lane * 16 : OOB;
      __builtin_amdgcn_raw_buffer_store_b128(pv, rws, off, 0, AUX_SC1);
      if (lane == 0) __hip_atomic_fetch_add(&L.cnt[3], 1u, __ATOMIC_RELAXED, __HIP_MEMORY_SCOPE_WORKGROUP);
      STAMP(5);
    }
    if (PAT == -2 && s >= 4) {
      __builtin_amdgcn_s_setprio(0);
      const u64 ga = (u64)wg, ua = (u64)wu;
      const v4u rg = {(unsigned)ga, (unsigned)(ga >> 32) & 0xffffu, 576u * 9216, 0x00020000u};
      const v4u ru = {(unsigned)ua, (unsigned)(ua >> 32) & 0xffffu, 576u * 9216, 0x00020000u};
      lds_wait(&L.cnt[PTRIG > 8 ? 3 : 5], (PTRIG > 8 ? PTRIG - 8 : PTRIG) - PSTEP * (7 - s) - (r < 9 ? WOE : 0));
      if (PDEL) __builtin_amdgcn_s_sleep(PDEL);
      STAMP(9);
      asm volatile("s_mov_b32 m0, %2\n\ts_nop 1\n\tbuffer_load_dword %0, %1, 0 offen nt lds\n\tbuffer_load_dword %0, %1, 0 offen sc1 lds\n\ts_waitcnt vmcnt(0)"
                   :: "v"(pv), "s"(rg), "s"(sink) : "m0");
      asm volatile("s_mov_b32 m0, %2\n\ts_nop 1\n\tbuffer_load_dword %0, %1, 0 offen nt lds\n\tbuffer_load_dword %0, %1, 0 offen sc1 lds"
                   :: "v"(pv), "s"(ru), "s"(sink) : "m0");
    }
    WAIT_VM0();
    FLUSH();
    return;
  }

  const rsrc_t rws = mk(ws, WS_END);
  if (wave == 3) {
    const int hrow = (lane & 8) ? 64 + 3 * (lane & 3) + ((lane & 16) ? 2 : lane >> 5) : 8 * c8 + ((lane >> 3) & 6);
    const rsrc_t rin = mk(xin, 1152);
    const unsigned e3p = LD32(rws, 0, 0);
    const v4u xa = LD128(rin, lane * 16, 0), xb = LD128(rin, lane * 16 + 1024, 0);
    const unsigned xp = LD32(rin, (72 * x + (hrow & ~1)) * 2, 0);
    const unsigned tg = __builtin_amdgcn_readfirstlane(e3p) + TAG_STEP;
    float s = 0.f, q = 0.f;
#pragma unroll
    for (int d = 0; d < 4; ++d) {
      float e0 = lo16(xa[d]), e1 = hi16(xa[d]), e2 = lo16(xb[d]), e3 = hi16(xb[d]);
      s += (e0 + e1) + (e2 + e3); q += (e0 * e0 + e1 * e1) + (e2 * e2 + e3 * e3);
    }
    s = wave_sum(s); q = wave_sum(q);
    float m, rsd; ln_stats(s, q, m, rsd);
    float n0 = rbf(rbf(lo16(xp >> ((hrow & 1) * 16)) - m) * rsd), n1 = rbf(rbf(hi16(xp) - m) * rsd);
    PIN(n0); PIN(n1);
    if (PD1) __builtin_amdgcn_s_sleep(PD1);
    STAMP(1);
    const unsigned vp = OFF_P + (x * 8 + r8) * 896 + c8 * 16;
    v4u g[7];
    _Pragma("unroll 1") for (int it = 0; it < GCAP; ++it) {
      unsigned bad = 0;
#pragma unroll
      for (int t = 0; t < 6; ++t) g[t] = LD128(rws, vp + t * 128, AUX_SC1);
      g[6] = LD128(rws, vp + ((lane & 3) == 3 ? 752 : 768), AUX_SC1);
#pragma unroll
      for (int t = 0; t < 7; ++t) bad |= g[t][3] ^ tg;
      if (all_true(bad == 0)) break;
      MEMFENCE();
    }
    STAMP(2);
#define G3(t, j) asf(g[(t) + (j) / 3][(j) % 3])
    float sa[2], ca[2];
#pragma unroll
    for (int e = 0; e < 2; ++e) {
      sa[e] = rs16(rs32(G3(0, e), G3(0, e + 4)), rs32(G3(0, e + 2), G3(0, e + 6)));
      ca[e] = rs16(rs32(G3(3, e), G3(3, e + 4)), rs32(G3(3, e + 2), G3(3, e + 6)));
      sa[e] += dppf<R8>(sa[e]); ca[e] += dppf<R8>(ca[e]);
    }
    float z = rs16(rs32(G3(6, 0), G3(6, 1)), rs32(G3(6, 2), G3(6, 2)));
    z += dppf<R8>(z);
    const float zc = dppf<R4>(z);
    if (lane & 8) { sa[0] = z; ca[0] = zc; }
    const unsigned hh = pack2(modulate(n0, rbf(ca[0]), rbf(sa[0])), modulate(n1, rbf(ca[1]), rbf(sa[1])));
    if ((lane & 8) == 0) L.hs[hrow >> 1] = hh;
    else if ((lane & 4) == 0) ((u16*)L.hs)[hrow] = (u16)hh;
    WAIT_LGKM0();
    if (lane == 0) __hip_atomic_store(&L.cnt[0], 1u, __ATOMIC_RELAXED, __HIP_MEMORY_SCOPE_WORKGROUP);
    if (PD2) __builtin_amdgcn_s_sleep(PD2);
    STAMP(3);
    const unsigned vq = OFF_Q + (head * 8 + r8) * 768 + c8 * 16;
    _Pragma("unroll 1") for (int it = 0; it < GCAP; ++it) {
      unsigned bad = 0;
#pragma unroll
      for (int t = 0; t < 6; ++t) g[t] = LD128(rws, vq + t * 128, AUX_SC1);
#pragma unroll
      for (int t = 0; t < 6; ++t) bad |= g[t][3] ^ tg;
      if (all_true(bad == 0)) break;
      MEMFENCE();
    }
#define Q3(j) rs32(asf(g[(j) / 3 * 2][(j) % 3]), asf(g[(j) / 3 * 2 + 1][(j) % 3]))
    float qq[4];
#pragma unroll
    for (int e = 0; e < 4; ++e) { const float t = rs16(Q3(e), Q3(e + 4)); qq[e] = t + dppf<R8>(t); }
    const v2u qp = {pack2(qq[0], qq[1]), pack2(qq[2], qq[3])};
    *(v2u*)&L.qs[((lane >> 5) * 8 + c8) * 4 + ((lane >> 3) & 2)] = qp;
    L.cnt[7] = tg;
    WAIT_LGKM0();
    if (lane == 0) __hip_atomic_store(&L.cnt[4], 1u, __ATOMIC_RELAXED, __HIP_MEMORY_SCOPE_WORKGROUP);
    STAMP(4);
  } else if (!is_mod || ffn) {
    asm volatile("s_waitcnt vmcnt(9)" : "+v"(ep), "+v"(ci), "+v"(cz) :: "memory");
    const unsigned tg0 = __builtin_amdgcn_readfirstlane(ep) + TAG_STEP;
    const int u = lane >> 4;
    unsigned vo;
    Hv H;
    if (is_mod) {
      const int o = (r - 9) * 8 + c8, kind = o >= 72, oc = o - 72 * kind, gr = oc / 9, i = oc - 9 * gr;
      vo = OFF_P + (gr * 8 + x) * 896 + (i < 8 ? (3 * kind + u) * 128 + i * 16 : 768 + (4 * kind + u) * 16);
      hv_silu(H, ci, cz);
    } else {
      vo = OFF_Q + b * 768 + u * 256 + ((wave - 1) * 8 + c8) * 16;
    }
    if ((lane & 8) || lane >= 48) vo = OOB;
    PIN(vo);
    asm volatile("s_waitcnt vmcnt(0)" : "+v"(w[0]), "+v"(w[1]), "+v"(w[2]), "+v"(w[3]), "+v"(w[4]), "+v"(w[5]), "+v"(w[6]), "+v"(w[7]), "+v"(w[8]) :: "memory");
    Slab S;
    slab_make(S, w);
    STAMP(1);
    if (!is_mod) {
      lds_wait(&L.cnt[0], 1);
      const v4u hp = *(const v4u*)&L.hs[4 * r8];
      H.za = ((const u16*)L.hs)[64 + r8]; H.zb = H.za << 16;
#pragma unroll
      for (int p = 0; p < 4; ++p) H.p[p] = hp[p];
    }
    STAMP(2);
    float a[8]; slab_dot(a, S, H);
    float e0 = rs16(rs32(a[0], a[6]), rs32(a[3], a[3])), e1 = rs16(rs32(a[1], a[7]), rs32(a[4], a[4])), e2 = rs16(rs32(a[2], a[2]), rs32(a[5], a[5]));
    e0 += dppf<R8>(e0); e1 += dppf<R8>(e1); e2 += dppf<R8>(e2);
    const v4u pv = {asu(e0), asu(e1), asu(e2), tg0};
    __builtin_amdgcn_raw_buffer_store_b128(pv, rws, vo, 0, AUX_SC1);
    STAMP(3);
  }
  const int qr = lane >> 2, qc = lane & 3;
  const unsigned mcopy = OFF_M + x * M_COPY;
  if (r < 9) {
    const rsrc_t rw = mk(wo + (size_t)(512 * x + 128 * wave) * 576 + 64 * r, 127 * 1152 + 128);
    v4u wo_w[16];
    if (WTRIG) lds_wait(&L.cnt[5], WTRIG);
    else if (LATEQ) lds_wait(&L.cnt[4], 1);
#pragma unroll
    for (int t = 0; t < 16; ++t) wo_w[t] = LD128(rw, (16 * r8 + t) * 1152 + c8 * 16, AUX_NT);
    unsigned P[8][8];
#pragma unroll
    for (int p = 0; p < 8; ++p) {
      interleave(P[p], wo_w[2 * p], wo_w[2 * p + 1]);
#pragma unroll
      for (int d = 0; d < 8; ++d) PIN(P[p][d]);
    }
    lds_wait(&L.cnt[4], 1);
    const unsigned tg = __builtin_amdgcn_readfirstlane(L.cnt[7]);
    lds_wait(&L.cnt[3], 9);
    STAMP(5);
    const unsigned va = OFF_AO + (4 * x + wave) * 8 * 768 + (lane < 43 ? lane : 43) * 16, va0 = va + (lane < 48 ? 0 : c8 * 768);
    v4u o[8];
    _Pragma("unroll 1") for (int it = 0; it < GCAP; ++it) {
      unsigned bad = 0;
#pragma unroll
      for (int j = 0; j < 8; ++j) o[j] = LD128(rws, j ? va + j * 768 : va0, AUX_SC1);
#pragma unroll
      for (int j = 0; j < 8; ++j) bad |= o[j][3] ^ tg;
      if (all_true(bad == 0)) break;
      MEMFENCE();
    }
    STAMP(6);
    const float mi = asf(o[0][0]);
    float M = fmaxf(mi, dppf<X1>(mi)); M = fmaxf(M, dppf<X2>(M)); M = fmaxf(M, dppf<HM>(M));
    const float e = ex2((mi - M) * LOG2E);
    float Ls = e * asf(o[0][1]); Ls += dppf<X1>(Ls); Ls += dppf<X2>(Ls); Ls += dppf<HM>(Ls);
    float oo[3] = {0.f, 0.f, 0.f};
#pragma unroll
    for (int j = 0; j < 8; ++j) {
      const float ej = asf(__builtin_amdgcn_readlane(asu(e), 48 + j));
#pragma unroll
      for (int d = 0; d < 3; ++d) oo[d] = fmaf(ej, asf(o[j][d]), oo[d]);
    }
    const float rl = asf(__builtin_amdgcn_readlane(asu(rcp(Ls)), 48));
    {
      const unsigned p01 = pack2(oo[0] * rl, oo[1] * rl), p2 = pack2(oo[2] * rl, 0.f);
      u16* const ot = (u16*)L.ob[wave] + 3 * lane;
      ot[0] = (u16)p01; ot[1] = (u16)(p01 >> 16); ot[2] = (u16)p2;
    }
    WAIT_LGKM0();
    const v4u oa = *(const v4u*)&L.ob[wave][r8 * 8], ob = *(const v4u*)&L.ob[wave][r8 * 8 + 4];
    float acc[8] = {0.f, 0.f, 0.f, 0.f, 0.f, 0.f, 0.f, 0.f};
#pragma unroll
    for (int p = 0; p < 8; ++p) {
      const unsigned hp = p < 4 ? oa[p & 3] : ob[p & 3];
#pragma unroll
      for (int d = 0; d < 8; ++d) acc[d] = dot2(P[p][d], hp, acc[d]);
    }
    float y[4];
#pragma unroll
    for (int j = 0; j < 4; ++j) y[j] = rs32(acc[j], acc[j + 4]);
    float z0 = rs16(y[0], y[2]), z1 = rs16(y[1], y[3]);
    z0 += dppf<R8>(z0); z1 += dppf<R8>(z1);
    if ((lane & 8) == 0) { v2f z = {z0, z1}; *(v2f*)&L.hp[wave][c8 * 8 + (lane >> 4) * 2] = z; }
    wg_barrier();
    if (wave == 0) {
      const int c4 = (lane & 15) * 4;
      const v4f t = (*(const v4f*)&L.hp[0][c4] + *(const v4f*)&L.hp[1][c4]) + (*(const v4f*)&L.hp[2][c4] + *(const v4f*)&L.hp[3][c4]);
      __builtin_amdgcn_raw_buffer_store_b128(seal(t, tg), rws, OFF_B + (lane >> 4) * 18432 + x * 2304 + r * 256 + c4 * 4, 0, AUX_SC1);
      STAMP(7);
    }
  } else if (r < 27) {
    const int f = 18 * x + (r - 9);
    const bool act3 = qc < 3;
    const int un4 = 144 * wave + (qc < 2 ? 8 * qr + 4 * qc : 128 + (qr & 12));
    const v2u xi = LD64(mk(xin, 1152), act3 ? un4 * 2 : OOB, 0);
    float xv[4], ga[4], sb[4], cb[4];
    lds_wait(&L.cnt[4], 1);
    const unsigned tg = __builtin_amdgcn_readfirstlane(L.cnt[7]);
    {
      for (int i = 0; i < MDEL; ++i) __builtin_amdgcn_s_sleep(127);
      const unsigned vm = act3 ? mcopy + un4 * 4 : OOB;
      v4u g[3];
      _Pragma("unroll 1") for (int it = 0; it < GCAP; ++it) {
        unsigned bad = 0;
#pragma unroll
        for (int j = 0; j < 3; ++j) g[j] = LD128(rws, vm + j * 2304, AUX_SC1);
#pragma unroll
        for (int j = 0; j < 3; ++j) bad |= (g[j][1] ^ tg) | (g[j][3] ^ tg);
        if (all_true(bad == 0 || !act3)) break;
        __builtin_amdgcn_s_sleep(32);
        MEMFENCE();
      }
      STAMP(4);
#pragma unroll
      for (int e = 0; e < 4; ++e) {
        const bool on = qc < 2 || (qc == 2 && (qr & 3) == e);
        const float a = (e & 1) ? hi16(xi[e >> 1]) : lo16(xi[e >> 1]), g0 = (e & 1) ? hi16(g[0][e & 2]) : lo16(g[0][e & 2]);
        const float g1 = (e & 1) ? hi16(g[1][e & 2]) : lo16(g[1][e & 2]), g2 = (e & 1) ? hi16(g[2][e & 2]) : lo16(g[2][e & 2]);
        xv[e] = on ? a : 0.f; ga[e] = on ? g0 : 0.f; sb[e] = on ? g1 : 0.f; cb[e] = on ? rbf(1.f + g2) : 0.f;
        PIN(xv[e]); PIN(ga[e]); PIN(sb[e]); PIN(cb[e]);
      }
    }
    __builtin_amdgcn_s_setprio(0);
    const rsrc_t rg = mk(wg + f * 32, 575 * 9216 + 64), ru = mk(wu + f * 32, 575 * 9216 + 64);
    const rsrc_t rd = mk(wd + (size_t)(32 * f + 8 * wave) * 576, 8 * 1152);
    v4u w1[9], w2[9], w3[8]; unsigned w4[8];
    SB(); lds_wait(&L.cnt[DTRIG > 8 ? 3 : 5], DTRIG > 8 ? DTRIG - 8 : DTRIG); if (DDEL) __builtin_amdgcn_s_sleep(DDEL); STAMP(5); SB();
#pragma unroll
    for (int t = 0; t < 8; ++t) { w3[t] = LD128(rd, t * 1152 + lane * 16, AUX_NT); w4[t] = LD16(rd, t * 1152 + 1024 + lane * 2, AUX_NT); }
    SB(); lds_wait(&L.cnt[3], GTRIG);
    if (GDEL) __builtin_amdgcn_s_sleep(GDEL);
    STAMP(9); SB();
    const unsigned vg = (144 * wave + 8 * qr) * 9216 + qc * 16, vz = (144 * wave + 128 + qr) * 9216 + qc * 16;
#pragma unroll
    for (int t = 0; t < 9; ++t) w1[t] = LD128(rg, t < 8 ? vg + t * 9216 : vz, AUX_NT);
#pragma unroll
    for (int t = 0; t < 9; ++t) w2[t] = LD128(ru, t < 8 ? vg + t * 9216 : vz, AUX_NT);
    SB();
    unsigned Dp[4][8], Dt[4];
#pragma unroll
    for (int p = 0; p < 4; ++p) {
      interleave(Dp[p], w3[2 * p], w3[2 * p + 1]); Dt[p] = w4[2 * p] | (w4[2 * p + 1] << 16);
#pragma unroll
      for (int d = 0; d < 8; ++d) PIN(Dp[p][d]);
      PIN(Dt[p]);
    }
    Slab G, U; slab_make(G, w1); slab_make(U, w2);
    STAMP(6);
    const unsigned vb = act3 ? OFF_B + (x & 3) * 18432 + un4 * 4 : OOB;
    v4u yb[8];
    _Pragma("unroll 1") for (int it = 0; it < GCAP; ++it) {
#pragma unroll
      for (int j = 0; j < 8; ++j) yb[j] = LD128(rws, vb + j * 2304, AUX_SC1);
      unsigned bad = 0;
#pragma unroll
      for (int j = 0; j < 8; ++j) bad |= unseal(yb[j], tg);
      if (all_true((bad & 0xff) == 0 || !act3)) break;
      MEMFENCE();
    }
    STAMP(7);
    float xr[4];
#pragma unroll
    for (int j = 0; j < 4; ++j) {
      const float t = ((asf(yb[0][j]) + asf(yb[1][j])) + (asf(yb[2][j]) + asf(yb[3][j]))) + ((asf(yb[4][j]) + asf(yb[5][j])) + (asf(yb[6][j]) + asf(yb[7][j])));
      xr[j] = rbf(xv[j] + rbf(ga[j] * rbf(t)));
    }
    float s = (xr[0] + xr[1]) + (xr[2] + xr[3]), q = (xr[0] * xr[0] + xr[1] * xr[1]) + (xr[2] * xr[2] + xr[3] * xr[3]);
    s = wave_sum(s); q = wave_sum(q);
    if (lane == 0) { v2f t = {s, q}; *(v2f*)&L.st[wave][0] = t; }
    wg_barrier();
    {
      const v4f t0 = *(const v4f*)&L.st[0][0], t1 = *(const v4f*)&L.st[2][0];
      s = (t0.x + t0.z) + (t1.x + t1.z); q = (t0.y + t0.w) + (t1.y + t1.w);
    }
    float m, rsd; ln_stats(s, q, m, rsd);
#pragma unroll
    for (int j = 0; j < 4; ++j) xr[j] = rbf(rbf(rbf(xr[j] - m) * rsd) * cb[j]) + sb[j];
    const unsigned h01 = pack2(xr[0], xr[1]), h23 = pack2(xr[2], xr[3]);
    Hv H;
    H.p[0] = dppu<0x00>(h01); H.p[1] = dppu<0x00>(h23); H.p[2] = dppu<0x55>(h01); H.p[3] = dppu<0x55>(h23);
    { const unsigned hz = dppu<0xAA>(h01 | h23); H.zb = (hz | (hz << 16)) & 0xffff0000u; H.za = H.zb >> 16; }
    float ag[8], au[8]; slab_dot(ag, G, H); slab_dot(au, U, H);
    float y[8];
#pragma unroll
    for (int j = 0; j < 8; ++j) y[j] = rs32(ag[j], au[j]);
    v4f z;
#pragma unroll
    for (int j = 0; j < 4; ++j) { float t = rs16(y[j], y[j + 4]); t += dppf<R4>(t); t += dppf<R8>(t); z[j] = t; }
    if ((lane & 12) == 0) *(v4f*)&L.hp[wave][(lane >> 5) * 32 + qc * 8 + ((lane >> 4) & 1) * 4] = z;
    wg_barrier();
    const int un = 8 * wave + (lane & 7);
    const float g = rbf((L.hp[0][un] + L.hp[1][un]) + (L.hp[2][un] + L.hp[3][un]));
    const float up = rbf((L.hp[0][32 + un] + L.hp[1][32 + un]) + (L.hp[2][32 + un] + L.hp[3][32 + un]));
    const float av = rbf(silu(g)) * up;
    const unsigned ap = pack2(av, dppf<X1>(av));
    float d8[8] = {0.f, 0.f, 0.f, 0.f, 0.f, 0.f, 0.f, 0.f}, dt = 0.f;
#pragma unroll
    for (int p = 0; p < 4; ++p) {
      const unsigned a2 = __builtin_amdgcn_readlane(ap, 2 * p);
#pragma unroll
      for (int d = 0; d < 8; ++d) d8[d] = dot2(Dp[p][d], a2, d8[d]);
      dt = dot2(Dt[p], a2, dt);
    }
    { v4f t0 = {d8[0], d8[1], d8[2], d8[3]}, t1 = {d8[4], d8[5], d8[6], d8[7]};
      *(v4f*)&L.dn[wave][lane * 8] = t0; *(v4f*)&L.dn[wave][lane * 8 + 4] = t1; L.dn[wave][512 + lane] = dt; }
    wg_barrier();
    {
      const int c4 = 144 * wave + (lane < 36 ? lane : 0) * 4;
      const v4f t = (*(const v4f*)&L.dn[0][c4] + *(const v4f*)&L.dn[1][c4]) + (*(const v4f*)&L.dn[2][c4] + *(const v4f*)&L.dn[3][c4]);
      __builtin_amdgcn_raw_buffer_store_b128(seal(t, tg), rws, lane < 36 ? OFF_C + f * 2304 + c4 * 4 : OOB, 0, AUX_SC1);
    }
    STAMP(8);
  } else {
    const int j = (r - 27) * 8 + x;
    if (j >= 36) { FLUSH(); return; }
    unsigned tg;
    {
      const rsrc_t rw = mk(wmod + 144 * wave * 3456 + 1152 + 64 * j, 143 * 6912 + 128), rin = mk(cin + 144 * wave, 288);
      if (LATEQ) lds_wait(&L.cnt[4], 1);
      v4u ci[2], w[2][9]; unsigned cz[2];
#pragma unroll
      for (int u = 0; u < 2; ++u) { ci[u] = LD128(rin, u * 144 + r8 * 16, 0); cz[u] = LD16(rin, u * 144 + 128 + r8 * 2, 0); }
#pragma unroll
      for (int u = 0; u < 2; ++u)
#pragma unroll
        for (int t = 0; t < 9; ++t) w[u][t] = LD128(rw, (72 * u + (t < 8 ? 8 * r8 + t : 64 + r8)) * 6912 + c8 * 16, AUX_NT);
      SB();
      v4f y = {0.f, 0.f, 0.f, 0.f};
#pragma unroll
      for (int u = 0; u < 2; ++u) {
        Hv H; Slab S; float acc[8];
        hv_silu(H, ci[u], cz[u]); slab_make(S, w[u]); slab_dot(acc, S, H);
        y += fold8(acc);
      }
      if ((lane & 24) == 0) *(v4f*)&L.hp[wave][c8 * 8 + (lane >> 5) * 4] = y;
      WAIT_LGKM0();
      if (lane == 0) __hip_atomic_fetch_add(&L.cnt[1], 1u, __ATOMIC_RELAXED, __HIP_MEMORY_SCOPE_WORKGROUP);
      if (wave != 0) { FLUSH(); return; }
      lds_wait(&L.cnt[1], 4);
      tg = __builtin_amdgcn_readfirstlane(L.cnt[7]);
      const int c4 = (lane & 15) * 4;
      const v4f t = (*(const v4f*)&L.hp[0][c4] + *(const v4f*)&L.hp[1][c4]) + (*(const v4f*)&L.hp[2][c4] + *(const v4f*)&L.hp[3][c4]);
      const v4u g = {pack2(t.x, t.y), tg, pack2(t.z, t.w), tg};
#pragma unroll
      for (int u = 0; u < 2; ++u)
        __builtin_amdgcn_raw_buffer_store_b128(g, rws, OFF_M + (4 * u + (lane >> 4)) * M_COPY + j * 256 + c4 * 4, 0, AUX_SC1);
      STAMP(4);
    }
    const int col = 16 * j + 4 * qc + (lane >> 4);
    const float xv = lo16(LD16(mk(xin, 1152), col * 2, 0));
    __builtin_amdgcn_s_sleep(127);
    v2u ga, gb;
    _Pragma("unroll 1") for (int it = 0; it < GCAP; ++it) {
      ga = LD64(rws, mcopy + (col >> 1) * 8, AUX_SC1); gb = LD64(rws, mcopy + 3 * 2304 + (col >> 1) * 8, AUX_SC1);
      unsigned bad = (ga[1] ^ tg) | (gb[1] ^ tg);
      if (all_true(bad == 0)) break;
      __builtin_amdgcn_s_sleep(32);
      MEMFENCE();
    }
    const float gav = lo16(ga[0] >> ((col & 1) * 16)), gbv = lo16(gb[0] >> ((col & 1) * 16));
    lds_wait(&L.cnt[3], 9);
    STAMP(5);
    v4u yb;
    _Pragma("unroll 1") for (int it = 0; it < GCAP; ++it) {
      yb = LD128(rws, lane < 32 ? OFF_B + (x & 3) * 18432 + qr * 2304 + j * 64 + qc * 16 : OOB, AUX_SC1);
      if (all_true((unseal(yb, tg) & 0xff) == 0 || lane >= 32)) break;
      MEMFENCE();
    }
    if (lane >= 32) yb = (v4u){0u, 0u, 0u, 0u};
    float ys = rs16(rs32(asf(yb[0]), asf(yb[2])), rs32(asf(yb[1]), asf(yb[3])));
    ys += dppf<R4>(ys); ys += dppf<R8>(ys);
    float x1 = rbf(xv + rbf(gav * rbf(ys)));
    PIN(x1);
    STAMP(6);
    v4u cc[9];
    _Pragma("unroll 1") for (int it = 0; it < GCAP; ++it) {
#pragma unroll
      for (int t = 0; t < 9; ++t) cc[t] = LD128(rws, OFF_C + (16 * t + qr) * 2304 + j * 64 + qc * 16, AUX_SC1);
      unsigned bad = 0;
#pragma unroll
      for (int t = 0; t < 9; ++t) bad |= unseal(cc[t], tg);
      if (all_true((bad & 0xff) == 0)) break;
      MEMFENCE();
    }
    STAMP(7);
    float a[4];
#pragma unroll
    for (int e = 0; e < 4; ++e)
      a[e] = (((asf(cc[0][e]) + asf(cc[1][e])) + (asf(cc[2][e]) + asf(cc[3][e]))) + ((asf(cc[4][e]) + asf(cc[5][e])) + (asf(cc[6][e]) + asf(cc[7][e])))) + asf(cc[8][e]);
    float t = rs16(rs32(a[0], a[2]), rs32(a[1], a[3]));
    t += dppf<R4>(t); t += dppf<R8>(t);
    const float res = x1 + rbf(gbv * rbf(t));
    __builtin_amdgcn_raw_buffer_store_b16((u16)pack2(res, 0.f), mk(out, 1152), (lane & 12) == 0 ? col * 2 : OOB, 0, 0);
    if (j == 0) __builtin_amdgcn_raw_buffer_store_b32(tg, rws, lane == 0 ? 0u : OOB, 0, AUX_SC1);
    STAMP(8);
  }
  FLUSH();
}

extern "C" void kernel_launch(void* const* d_in, const int* in_sizes, int n_in, void* d_out, int out_size, void* d_ws,
                              size_t ws_size, hipStream_t stream) {
  const Args a = {(const u16*)d_in[0], (const u16*)d_in[3], (const u16*)d_in[9], (const u16*)d_in[10], (char*)d_ws, (u16*)d_out,
                  (const u16*)d_in[4], (const u16*)d_in[8], (const u16*)d_in[6], (const u16*)d_in[7], (const u16*)d_in[1], (const u16*)d_in[2]};
  fused_block<<<256, 768, 0, stream>>>(a);
}
```

```cpp
#include <hip/hip_runtime.h>
#include <hip/hip_bf16.h>
#include <stdint.h>

typedef unsigned short u16;
typedef unsigned long long u64;
typedef unsigned v4u __attribute__((ext_vector_type(4)));
typedef unsigned v3u __attribute__((ext_vector_type(3)));
typedef unsigned v2u __attribute__((ext_vector_type(2)));
typedef float v2f __attribute__((ext_vector_type(2)));
typedef float v4f __attribute__((ext_vector_type(4)));
typedef __bf16 bf2 __attribute__((ext_vector_type(2)));
typedef __amdgpu_buffer_rsrc_t rsrc_t;

#define TF 4
#define TS 5
#define STAMPS 0
#define WTRIG 0
#define ENDX 0
#define PAT 0
#define DTRIG 17
#define GTRIG 9
#define GDEL 0
#define SLIM 255
#define PTRIG 8
#define PDEL 0
#define PSTEP 1
#define GAPK 0
#define WOE 4
#define DDEL 0
#define ROTX ((head >> 2) * 64)
#define NL 1
#define CPRE 5
#define CSZ 20480u
#define PR 32
#define PC 9
#define DE 0
#define DL 8
#define DB 8
#define CN 2
#define CD 8
#define XP 200
#define XD 0
#define XG 400
#define POUT 1
#define MDEL 2
#define PD1 30
#define PD2 40
#define LATEQ 1

#define EB 0
#define BC 8
#define BAUX 16
constexpr int AUX_NT = 2, AUX_SC1 = 16;
constexpr unsigned TAG_STEP = 0x01010101u;
constexpr unsigned OOB = 0x40000000u;
constexpr unsigned OFF_T = 4096;
constexpr unsigned OFF_P = 16384;
constexpr unsigned OFF_Q = OFF_P + 64 * 896;
constexpr unsigned OFF_M = OFF_Q + 256 * 768;
constexpr unsigned M_COPY = 4 * 2304;
constexpr unsigned OFF_AO = OFF_M + 8 * M_COPY;
constexpr unsigned OFF_B = OFF_AO + 256 * 768;
constexpr unsigned OFF_C = OFF_B + 8 * 18432;
constexpr unsigned WS_END = OFF_C + 144 * 3072;
constexpr unsigned OFF_STAMP = 2u << 20;
constexpr int GCAP = 1 << 15;
constexpr int LCAP = 1 << 20;
constexpr float LOG2E = 1.4426950408889634f;

__device__ __forceinline__ float asf(unsigned u) { return __builtin_bit_cast(float, u); }
__device__ __forceinline__ unsigned asu(float f) { return __builtin_bit_cast(unsigned, f); }
__device__ __forceinline__ float lo16(unsigned u) { return asf(u << 16); }
__device__ __forceinline__ float hi16(unsigned u) { return asf(u & 0xffff0000u); }
__device__ __forceinline__ float rbf(float f) { __bf16 b = (__bf16)f; return (float)b; }
__device__ __forceinline__ unsigned pack2(float a, float b) { bf2 v = {(__bf16)a, (__bf16)b}; return __builtin_bit_cast(unsigned, v); }
__device__ __forceinline__ float dot2(unsigned a, unsigned b, float c) {
  return __builtin_amdgcn_fdot2_f32_bf16(__builtin_bit_cast(bf2, a), __builtin_bit_cast(bf2, b), c, false);
}
template <int CTRL> __device__ __forceinline__ unsigned dppu(unsigned v) { return __builtin_amdgcn_update_dpp(0u, v, CTRL, 0xf, 0xf, true); }
template <int CTRL> __device__ __forceinline__ float dppf(float v) { return asf(dppu<CTRL>(asu(v))); }
constexpr int X1 = 0xB1, X2 = 0x4E, HM = 0x141, R4 = 0x124, R8 = 0x128;
__device__ __forceinline__ float rs32(float a, float b) {
  auto r = __builtin_amdgcn_permlane32_swap(asu(a), asu(b), false, false);
  unsigned r0 = r[0], r1 = r[1]; return asf(r0) + asf(r1);
}
__device__ __forceinline__ float rs16(float a, float b) {
  auto r = __builtin_amdgcn_permlane16_swap(asu(a), asu(b), false, false);
  unsigned r0 = r[0], r1 = r[1]; return asf(r0) + asf(r1);
}
__device__ __forceinline__ float fmx(float a, float b) { return __builtin_elementwise_maximum(a, b); }
__device__ __forceinline__ v2f rbf2(v2f a) { unsigned u = pack2(a.x, a.y); asm("" : "+v"(u)); return v2f{lo16(u), hi16(u)}; }
__device__ __forceinline__ float mx32(float a) {
  auto r = __builtin_amdgcn_permlane32_swap(asu(a), asu(a), false, false);
  unsigned r0 = r[0], r1 = r[1]; return fmx(asf(r0), asf(r1));
}
__device__ __forceinline__ float mx16(float a) {
  auto r = __builtin_amdgcn_permlane16_swap(asu(a), asu(a), false, false);
  unsigned r0 = r[0], r1 = r[1]; return fmx(asf(r0), asf(r1));
}
__device__ __forceinline__ float wave_sum(float v) {
  v += dppf<X1>(v); v += dppf<X2>(v); v += dppf<R4>(v); v += dppf<R8>(v);
  v = rs16(v, v); v = rs32(v, v); return v;
}
__device__ __forceinline__ float ex2(float x) { return __builtin_amdgcn_exp2f(x); }
__device__ __forceinline__ float rcp(float x) { return __builtin_amdgcn_rcpf(x); }
__device__ __forceinline__ float silu(float g) { return g * rcp(1.f + ex2(-g * LOG2E)); }
__device__ __forceinline__ v4u seal(v4f t, unsigned tg) {
  v4u o;
#pragma unroll
  for (int j = 0; j < 4; ++j) o[j] = ((asu(t[j]) + 0x80u) & ~0xffu) | ((tg >> (8 * j)) & 0xffu);
  return o;
}
__device__ __forceinline__ unsigned unseal(v4u& v, unsigned tg) {
#pragma unroll
  for (int j = 0; j < 4; ++j) v[j] ^= (tg >> (8 * j)) & 0xffu;
  asm volatile("" : "+v"(v));
  return (v[0] | v[1]) | (v[2] | v[3]);
}
__device__ __forceinline__ bool all_true(bool ok) { return __builtin_amdgcn_ballot_w64(!ok) == 0; }
__device__ __forceinline__ rsrc_t mk(const void* p, unsigned n) { return __builtin_amdgcn_make_buffer_rsrc((void*)p, 0, n, 0x00020000); }
#define LD128(r, v, aux) __builtin_amdgcn_raw_buffer_load_b128(r, v, 0, aux)
#define LD96(r, v, aux) __builtin_amdgcn_raw_buffer_load_b96(r, v, 0, aux)
#define LD64(r, v, aux) __builtin_amdgcn_raw_buffer_load_b64(r, v, 0, aux)
#define LD32(r, v, aux) __builtin_amdgcn_raw_buffer_load_b32(r, v, 0, aux)
#define LD16(r, v, aux) __builtin_amdgcn_raw_buffer_load_b16(r, v, 0, aux)
#define SB() __builtin_amdgcn_sched_barrier(0)
#define MEMFENCE() asm volatile("" ::: "memory")
#define WAIT_LGKM0() asm volatile("s_waitcnt lgkmcnt(0)" ::: "memory")
#define WAIT_VM0() asm volatile("s_waitcnt vmcnt(0)" ::: "memory")
#define PIN(x) asm volatile("" : "+v"(x))
#define PINL(p) do { auto q_ = (__attribute__((address_space(3))) char*)(p); PIN(q_); p = (decltype(p))q_; } while (0)
__device__ __forceinline__ void wg_barrier() { WAIT_LGKM0(); __builtin_amdgcn_s_barrier(); MEMFENCE(); }

struct __attribute__((aligned(16))) Lds {
  float rec_o[8][176];
  float rec_m[16];
  float dn[4][576];
  float hp[4][64];
  unsigned ob[4][96];
  unsigned qs[64];
  unsigned hs[40];
  unsigned sink[64];
  float st[4][2];
  v4u stg[8][NL ? NL : 1][8][64];
  unsigned cnt[8];
};

__device__ __forceinline__ void lds_wait(const unsigned* w, unsigned target) {
  _Pragma("unroll 1") for (int it = 0; it < LCAP; ++it) {
    unsigned v = __hip_atomic_load(w, __ATOMIC_RELAXED, __HIP_MEMORY_SCOPE_WORKGROUP);
    if (__builtin_amdgcn_readfirstlane(v) >= target) break;
    __builtin_amdgcn_s_sleep(1);
  }
  MEMFENCE();
}

__device__ __forceinline__ unsigned now32() { return (unsigned)__builtin_amdgcn_s_memrealtime(); }
__device__ __forceinline__ void wait_until(unsigned t) {
  _Pragma("unroll 1") for (int it = 0; it < 200 && (int)(now32() - t) < 0; ++it) __builtin_amdgcn_s_sleep(1);
}

struct Slab { unsigned p[4][8]; unsigned z[4]; };
__device__ __forceinline__ void interleave(unsigned (&o)[8], v4u a, v4u b) {
#pragma unroll
  for (int d = 0; d < 4; ++d) {
    unsigned x = a[d], y = b[d];
    o[2 * d] = __builtin_amdgcn_perm(y, x, 0x05040100u);
    o[2 * d + 1] = __builtin_amdgcn_perm(y, x, 0x07060302u);
  }
}
__device__ __forceinline__ void slab_make(Slab& s, const v4u (&w)[9]) {
#pragma unroll
  for (int p = 0; p < 4; ++p) {
    interleave(s.p[p], w[2 * p], w[2 * p + 1]); s.z[p] = w[8][p];
#pragma unroll
    for (int d = 0; d < 8; ++d) PIN(s.p[p][d]);
    PIN(s.z[p]);
  }
}
struct Hv { unsigned p[4], za, zb; };
__device__ __forceinline__ void hv_silu(Hv& h, v4u a, unsigned z) {
#pragma unroll
  for (int p = 0; p < 4; ++p) h.p[p] = pack2(silu(lo16(a[p])), silu(hi16(a[p])));
  h.za = pack2(silu(lo16(z)), 0.f); h.zb = h.za << 16;
}
__device__ __forceinline__ void slab_dot(float (&acc)[8], const Slab& s, const Hv& h) {
#pragma unroll
  for (int p = 0; p < 4; ++p)
#pragma unroll
    for (int d = 0; d < 8; ++d) acc[d] = dot2(s.p[p][d], h.p[p], acc[d]);
#pragma unroll
  for (int d = 0; d < 4; ++d) { acc[2 * d] = dot2(s.z[d], h.za, acc[2 * d]); acc[2 * d + 1] = dot2(s.z[d], h.zb, acc[2 * d + 1]); }
}
typedef __attribute__((address_space(3))) float* lds_f;
#define LF(a) ((lds_f)(a))
#define LF4(a) ((__attribute__((address_space(3))) v4f*)(a))
#define ZERO8(a) _Pragma("unroll") for (int d_ = 0; d_ < 8; ++d_) { a[d_] = 0.f; PIN(a[d_]); }
__device__ __forceinline__ v4f fold8(const float (&acc)[8]) {
  v4f y;
#pragma unroll
  for (int j = 0; j < 4; ++j) { float t = rs32(acc[j], acc[j + 4]); t = rs16(t, t); y[j] = t + dppf<R8>(t); }
  return y;
}
__device__ __forceinline__ void ln_stats(float s, float q, float& m, float& rsd) {
  float mean = s * (1.f / 576.f);
  float var = fmaxf(q * (1.f / 576.f) - mean * mean, 0.f);
  m = rbf(mean);
  float sd = rbf(__builtin_amdgcn_sqrtf(rbf(rbf(var) + 1e-5f)));
  rsd = rcp(sd);
}
__device__ __forceinline__ float modulate(float t, float scale, float shift) { return rbf(rbf(t * rbf(1.f + scale)) + shift); }

#define PROF() do {} while (0)
#define STAMP(k) do {} while (0)
#define FLUSH() do {} while (0)

struct Args { const u16 *xin, *wq, *cin, *wmod; char* ws; u16* out; const u16 *wo, *wd, *wg, *wu, *Kc, *Vc; };
typedef const Args __attribute__((address_space(4))) * args_t;
__device__ __forceinline__ args_t args_ptr() {
  args_t p = (args_t)__builtin_amdgcn_kernarg_segment_ptr();
  asm volatile("" : "+s"(p));
  return p;
}

__global__ void __launch_bounds__(768) fused_block(Args) {
  __shared__ Lds L;
  const int tid = threadIdx.x, lane = tid & 63, wave = __builtin_amdgcn_readfirstlane(tid >> 6);
  const int b = blockIdx.x, x = b & 7, head = b >> 3, r = (head + 4 * x) & 31;
  STAMP(0);
  const int k = r >> 2;
  const int r8 = lane >> 3, c8 = lane & 7;
  const unsigned sink = (unsigned)(size_t)(__attribute__((address_space(3))) void*)L.sink;

  const bool is_mod = wave == 0, ffn = r >= 9 && r < 27;
  unsigned ep, cz;
  v4u ci, w[9];
  const u16 *xin, *cin, *wmod, *wo, *wd, *wg, *wu; u16* out; char* ws;
  if (wave < 4) {
    __builtin_amdgcn_s_setprio(3);
    const args_t A = args_ptr();
    const u16* wq = A->wq;
    xin = A->xin; cin = A->cin; wmod = A->wmod; ws = A->ws; out = A->out; wo = A->wo; wd = A->wd; wg = A->wg; wu = A->wu;
    if (tid < 8) L.cnt[tid] = 0;
    if (CPRE && wave == 3) {
      u64 pc; unsigned junk;
      asm volatile("s_getpc_b64 %0" : "=s"(pc));
      const v4u rc = {(unsigned)pc & ~255u, (unsigned)(pc >> 32) & 0xffffu, CSZ, 0x00020000u};
      asm volatile("s_mov_b32 m0, %2\n\ts_nop 1\n\tbuffer_load_dword %0, %1, 0 offen lds" :: "v"(lane < CPRE ? (lane * 32 + r) * 128 : OOB), "s"(rc), "s"(sink) : "m0");
    }
    unsigned ld = is_mod ? (ffn ? 6912 : 0) : (wave == 3 ? 0 : 8192);
    unsigned moff = 72 * x * ld + (is_mod ? r - 9 : 2 * head + wave - 1) * 128;
    unsigned v0 = 8 * r8 * ld + c8 * 16, v8 = (64 + r8) * ld + c8 * 16, vi = r8 * 16, vz = 128 + r8 * 2;
    unsigned vt = (lane == 0 && wave == 1 && x == 0) ? OFF_T + head * 256 : OOB, t0 = 64 * NL;
    asm volatile("" : "+s"(ld), "+s"(moff));
    asm volatile("" : "+v"(v0), "+v"(v8), "+v"(vz), "+v"(vi), "+v"(vt), "+v"(t0));
    SB();
    asm volatile("" :: "s"(xin), "s"(wq), "s"(wo), "s"(wg), "s"(wu), "s"(wd), "s"(cin), "s"(wmod), "s"(out), "s"(ws));
    const u64 wa = (u64)ws, ia = (u64)cin + 144 * x, ma = (u64)(is_mod ? wmod : wq) + moff;
    const v4u r0 = {(unsigned)wa, (unsigned)(wa >> 32) & 0xffffu, WS_END, 0x00020000u};
    const v4u ri = {(unsigned)ia, (unsigned)(ia >> 32) & 0xffffu, 144u, 0x00020000u};
    const v4u rm = {(unsigned)ma, (unsigned)(ma >> 32) & 0xffffu, 71 * ld + 128, 0x00020000u};
    unsigned so;
#define LDW(n) "buffer_load_dwordx4 %" #n ", %[v0], %[rm], %[so] offen nt\n\ts_add_u32 %[so], %[so], %[ld]\n\t"
    asm volatile("s_cmp_eq_u32 %[ld], 0\n\ts_cbranch_scc1 1f\n\ts_mov_b32 %[so], 0\n\t"
                 "buffer_load_dword %9, off, %[r0], 0\n\tbuffer_load_dwordx4 %10, %[vi], %[ri], 0 offen\n\t"
                 "buffer_load_ushort %11, %[vz], %[ri], 0 offen\n\t" LDW(0) LDW(1) LDW(2) LDW(3) LDW(4) LDW(5) LDW(6) LDW(7)
                 "buffer_load_dwordx4 %8, %[v8], %[rm], 0 offen nt\n\tbuffer_store_dword %[t0], %[vt], %[r0], 0 offen sc1\n"
                 "1:\n\ts_waitcnt lgkmcnt(0)\n\ts_barrier"
                 : "=&v"(w[0]), "=&v"(w[1]), "=&v"(w[2]), "=&v"(w[3]), "=&v"(w[4]), "=&v"(w[5]), "=&v"(w[6]), "=&v"(w[7]), "=&v"(w[8]),
                   "=&v"(ep), "=&v"(ci), "=&v"(cz), [so] "=&s"(so)
                 : [r0] "s"(r0), [ri] "s"(ri), [rm] "s"(rm), [v0] "v"(v0), [v8] "v"(v8), [vi] "v"(vi), [vz] "v"(vz), [ld] "s"(ld),
                   [vt] "v"(vt), [t0] "v"(t0)
                 : "scc", "memory");
  }

  if (wave >= 4) {
    const int s = wave - 4;
    const args_t A = args_ptr();
    const u16 *Kc = A->Kc, *Vc = A->Vc, *wg = A->wg, *wu = A->wu; char* ws = A->ws;
    const rsrc_t rws = mk(ws, WS_END);
    const u64 ka = (u64)(Kc + ((size_t)head << 20)), va = (u64)(Vc + ((size_t)head << 20)), wa = (u64)ws;
    const v4u rk = {(unsigned)ka, (unsigned)(ka >> 32) & 0xffffu, 1u << 21, 0x00020000u};
    const v4u rv = {(unsigned)va, (unsigned)(va >> 32) & 0xffffu, 1u << 21, 0x00020000u};
    const v4u rt = {(unsigned)wa, (unsigned)(wa >> 32) & 0xffffu, OFF_P, 0x00020000u};
    const unsigned vo = lane * 16, vt = lane == 0 ? OFF_T + head * 256 : OOB;
    const unsigned rot = ROTX, p0 = rot + k * 8 + s;
    v4u Kr[3][4], Vr[3][4];
    asm volatile("s_barrier" :: "s"(rk), "s"(rv), "s"(p0), "v"(vo) : "memory");
    STAMP(10);
#define S_(x) #x
#define S(x) S_(x)
#define LD1(n, off, T) "s_waitcnt vmcnt(" S(T) "-1)\n\tbuffer_load_dwordx4 %" #n ", %[vo], %[rs], %[so] offen offset:" #off " nt\n\t"
#define VOS [vo] "v"(vo)
#define CHUNK(c) (((c) & 511) << 12)
#define LD4(T) LD1(0, 0, T) LD1(1, 1024, T) LD1(2, 2048, T) LD1(3, 3072, T)
#define LD4E(T) "s_cmp_ge_i32 %[tn], 256-" S(ENDX) "\n\ts_cbranch_scc1 2f\n\t" LD4(T) "s_branch 1f\n2:\n\t" LD4(40)
#define LL1(off) "s_waitcnt vmcnt(" S(TF) "-1)\n\tbuffer_load_dwordx4 %[vo], %[rs], %[so] offen offset:" #off " nt lds\n\t"
#pragma unroll
    for (int i = 0; i < NL; ++i) {
      const unsigned so = CHUNK(p0 + 64 * i), lb = (unsigned)(size_t)(__attribute__((address_space(3))) void*)L.stg[s][i];
      asm volatile("s_mov_b32 m0, %[lb]\n\t" LL1(0) LL1(1024) LL1(2048) LL1(3072) :: VOS, [rs] "s"(rk), [so] "s"(so), [lb] "s"(lb) : "m0", "memory");
      PROF();
      asm volatile("s_mov_b32 m0, %[lb]\n\t" LL1(0) LL1(1024) LL1(2048) LL1(3072) :: VOS, [rs] "s"(rv), [so] "s"(so), [lb] "s"(lb + 4096) : "m0", "memory");
      PROF();
    }
#pragma unroll
    for (int i = 0; i < 3; ++i) {
      const unsigned so = CHUNK(p0 + 64 * (NL + i));
      asm volatile(LD4(TF) : "=&v"(Kr[i][0]), "=&v"(Kr[i][1]), "=&v"(Kr[i][2]), "=&v"(Kr[i][3]) : VOS, [rs] "s"(rk), [so] "s"(so));
      PROF();
      asm volatile(LD4(TF) : "=&v"(Vr[i][0]), "=&v"(Vr[i][1]), "=&v"(Vr[i][2]), "=&v"(Vr[i][3]) : VOS, [rs] "s"(rv), [so] "s"(so));
      PROF();
      if (i == 0) STAMP(11);
    }
    STAMP(6);
    const int pi = (s & 3) * 41 + lane;
    const unsigned pv = (lane < 41 && pi < 162) ? (18 * r + pi / 9) * 9216 + x * 1152 + (pi % 9) * 128 : OOB;
    const int prk = (POUT && r < 9) ? 256 : 256 + PR;
    unsigned tz = 0;
    const u64 pa = (u64)(s < 4 ? wg : wu);
    const v4u rp = {(unsigned)pa, (unsigned)(pa >> 32) & 0xffffu, 576u * 9216, 0x00020000u};
    lds_wait(&L.cnt[4], 1);
    const v4u qv = *(const v4u*)&L.qs[(lane & 15) * 4];
    unsigned q0 = qv[0], q1 = qv[1], q2 = qv[2], q3 = qv[3];
    PIN(q0); PIN(q1); PIN(q2); PIN(q3);
#define LA(p) ((unsigned)(size_t)(__attribute__((address_space(3))) void*)(p))
    const int d0 = (lane & 15) * 8 + (lane >> 4) * 2;
    unsigned wa0 = LA(&L.rec_o[s][d0 + (d0 * 171 >> 9)]), wa1 = LA(&L.rec_o[s][d0 + 1 + ((d0 + 1) * 171 >> 9)]);
    unsigned ra0 = LA(&L.rec_o[0][(lane < 44 ? lane : 0) * 4]), ra1 = LA(&L.rec_m[c8]), wam = LA(&L.rec_m[s]), wac = LA(L.cnt);
    unsigned off = lane < 44 ? OFF_AO + b * 768 + lane * 16 : OOB, tgs = L.cnt[7];
    PIN(wa0); PIN(wa1); PIN(ra0); PIN(ra1); PIN(wam); PIN(wac); PIN(off); PIN(tgs);
    STAMP(7);
    float m = -INFINITY, l = 0.f;
    v2f acc[4] = {{0.f, 0.f}, {0.f, 0.f}, {0.f, 0.f}, {0.f, 0.f}};
    unsigned tk = k * 8 + s - 64 + 64 * NL;
#define TICKET "v_mov_b32 %[tk], 1\n\tbuffer_atomic_add %[tk], %[vt], %[rt], 0 offen sc0 sc1\n\t"
#define PFM 0
#define PFI "buffer_load_dword %[pv], %[rp], 0 offen nt lds\n\tbuffer_load_dword %[pv], %[rp], 0 offen sc1 lds"
#define PREF0 "s_mov_b32 m0, %[sink]\n\ts_nop 1\n\t" PFI "\n\t"
#define PREF "s_cmp_lt_i32 %[tn], %[prk]\n\ts_cbranch_scc1 7f\n\t" PREF0 "7:\n\t"
#define LASTW "s_waitcnt vmcnt(0)\n\t" PREF
#define DOTS(K) \
        _Pragma("unroll") for (int t = 0; t < 4; ++t)                                               \
          sc[t] = dot2(K[t][3], q3, dot2(K[t][2], q2, dot2(K[t][1], q1, dot2(K[t][0], q0, 0.f))));
#define PV(V) \
        _Pragma("unroll") for (int t = 0; t < 4; ++t) {                                             \
          const v2f pp = {p[t], p[t]};                                                              \
          _Pragma("unroll") for (int j = 0; j < 4; ++j) {                                           \
            const v2f e = {lo16(V[t][j]), hi16(V[t][j])};                                           \
            acc[j] = __builtin_elementwise_fma(pp, e, acc[j]);                                      \
          }                                                                                         \
        }
#define SOFT \
        SB();                                                                                       \
        _Pragma("unroll") for (int t = 0; t < 4; ++t) sc[t] += dppf<X1>(sc[t]);                     \
        SB();                                                                                       \
        _Pragma("unroll") for (int t = 0; t < 4; ++t) sc[t] += dppf<X2>(sc[t]);                     \
        SB();                                                                                       \
        _Pragma("unroll") for (int t = 0; t < 4; ++t) sc[t] += dppf<R4>(sc[t]);                     \
        SB();                                                                                       \
        _Pragma("unroll") for (int t = 0; t < 4; ++t) sc[t] += dppf<R8>(sc[t]);                     \
        SB();                                                                                       \
        _Pragma("unroll") for (int t = 0; t < 4; ++t) sc[t] = rbf(rbf(sc[t]) * 0.08838834764831845f); \
        const float mn = fmaxf(fmaxf(fmaxf(sc[0], sc[1]), fmaxf(sc[2], sc[3])), m);                 \
        const float al = ex2((m - mn) * LOG2E);                                                     \
        m = mn;                                                                                     \
        float p[4];                                                                                 \
        _Pragma("unroll") for (int t = 0; t < 4; ++t) p[t] = ex2((sc[t] - mn) * LOG2E);             \
        l = l * al + ((p[0] + p[1]) + (p[2] + p[3]));                                               \
        _Pragma("unroll") for (int j = 0; j < 4; ++j) acc[j] *= al;
    int nb = 3, tn = 0;
    const unsigned sa = (unsigned)(size_t)(__attribute__((address_space(3))) void*)&L.stg[s][0][0][lane];
    asm volatile("s_waitcnt vmcnt(4)" ::: "memory");
    _Pragma("unroll 1") for (int it = 0; it < 600; ++it) {
      if (tn < 256) {
        float sc[4];
        asm volatile("s_waitcnt vmcnt(5)" : "+v"(Kr[2][0]), "+v"(Kr[2][1]), "+v"(Kr[2][2]), "+v"(Kr[2][3]));
        DOTS(Kr[2])
        SOFT
        asm volatile("s_waitcnt vmcnt(4)" : "+v"(tk), "+v"(p[0]), "+v"(p[1]), "+v"(p[2]), "+v"(p[3]), "+v"(l),
                     "+v"(acc[0]), "+v"(acc[1]), "+v"(acc[2]), "+v"(acc[3]));
        tn = __builtin_amdgcn_readfirstlane(tk);
        const unsigned so = CHUNK(tn + 256 + rot);
        if (STAMPS && tn >= 256) STAMP(8);
        tz = now32();
        asm volatile("s_cmp_lt_i32 %[tn], 0x100\n\ts_cbranch_scc1 5f\n\ts_setprio 3\n\t" LASTW "s_branch 6f\n5:\n\t" LD4E(TS) "1:\n\ts_waitcnt vmcnt(4)\n6:"
                     : "+v"(Kr[2][0]), "+v"(Kr[2][1]), "+v"(Kr[2][2]), "+v"(Kr[2][3]), "+v"(Vr[2][0]), "+v"(Vr[2][1]), "+v"(Vr[2][2]), "+v"(Vr[2][3])
                     : VOS, [rs] "s"(rk), [so] "s"(so), [tn] "s"(tn), [pv] "v"(pv), [rp] "s"(rp), [sink] "s"(sink), [prk] "s"(prk) : "scc", "m0");
        if (tn < 256) PROF();
        PV(Vr[2])
        asm volatile("s_cmp_lt_i32 %[tn], 0x100\n\ts_cbranch_scc0 1f\n\t" TICKET LD4E(TS) "1:"
                     : "+v"(Vr[2][0]), "+v"(Vr[2][1]), "+v"(Vr[2][2]), "+v"(Vr[2][3]), [tk] "+v"(tk),
                       "+v"(acc[0]), "+v"(acc[1]), "+v"(acc[2]), "+v"(acc[3])
                     : VOS, [rs] "s"(rv), [so] "s"(so), [vt] "v"(vt), [rt] "s"(rt), [tn] "s"(tn) : "scc", "memory");
        PROF();
      }
      if (nb) {
        --nb;
        float sc[4];
        asm volatile("s_waitcnt lgkmcnt(0)" : "+v"(Kr[0][0]), "+v"(Kr[0][1]), "+v"(Kr[0][2]), "+v"(Kr[0][3]), "+v"(Vr[0][0]), "+v"(Vr[0][1]), "+v"(Vr[0][2]), "+v"(Vr[0][3]));
        DOTS(Kr[0])
        SOFT
        SB();
        PV(Vr[0])
#define DR(n, off) "ds_read_b128 %" #n ", %[sa] offset:" #off "\n\t"
#define DW(n, off) "ds_write_b128 %[sa], %" #n " offset:" #off "\n\t"
        asm volatile(DR(0, 0) DR(1, 1024) DR(2, 2048) DR(3, 3072) DR(4, 4096) DR(5, 5120) DR(6, 6144) DR(7, 7168)
                     DW(12, 0) DW(13, 1024) DW(14, 2048) DW(15, 3072) DW(16, 4096) DW(17, 5120) DW(18, 6144) DW(19, 7168)
                     : "+v"(Kr[0][0]), "+v"(Kr[0][1]), "+v"(Kr[0][2]), "+v"(Kr[0][3]), "+v"(Vr[0][0]), "+v"(Vr[0][1]), "+v"(Vr[0][2]), "+v"(Vr[0][3]),
                       "+v"(acc[0]), "+v"(acc[1]), "+v"(acc[2]), "+v"(acc[3])
                     : "v"(Kr[1][0]), "v"(Kr[1][1]), "v"(Kr[1][2]), "v"(Kr[1][3]), "v"(Vr[1][0]), "v"(Vr[1][1]), "v"(Vr[1][2]), "v"(Vr[1][3]), [sa] "v"(sa)
                     : "memory");
      }
      if (nb == 0 && tn >= 256) break;
    }
    asm volatile("s_waitcnt lgkmcnt(0)" :: "v"(Kr[0][0]), "v"(Kr[0][1]), "v"(Kr[0][2]), "v"(Kr[0][3]), "v"(Vr[0][0]), "v"(Vr[0][1]), "v"(Vr[0][2]), "v"(Vr[0][3]));
    tz -= ((tn - 256) * PC) >> 1;
    STAMP(4);
    const float M = mx32(mx16(m));
    const float w = ex2((m - M) * LOG2E);
    l *= w; l = rs16(l, l); l = rs32(l, l);
#pragma unroll
    for (int j = 0; j < 4; ++j) acc[j] *= w;
    const float y0 = rs16(rs32(acc[0].x, acc[2].x), rs32(acc[1].x, acc[3].x)), y1 = rs16(rs32(acc[0].y, acc[2].y), rs32(acc[1].y, acc[3].y));
    unsigned old = 1; float mi; v4f o[8];
#define RR(n, off) "ds_read_b128 %" #n ", %[ra0] offset:" #off "\n\t"
    asm volatile("ds_write_b32 %[wa0], %[y0]\n\tds_write_b32 %[wa1], %[y1]\n\ts_mov_b64 exec, 1\n\tds_write_b32 %[wa0], %[l] offset:692\n\t"
                 "ds_write_b32 %[wam], %[M]\n\tds_max_u32 %[wac], %[tz] offset:20\n\tds_add_rtn_u32 %9, %[wac], %9 offset:12\n\ts_mov_b64 exec, -1\n\t"
                 "ds_read_b32 %8, %[ra1]\n\t" RR(0, 0) RR(1, 704) RR(2, 1408) RR(3, 2112) RR(4, 2816) RR(5, 3520) RR(6, 4224) RR(7, 4928) "s_waitcnt lgkmcnt(8)"
                 : "=&v"(o[0]), "=&v"(o[1]), "=&v"(o[2]), "=&v"(o[3]), "=&v"(o[4]), "=&v"(o[5]), "=&v"(o[6]), "=&v"(o[7]), "=&v"(mi), "+v"(old)
                 : [wa0] "v"(wa0), [wa1] "v"(wa1), [ra0] "v"(ra0), [ra1] "v"(ra1), [wam] "v"(wam), [wac] "v"(wac), [y0] "v"(y0), [y1] "v"(y1),
                   [l] "v"(l), [M] "v"(M), [tz] "v"(tz) : "memory");
    float MM = fmx(mi, dppf<X1>(mi)); MM = fmx(MM, dppf<X2>(MM)); MM = fmx(MM, dppf<HM>(MM));
    const float e = ex2((mi - MM) * LOG2E);
    float es[8];
#pragma unroll
    for (int j = 0; j < 8; ++j) es[j] = asf(__builtin_amdgcn_readlane(asu(e), j));
    asm volatile("s_waitcnt lgkmcnt(0)" : "+v"(o[0]), "+v"(o[1]), "+v"(o[2]), "+v"(o[3]), "+v"(o[4]), "+v"(o[5]), "+v"(o[6]), "+v"(o[7])
                 : "s"(es[0]), "s"(es[1]), "s"(es[2]), "s"(es[3]), "s"(es[4]), "s"(es[5]), "s"(es[6]), "s"(es[7]));
    if (__builtin_amdgcn_readfirstlane(old) == 7) {
      v2f oa; float oc;
#pragma unroll
      for (int j = 0; j < 8; ++j) {
        const float ej = es[j];
        const v2f e2 = {ej, ej}, t = {o[j].x, o[j].y};
        oa = j ? __builtin_elementwise_fma(e2, t, oa) : e2 * t; oc = j ? fmaf(ej, o[j].z, oc) : ej * o[j].z;
      }
      const v4u pv = {asu(lane < 43 ? oa.x : MM), asu(oa.y), asu(oc), tgs};
      __builtin_amdgcn_raw_buffer_store_b128(pv, rws, off, 0, AUX_SC1);
      if (lane == 0) __hip_atomic_fetch_add(&L.cnt[3], 1u, __ATOMIC_RELAXED, __HIP_MEMORY_SCOPE_WORKGROUP);
      STAMP(5);
    }
    if (PR && tn < prk) {
      __builtin_amdgcn_s_setprio(0);
      wait_until(tz + XP);
      STAMP(9);
      asm volatile(PREF0 :: [pv] "v"(pv), [rp] "s"(rp), [sink] "s"(sink) : "m0");
    }
    WAIT_VM0();
    FLUSH();
    return;
  }

  const rsrc_t rws = mk(ws, WS_END);
  if (wave == 3) {
    const int hrow = (lane & 8) ? 64 + 3 * (lane & 3) + ((lane & 16) ? 2 : lane >> 5) : 8 * c8 + ((lane >> 3) & 6);
    const rsrc_t rin = mk(xin, 1152);
    const unsigned e3p = LD32(rws, 0, 0);
    const v4u xa = LD128(rin, lane * 16, 0), xb = LD128(rin, lane * 16 + 1024, 0);
    const unsigned xp = LD32(rin, (72 * x + (hrow & ~1)) * 2, 0);
    const unsigned tg = __builtin_amdgcn_readfirstlane(e3p) + TAG_STEP;
    float s = 0.f, q = 0.f;
#pragma unroll
    for (int d = 0; d < 4; ++d) {
      float e0 = lo16(xa[d]), e1 = hi16(xa[d]), e2 = lo16(xb[d]), e3 = hi16(xb[d]);
      s += (e0 + e1) + (e2 + e3); q += (e0 * e0 + e1 * e1) + (e2 * e2 + e3 * e3);
    }
    s = wave_sum(s); q = wave_sum(q);
    float m, rsd; ln_stats(s, q, m, rsd);
    float n0 = rbf(rbf(lo16(xp >> ((hrow & 1) * 16)) - m) * rsd), n1 = rbf(rbf(hi16(xp) - m) * rsd);
    PIN(n0); PIN(n1);
    if (PD1) __builtin_amdgcn_s_sleep(PD1);
    STAMP(1);
    const unsigned vp = OFF_P + (x * 8 + r8) * 896 + c8 * 16;
    v4u g[7];
    _Pragma("unroll 1") for (int it = 0; it < GCAP; ++it) {
      unsigned bad = 0;
#pragma unroll
      for (int t = 0; t < 6; ++t) g[t] = LD128(rws, vp + t * 128, AUX_SC1);
      g[6] = LD128(rws, vp + ((lane & 3) == 3 ? 752 : 768), AUX_SC1);
#pragma unroll
      for (int t = 0; t < 7; ++t) bad |= g[t][3] ^ tg;
      if (all_true(bad == 0)) break;
      MEMFENCE();
    }
    STAMP(2);
#define G3(t, j) asf(g[(t) + (j) / 3][(j) % 3])
    float sa[2], ca[2];
#pragma unroll
    for (int e = 0; e < 2; ++e) {
      sa[e] = rs16(rs32(G3(0, e), G3(0, e + 4)), rs32(G3(0, e + 2), G3(0, e + 6)));
      ca[e] = rs16(rs32(G3(3, e), G3(3, e + 4)), rs32(G3(3, e + 2), G3(3, e + 6)));
      sa[e] += dppf<R8>(sa[e]); ca[e] += dppf<R8>(ca[e]);
    }
    float z = rs16(rs32(G3(6, 0), G3(6, 1)), rs32(G3(6, 2), G3(6, 2)));
    z += dppf<R8>(z);
    const float zc = dppf<R4>(z);
    if (lane & 8) { sa[0] = z; ca[0] = zc; }
    const unsigned hh = pack2(modulate(n0, rbf(ca[0]), rbf(sa[0])), modulate(n1, rbf(ca[1]), rbf(sa[1])));
    if ((lane & 8) == 0) L.hs[hrow >> 1] = hh;
    else if ((lane & 4) == 0) ((u16*)L.hs)[hrow] = (u16)hh;
    WAIT_LGKM0();
    if (lane == 0) __hip_atomic_store(&L.cnt[0], 1u, __ATOMIC_RELAXED, __HIP_MEMORY_SCOPE_WORKGROUP);
    if (PD2) __builtin_amdgcn_s_sleep(PD2);
    STAMP(3);
    const unsigned vq = OFF_Q + (head * 8 + r8) * 768 + c8 * 16;
    _Pragma("unroll 1") for (int it = 0; it < GCAP; ++it) {
      unsigned bad = 0;
#pragma unroll
      for (int t = 0; t < 6; ++t) g[t] = LD128(rws, vq + t * 128, AUX_SC1);
#pragma unroll
      for (int t = 0; t < 6; ++t) bad |= g[t][3] ^ tg;
      if (all_true(bad == 0)) break;
      MEMFENCE();
    }
#define Q3(j) rs32(asf(g[(j) / 3 * 2][(j) % 3]), asf(g[(j) / 3 * 2 + 1][(j) % 3]))
    float qq[4];
#pragma unroll
    for (int e = 0; e < 4; ++e) { const float t = rs16(Q3(e), Q3(e + 4)); qq[e] = t + dppf<R8>(t); }
    const v2u qp = {pack2(qq[0], qq[1]), pack2(qq[2], qq[3])};
    *(v2u*)&L.qs[((lane >> 5) * 8 + c8) * 4 + ((lane >> 3) & 2)] = qp;
    L.cnt[7] = tg;
    WAIT_LGKM0();
    if (lane == 0) __hip_atomic_store(&L.cnt[4], 1u, __ATOMIC_RELAXED, __HIP_MEMORY_SCOPE_WORKGROUP);
    STAMP(4);
  } else if (!is_mod || ffn) {
    asm volatile("s_waitcnt vmcnt(9)" : "+v"(ep), "+v"(ci), "+v"(cz) :: "memory");
    const unsigned tg0 = __builtin_amdgcn_readfirstlane(ep) + TAG_STEP;
    const int u = lane >> 4;
    unsigned vo;
    Hv H;
    if (is_mod) {
      const int o = (r - 9) * 8 + c8, kind = o >= 72, oc = o - 72 * kind, gr = oc / 9, i = oc - 9 * gr;
      vo = OFF_P + (gr * 8 + x) * 896 + (i < 8 ? (3 * kind + u) * 128 + i * 16 : 768 + (4 * kind + u) * 16);
      hv_silu(H, ci, cz);
    } else {
      vo = OFF_Q + b * 768 + u * 256 + ((wave - 1) * 8 + c8) * 16;
    }
    if ((lane & 8) || lane >= 48) vo = OOB;
    PIN(vo);
    asm volatile("s_waitcnt vmcnt(0)" : "+v"(w[0]), "+v"(w[1]), "+v"(w[2]), "+v"(w[3]), "+v"(w[4]), "+v"(w[5]), "+v"(w[6]), "+v"(w[7]), "+v"(w[8]) :: "memory");
    Slab S;
    slab_make(S, w);
    STAMP(1);
    if (!is_mod) {
      lds_wait(&L.cnt[0], 1);
      const v4u hp = *(const v4u*)&L.hs[4 * r8];
      H.za = ((const u16*)L.hs)[64 + r8]; H.zb = H.za << 16;
#pragma unroll
      for (int p = 0; p < 4; ++p) H.p[p] = hp[p];
    }
    STAMP(2);
    float a[8] = {0.f, 0.f, 0.f, 0.f, 0.f, 0.f, 0.f, 0.f}; slab_dot(a, S, H);
    float e0 = rs16(rs32(a[0], a[6]), rs32(a[3], a[3])), e1 = rs16(rs32(a[1], a[7]), rs32(a[4], a[4])), e2 = rs16(rs32(a[2], a[2]), rs32(a[5], a[5]));
    e0 += dppf<R8>(e0); e1 += dppf<R8>(e1); e2 += dppf<R8>(e2);
    const v4u pv = {asu(e0), asu(e1), asu(e2), tg0};
    __builtin_amdgcn_raw_buffer_store_b128(pv, rws, vo, 0, AUX_SC1);
    STAMP(3);
  }
  const int qr = lane >> 2, qc = lane & 3;
  const unsigned mcopy = OFF_M + x * M_COPY;
  if (r < 9) {
    const rsrc_t rw = mk(wo + (size_t)(512 * x + 128 * wave) * 576 + 64 * r, 127 * 1152 + 128);
    v4u wo_w[16];
    if (LATEQ) lds_wait(&L.cnt[4], 1);
#pragma unroll
    for (int t = 0; t < 16; ++t) wo_w[t] = LD128(rw, (16 * r8 + t) * 1152 + c8 * 16, AUX_NT);
    unsigned P[8][8];
#pragma unroll
    for (int p = 0; p < 8; ++p) {
      interleave(P[p], wo_w[2 * p], wo_w[2 * p + 1]);
#pragma unroll
      for (int d = 0; d < 8; ++d) PIN(P[p][d]);
    }
    lds_wait(&L.cnt[4], 1);
    const unsigned tg = __builtin_amdgcn_readfirstlane(L.cnt[7]);
    u16* ot = (u16*)L.ob[wave] + 3 * lane; const unsigned* oi = &L.ob[wave][r8 * 8];
    float* hw = &L.hp[wave][c8 * 8 + (lane >> 4) * 2]; const float* hr = &L.hp[0][(lane & 15) * 4];
    unsigned vs = OFF_B + (4 * wave + (lane >> 4)) * 18432 + x * 2304 + r * 256 + (lane & 15) * 16;
    PINL(ot); PINL(oi); PINL(hw); PINL(hr); PIN(vs);
    lds_wait(&L.cnt[3], 9);
    STAMP(5);
    const unsigned va = OFF_AO + (4 * x + wave) * 8 * 768 + (lane < 43 ? lane : 43) * 16, va0 = va + (lane < 48 ? 0 : c8 * 768);
    v4u o[8];
    {
      int left = GCAP; u64 miss;
      _Pragma("unroll 1") do {
        MEMFENCE();
#pragma unroll
        for (int j = 0; j < 8; ++j) { o[j] = __builtin_amdgcn_raw_buffer_load_b128(rws, j ? va : va0, j * 768, AUX_SC1); SB(); }
        --left; miss = 0;
#pragma unroll
        for (int j = 0; j < 8; ++j) { miss |= __builtin_amdgcn_ballot_w64(o[j][3] != tg); SB(); }
        miss &= left ? ~0ull : 0ull;
      } while (miss);
    }
    STAMP(6);
    const float mi = asf(o[0][0]);
    float M = fmx(mi, dppf<X1>(mi)); M = fmx(M, dppf<X2>(M)); M = fmx(M, dppf<HM>(M));
    const float e = ex2((mi - M) * LOG2E);
    float Ls = e * asf(o[0][1]); Ls += dppf<X1>(Ls); Ls += dppf<X2>(Ls); Ls += dppf<HM>(Ls);
    float oo[3] = {0.f, 0.f, 0.f};
#pragma unroll
    for (int j = 0; j < 8; ++j) {
      const float ej = asf(__builtin_amdgcn_readlane(asu(e), 48 + j));
#pragma unroll
      for (int d = 0; d < 3; ++d) oo[d] = fmaf(ej, asf(o[j][d]), oo[d]);
    }
    const float rl = asf(__builtin_amdgcn_readlane(asu(rcp(Ls)), 48));
    {
      const unsigned p01 = pack2(oo[0] * rl, oo[1] * rl), p2 = pack2(oo[2] * rl, 0.f);
      ot[0] = (u16)p01; ot[1] = (u16)(p01 >> 16); ot[2] = (u16)p2;
    }
    MEMFENCE();
    const v4u oa = *(const v4u*)oi, ob = *(const v4u*)(oi + 4);
    float acc[8] = {0.f, 0.f, 0.f, 0.f, 0.f, 0.f, 0.f, 0.f};
#pragma unroll
    for (int p = 0; p < 8; ++p) {
      const unsigned hp = p < 4 ? oa[p & 3] : ob[p & 3];
#pragma unroll
      for (int d = 0; d < 8; ++d) acc[d] = dot2(P[p][d], hp, acc[d]);
    }
    float y[4];
#pragma unroll
    for (int j = 0; j < 4; ++j) y[j] = rs32(acc[j], acc[j + 4]);
    float z0 = rs16(y[0], y[2]), z1 = rs16(y[1], y[3]);
    z0 += dppf<R8>(z0); z1 += dppf<R8>(z1);
    { PIN(z0); PIN(z1); v2f z = {z0, z1}; *(v2f*)hw = z; }
    wg_barrier();
    if (wave < 2) {
      const v4f t = (*(const v4f*)hr + *(const v4f*)(hr + 64)) + (*(const v4f*)(hr + 128) + *(const v4f*)(hr + 192));
      __builtin_amdgcn_raw_buffer_store_b128(seal(t, tg), rws, vs, 0, AUX_SC1);
      STAMP(7);
    }
  } else if (r < 27) {
    const int f = 18 * x + (r - 9);
    const bool act3 = qc < 3;
    const int un4 = 144 * wave + (qc < 2 ? 8 * qr + 4 * qc : 128 + (qr & 12));
    const v2u xi = LD64(mk(xin, 1152), act3 ? un4 * 2 : OOB, 0);
    v2f xv[2], ga[2], sb[2], cb[2];
    lds_wait(&L.cnt[4], 1);
    const unsigned tg = __builtin_amdgcn_readfirstlane(L.cnt[7]);
    {
      for (int i = 0; i < MDEL; ++i) __builtin_amdgcn_s_sleep(127);
      const unsigned vm = act3 ? mcopy + un4 * 4 : OOB;
      v4u g[3];
      _Pragma("unroll 1") for (int it = 0; it < GCAP; ++it) {
        unsigned bad = 0;
#pragma unroll
        for (int j = 0; j < 3; ++j) g[j] = LD128(rws, vm + j * 2304, AUX_SC1);
#pragma unroll
        for (int j = 0; j < 3; ++j) bad |= (g[j][1] ^ tg) | (g[j][3] ^ tg);
        if (all_true(bad == 0 || !act3)) break;
        __builtin_amdgcn_s_sleep(32);
        MEMFENCE();
      }
      STAMP(4);
#pragma unroll
      for (int e = 0; e < 4; ++e) {
        const bool on = qc < 2 || (qc == 2 && (qr & 3) == e);
        const float a = (e & 1) ? hi16(xi[e >> 1]) : lo16(xi[e >> 1]), g0 = (e & 1) ? hi16(g[0][e & 2]) : lo16(g[0][e & 2]);
        const float g1 = (e & 1) ? hi16(g[1][e & 2]) : lo16(g[1][e & 2]), g2 = (e & 1) ? hi16(g[2][e & 2]) : lo16(g[2][e & 2]);
        xv[e >> 1][e & 1] = on ? a : 0.f; ga[e >> 1][e & 1] = on ? g0 : 0.f; sb[e >> 1][e & 1] = on ? g1 : 0.f; cb[e >> 1][e & 1] = on ? rbf(1.f + g2) : 0.f;
        if (e & 1) { PIN(xv[e >> 1]); PIN(ga[e >> 1]); PIN(sb[e >> 1]); PIN(cb[e >> 1]); }
      }
    }
    __builtin_amdgcn_s_setprio(0);
    const rsrc_t rg = mk(wg + f * 32, 575 * 9216 + 64), ru = mk(wu + f * 32, 575 * 9216 + 64);
    const rsrc_t rd = mk(wd + (size_t)(32 * f + 8 * wave) * 576, 8 * 1152);
    v4u w1[9], w2[9], w3[8]; unsigned w4[8];
#define LDD(t0, t1) _Pragma("unroll") for (int t = t0; t < t1; ++t) { w3[t] = LD128(rd, t * 1152 + lane * 16, AUX_NT); w4[t] = LD16(rd, t * 1152 + 1024 + lane * 2, AUX_NT); SB(); }
    LDD(0, DE)
    SB(); lds_wait(&L.cnt[3], DTRIG - 8); if (DDEL) __builtin_amdgcn_s_sleep(DDEL);
    const unsigned tz = __builtin_amdgcn_readfirstlane(L.cnt[5]);
    if (XD) wait_until(tz + XD);
    STAMP(5); SB();
    LDD(DE, DL)
    SB();
    if (XG) wait_until(tz + XG);
    STAMP(9); SB();
    const unsigned vg = (144 * wave + 8 * qr) * 9216 + qc * 16, vz = (144 * wave + 128 + qr) * 9216 + qc * 16;
#pragma unroll
    for (int t = 0; t < 9; ++t) { w1[t] = __builtin_amdgcn_raw_buffer_load_b128(rg, t < 8 ? vg : vz, t < 8 ? t * 9216 : 0, AUX_NT); SB(); }
#pragma unroll
    for (int t = 0; t < 9; ++t) { w2[t] = __builtin_amdgcn_raw_buffer_load_b128(ru, t < 8 ? vg : vz, t < 8 ? t * 9216 : 0, AUX_NT); SB(); }
    SB();
    LDD(DL, DB)
    SB();
    unsigned Dp[4][8], Dt[4];
#define MKD(p0, p1) _Pragma("unroll") for (int p = p0; p < p1; ++p) { \
      interleave(Dp[p], w3[2 * p], w3[2 * p + 1]); Dt[p] = w4[2 * p] | (w4[2 * p + 1] << 16); \
      _Pragma("unroll") for (int d = 0; d < 8; ++d) PIN(Dp[p][d]); \
      PIN(Dt[p]); SB(); }
    MKD(0, DB / 2)
    STAMP(10);
    const unsigned vb = act3 ? OFF_B + ((x + 8 * r) & (BC - 1)) * 18432 + un4 * 4 : OOB;
    v4u yb[8];
    unsigned bm = act3 ? 0xffu : 0u; float* sw = &L.st[wave][lane >> 5];
    unsigned hws = LA(L.hp[wave]), hrs = LA(&L.hp[0][8 * wave]), dws = LA(L.dn[wave]), drs = LA(&L.dn[0][144 * wave]), vss = OFF_C + f * 3072 + 768 * wave;
    const v4u tq = {tg & 0xffu, (tg >> 8) & 0xffu, (tg >> 16) & 0xffu, tg >> 24};
    PIN(bm); PINL(sw);
    asm volatile("" : "+s"(hws), "+s"(hrs), "+s"(dws), "+s"(drs), "+s"(vss));
#define LDB _Pragma("unroll") for (int j = 0; j < 8; ++j) { yb[j] = __builtin_amdgcn_raw_buffer_load_b128(rws, vb, j * 2304, AUX_SC1); SB(); }
    Slab G, U;
    SB();
    slab_make(G, w1); SB(); slab_make(U, w2); SB();
    STAMP(6);
    v4f ys;
    {
      int left = GCAP; u64 miss;
      _Pragma("unroll 1") do {
        MEMFENCE();
        LDB
        --left;
        unsigned bad = 0;
#pragma unroll
        for (int j = 0; j < 8; ++j) {
          yb[j] ^= tq; bad |= (yb[j][0] | yb[j][1]) | (yb[j][2] | yb[j][3]);
          ys = j ? ys + __builtin_bit_cast(v4f, yb[j]) : __builtin_bit_cast(v4f, yb[j]);
          SB();
        }
        miss = __builtin_amdgcn_ballot_w64((bad & bm) != 0) & (left ? ~0ull : 0ull);
        PIN(ys);
      } while (miss);
    }
    STAMP(7);
    v2f xr[2] = {{ys.x, ys.y}, {ys.z, ys.w}};
#pragma unroll
    for (int j = 0; j < 2; ++j) xr[j] = ga[j] * rbf2(xr[j]);
    SB();
#pragma unroll
    for (int j = 0; j < 2; ++j) xr[j] = xv[j] + rbf2(xr[j]);
    SB();
#pragma unroll
    for (int j = 0; j < 2; ++j) xr[j] = rbf2(xr[j]);
    SB();
    {
      const v2f s2 = xr[0] + xr[1], q2 = __builtin_elementwise_fma(xr[0], xr[0], xr[1] * xr[1]);
      float t = rs32(s2.x + s2.y, q2.x + q2.y);
      t += dppf<X1>(t); t += dppf<X2>(t); t += dppf<R4>(t); t += dppf<R8>(t);
      *sw = rs16(t, t);
    }
    float ag[8], au[8];
    ZERO8(ag) ZERO8(au)
    wg_barrier();
    const v4f t0 = *(const v4f*)&L.st[0][0], t1 = *(const v4f*)&L.st[2][0];
    SB();
    unsigned hw = hws + (lane & 32) * 4 + qc * 32 + (lane & 16), hr = hrs + c8 * 4, dw = dws + lane * 32, de = dws + 2048 + lane * 4;
    unsigned dr0 = drs + (lane < 47 ? lane : 47) * 12, dr1 = dr0 + 2304, dr2 = dr0 + 4608, dr3 = dr0 + 6912, vs = lane < 48 ? vss + lane * 16 : OOB;
    PIN(hw); PIN(hr); PIN(dw); PIN(de); PIN(dr0); PIN(dr1); PIN(dr2); PIN(dr3); PIN(vs);
    SB();
    const v2f sq = (v2f{t0.x, t0.y} + v2f{t0.z, t0.w}) + (v2f{t1.x, t1.y} + v2f{t1.z, t1.w});
    float m, rsd; ln_stats(sq.x, sq.y, m, rsd);
#pragma unroll
    for (int j = 0; j < 2; ++j) xr[j] = rbf2(xr[j] - m) * rsd;
    SB();
#pragma unroll
    for (int j = 0; j < 2; ++j) xr[j] = rbf2(xr[j]) * cb[j];
    SB();
#pragma unroll
    for (int j = 0; j < 2; ++j) xr[j] = rbf2(xr[j]) + sb[j];
    SB();
    const unsigned h01 = pack2(xr[0].x, xr[0].y), h23 = pack2(xr[1].x, xr[1].y);
    Hv H;
    H.p[0] = dppu<0x00>(h01); H.p[1] = dppu<0x00>(h23); H.p[2] = dppu<0x55>(h01); H.p[3] = dppu<0x55>(h23);
    { const unsigned hz = dppu<0xAA>(h01 | h23); H.zb = (hz | (hz << 16)) & 0xffff0000u; H.za = H.zb >> 16; }
    slab_dot(ag, G, H); slab_dot(au, U, H);
    float y[8];
#pragma unroll
    for (int j = 0; j < 8; ++j) y[j] = rs32(ag[j], au[j]);
    float z[4];
#pragma unroll
    for (int j = 0; j < 4; ++j) z[j] = rs16(y[j], y[j + 4]);
#pragma unroll
    for (int j = 0; j < 4; ++j) { z[j] += dppf<R4>(z[j]); PIN(z[j]); }
#pragma unroll
    for (int j = 0; j < 4; ++j) { z[j] += dppf<R8>(z[j]); PIN(z[j]); }
    *LF4(hw) = v4f{z[0], z[1], z[2], z[3]};
    float d8[8], dt = 0.f;
    ZERO8(d8) PIN(dt);
    wg_barrier();
    const v2f gu = rbf2((v2f{LF(hr)[0], LF(hr)[32]} + v2f{LF(hr)[64], LF(hr)[96]}) + (v2f{LF(hr)[128], LF(hr)[160]} + v2f{LF(hr)[192], LF(hr)[224]}));
    const float av = rbf(silu(gu.x)) * gu.y;
    const unsigned ap = pack2(av, dppf<X1>(av));
    unsigned a2[4];
#pragma unroll
    for (int p = 0; p < 4; ++p) a2[p] = __builtin_amdgcn_readlane(ap, 2 * p);
    asm volatile("" :: "s"(a2[0]), "s"(a2[1]), "s"(a2[2]), "s"(a2[3]));
    SB();
#pragma unroll
    for (int p = 0; p < 4; ++p) {
#pragma unroll
      for (int d = 0; d < 8; ++d) d8[d] = dot2(Dp[p][d], a2[p], d8[d]);
      dt = dot2(Dt[p], a2[p], dt);
    }
    LF4(dw)[0] = v4f{d8[0], d8[1], d8[2], d8[3]}; LF4(dw)[1] = v4f{d8[4], d8[5], d8[6], d8[7]}; *LF(de) = dt;
    wg_barrier();
    {
      const v2f u = (v2f{LF(dr0)[0], LF(dr0)[1]} + v2f{LF(dr1)[0], LF(dr1)[1]}) + (v2f{LF(dr2)[0], LF(dr2)[1]} + v2f{LF(dr3)[0], LF(dr3)[1]});
      float u0 = LF(dr0)[2] + LF(dr1)[2], u1 = LF(dr2)[2] + LF(dr3)[2];
      PIN(u0); PIN(u1);
      const v4u t = {asu(u.x), asu(u.y), asu(u0 + u1), tg};
      __builtin_amdgcn_raw_buffer_store_b128(t, rws, vs, 0, AUX_SC1);
    }
    STAMP(8);
  } else {
    const int j = (r - 27) * 8 + x;
    if (j >= 36) { FLUSH(); return; }
    unsigned tg;
    {
      const rsrc_t rw = mk(wmod + 144 * wave * 3456 + 1152 + 64 * j, 143 * 6912 + 128), rin = mk(cin + 144 * wave, 288);
      if (LATEQ) lds_wait(&L.cnt[4], 1);
      v4u ci[2], w[2][9]; unsigned cz[2];
#pragma unroll
      for (int u = 0; u < 2; ++u) { ci[u] = LD128(rin, u * 144 + r8 * 16, 0); cz[u] = LD16(rin, u * 144 + 128 + r8 * 2, 0); }
#pragma unroll
      for (int u = 0; u < 2; ++u)
#pragma unroll
        for (int t = 0; t < 9; ++t) w[u][t] = LD128(rw, (72 * u + (t < 8 ? 8 * r8 + t : 64 + r8)) * 6912 + c8 * 16, AUX_NT);
      SB();
      v4f y = {0.f, 0.f, 0.f, 0.f};
#pragma unroll
      for (int u = 0; u < 2; ++u) {
        Hv H; Slab S; float acc[8] = {0.f, 0.f, 0.f, 0.f, 0.f, 0.f, 0.f, 0.f};
        hv_silu(H, ci[u], cz[u]); slab_make(S, w[u]); slab_dot(acc, S, H);
        y += fold8(acc);
      }
      if ((lane & 24) == 0) *(v4f*)&L.hp[wave][c8 * 8 + (lane >> 5) * 4] = y;
      WAIT_LGKM0();
      if (lane == 0) __hip_atomic_fetch_add(&L.cnt[1], 1u, __ATOMIC_RELAXED, __HIP_MEMORY_SCOPE_WORKGROUP);
      if (wave > 1) { FLUSH(); return; }
      lds_wait(&L.cnt[1], 4);
      tg = __builtin_amdgcn_readfirstlane(L.cnt[7]);
      const int c4 = (lane & 15) * 4;
      const v4f t = (*(const v4f*)&L.hp[0][c4] + *(const v4f*)&L.hp[1][c4]) + (*(const v4f*)&L.hp[2][c4] + *(const v4f*)&L.hp[3][c4]);
      const v4u g = {pack2(t.x, t.y), tg, pack2(t.z, t.w), tg};
      __builtin_amdgcn_raw_buffer_store_b128(g, rws, OFF_M + (4 * wave + (lane >> 4)) * M_COPY + j * 256 + c4 * 4, 0, AUX_SC1);
      STAMP(4);
      if (j >= 24) { FLUSH(); return; }
    }
    lds_wait(&L.cnt[3], 9);
    const int jj = 2 * j + wave, col = 12 * jj + 3 * qc + ((lane & 16) ? 2 : lane >> 5);
    const float xv = lo16(LD16(mk(xin, 1152), col * 2, 0));
    v2u ga, gb;
    _Pragma("unroll 1") for (int it = 0; it < GCAP; ++it) {
      ga = LD64(rws, mcopy + (col >> 1) * 8, AUX_SC1); gb = LD64(rws, mcopy + 3 * 2304 + (col >> 1) * 8, AUX_SC1);
      unsigned bad = (ga[1] ^ tg) | (gb[1] ^ tg);
      if (all_true(bad == 0)) break;
      __builtin_amdgcn_s_sleep(32);
      MEMFENCE();
    }
    float gav = lo16(ga[0] >> ((col & 1) * 16)), gbv = lo16(gb[0] >> ((col & 1) * 16));
    STAMP(5);
    const unsigned vb = OFF_B + ((x + 8 * r) & (BC - 1)) * 18432 + (qr & 3) * 4608 + col * 4, tb = (tg >> (8 * (col & 3))) & 0xff;
    unsigned y0, y1;
    _Pragma("unroll 1") for (int it = 0; it < GCAP; ++it) {
      y0 = LD32(rws, vb, AUX_SC1) ^ tb; y1 = LD32(rws, vb + 2304, AUX_SC1) ^ tb;
      if (all_true(((y0 | y1) & 0xff) == 0)) break;
      __builtin_amdgcn_s_sleep(4);
      MEMFENCE();
    }
    float ys = asf(y0) + asf(y1);
    ys += dppf<R4>(ys); ys += dppf<R8>(ys);
    float x1 = rbf(xv + rbf(gav * rbf(ys)));
    unsigned vout = ((lane & 12) == 0 && lane < 48) ? col * 2 : OOB;
    PIN(x1); PIN(gbv); PIN(vout);
    STAMP(6);
    const unsigned vc = OFF_C + qr * 3072 + jj * 64 + qc * 16;
    const u64 wa = (u64)ws;
    const v4u rc = {(unsigned)wa, (unsigned)(wa >> 32) & 0xffffu, WS_END, 0x00020000u};
    v4u cc[CN][9];
    v2f a01; float a2;
#define CL1(n) "buffer_load_dwordx4 %" #n ", %[vc], %[rc], %[so] offen sc1\n\ts_add_u32 %[so], %[so], 0xc000\n\t"
#define CREG(m, n) m(cc[n][0]), m(cc[n][1]), m(cc[n][2]), m(cc[n][3]), m(cc[n][4]), m(cc[n][5]), m(cc[n][6]), m(cc[n][7]), m(cc[n][8])
#define CLD(n) { unsigned so; asm volatile("s_mov_b32 %[so], 0\n\t" CL1(0) CL1(1) CL1(2) CL1(3) CL1(4) CL1(5) CL1(6) CL1(7) CL1(8) \
                              : CREG("=&v", n), [so] "=&s"(so) : [vc] "v"(vc), [rc] "s"(rc) : "scc", "memory"); }
#pragma unroll
    for (int n = 0; n < CN; ++n) { CLD(n) if (n < CN - 1) __builtin_amdgcn_s_sleep(CD); }
    _Pragma("unroll 1") for (int it = 0; it < GCAP; ++it) {
#pragma unroll
      for (int n = 0; n < CN; ++n) {
        unsigned bad = 0;
#pragma unroll
        for (int t = 0; t < 9; t += 3) {
          asm volatile("s_waitcnt vmcnt(%3)" : "+v"(cc[n][t]), "+v"(cc[n][t + 1]), "+v"(cc[n][t + 2]) : "n"(9 * CN - 3 - t));
#pragma unroll
          for (int u = t; u < t + 3; ++u) {
            const v2f e = {asf(cc[n][u][0]), asf(cc[n][u][1])};
            bad |= cc[n][u][3] ^ tg; a01 = u ? a01 + e : e; a2 = u ? a2 + asf(cc[n][u][2]) : asf(cc[n][u][2]);
          }
          PIN(a01); PIN(a2); PIN(bad);
        }
        if (all_true(bad == 0)) goto have_c;
        CLD(n)
      }
    }
  have_c:
    STAMP(7);
    float t = rs16(rs32(a01.x, a01.y), rs32(a2, a2));
    t += dppf<R4>(t); t += dppf<R8>(t);
    const float res = x1 + rbf(gbv * rbf(t));
    __builtin_amdgcn_raw_buffer_store_b16((u16)pack2(res, 0.f), mk(out, 1152), vout, 0, 0);
    if (jj == 0) __builtin_amdgcn_raw_buffer_store_b32(tg, rws, lane == 0 ? 0u : OOB, 0, AUX_SC1);
    STAMP(8);
#pragma unroll
    for (int n = 0; n < CN; ++n) asm volatile("s_waitcnt vmcnt(0)" :: CREG("v", n));
  }
  FLUSH();
}

extern "C" void kernel_launch(void* const* d_in, const int* in_sizes, int n_in, void* d_out, int out_size, void* d_ws,
                              size_t ws_size, hipStream_t stream) {
  const Args a = {(const u16*)d_in[0], (const u16*)d_in[3], (const u16*)d_in[9], (const u16*)d_in[10], (char*)d_ws, (u16*)d_out,
                  (const u16*)d_in[4], (const u16*)d_in[8], (const u16*)d_in[6], (const u16*)d_in[7], (const u16*)d_in[1], (const u16*)d_in[2]};
  fused_block<<<256, 768, 0, stream>>>(a);
}
```

```cpp
#include <hip/hip_runtime.h>
#include <hip/hip_bf16.h>
#include <stdint.h>

typedef unsigned short u16;
typedef unsigned long long u64;
typedef unsigned v4u __attribute__((ext_vector_type(4)));
typedef unsigned v3u __attribute__((ext_vector_type(3)));
typedef unsigned v2u __attribute__((ext_vector_type(2)));
typedef float v2f __attribute__((ext_vector_type(2)));
typedef float v4f __attribute__((ext_vector_type(4)));
typedef __bf16 bf2 __attribute__((ext_vector_type(2)));
typedef __amdgpu_buffer_rsrc_t rsrc_t;

#define TF 4
#define TS 5
#define DTRIG 17
#define DDEL 0
#define ROTX ((head >> 2) * 64)
#define NL 1
#define CPRE 5
#define CSZ 20416u
#define PR 24
#define PC 9
#define DE 0
#define DL 8
#define DB 8
#define CN 2
#define CD 8
#define XP 200
#define XD 0
#define XG 400
#define POUT 1
#define MDEL 2
#define PD1 30
#define PD2 40
#define LATEQ 1

#define BC 8
constexpr int AUX_NT = 2, AUX_SC1 = 16;
constexpr unsigned TAG_STEP = 0x01010101u;
constexpr unsigned OOB = 0x40000000u;
constexpr unsigned OFF_T = 4096;
constexpr unsigned OFF_P = 16384;
constexpr unsigned OFF_Q = OFF_P + 64 * 896;
constexpr unsigned OFF_M = OFF_Q + 256 * 768;
constexpr unsigned M_COPY = 4 * 2304;
constexpr unsigned OFF_AO = OFF_M + 8 * M_COPY;
constexpr unsigned OFF_B = OFF_AO + 256 * 768;
constexpr unsigned OFF_C = OFF_B + 8 * 18432;
constexpr unsigned WS_END = OFF_C + 144 * 3072;
constexpr int GCAP = 1 << 15;
constexpr int LCAP = 1 << 20;
constexpr float LOG2E = 1.4426950408889634f;

__device__ __forceinline__ float asf(unsigned u) { return __builtin_bit_cast(float, u); }
__device__ __forceinline__ unsigned asu(float f) { return __builtin_bit_cast(unsigned, f); }
__device__ __forceinline__ float lo16(unsigned u) { return asf(u << 16); }
__device__ __forceinline__ float hi16(unsigned u) { return asf(u & 0xffff0000u); }
__device__ __forceinline__ float rbf(float f) { __bf16 b = (__bf16)f; return (float)b; }
__device__ __forceinline__ unsigned pack2(float a, float b) { bf2 v = {(__bf16)a, (__bf16)b}; return __builtin_bit_cast(unsigned, v); }
__device__ __forceinline__ float dot2(unsigned a, unsigned b, float c) {
  return __builtin_amdgcn_fdot2_f32_bf16(__builtin_bit_cast(bf2, a), __builtin_bit_cast(bf2, b), c, false);
}
template <int CTRL> __device__ __forceinline__ unsigned dppu(unsigned v) { return __builtin_amdgcn_update_dpp(0u, v, CTRL, 0xf, 0xf, true); }
template <int CTRL> __device__ __forceinline__ float dppf(float v) { return asf(dppu<CTRL>(asu(v))); }
constexpr int X1 = 0xB1, X2 = 0x4E, HM = 0x141, R4 = 0x124, R8 = 0x128;
__device__ __forceinline__ float rs32(float a, float b) {
  auto r = __builtin_amdgcn_permlane32_swap(asu(a), asu(b), false, false);
  unsigned r0 = r[0], r1 = r[1]; return asf(r0) + asf(r1);
}
__device__ __forceinline__ float rs16(float a, float b) {
  auto r = __builtin_amdgcn_permlane16_swap(asu(a), asu(b), false, false);
  unsigned r0 = r[0], r1 = r[1]; return asf(r0) + asf(r1);
}
__device__ __forceinline__ float fmx(float a, float b) { return __builtin_elementwise_maximum(a, b); }
__device__ __forceinline__ v2f rbf2(v2f a) { unsigned u = pack2(a.x, a.y); asm("" : "+v"(u)); return v2f{lo16(u), hi16(u)}; }
__device__ __forceinline__ float mx32(float a) {
  auto r = __builtin_amdgcn_permlane32_swap(asu(a), asu(a), false, false);
  unsigned r0 = r[0], r1 = r[1]; return fmx(asf(r0), asf(r1));
}
__device__ __forceinline__ float mx16(float a) {
  auto r = __builtin_amdgcn_permlane16_swap(asu(a), asu(a), false, false);
  unsigned r0 = r[0], r1 = r[1]; return fmx(asf(r0), asf(r1));
}
__device__ __forceinline__ float wave_sum(float v) {
  v += dppf<X1>(v); v += dppf<X2>(v); v += dppf<R4>(v); v += dppf<R8>(v);
  v = rs16(v, v); v = rs32(v, v); return v;
}
__device__ __forceinline__ float ex2(float x) { return __builtin_amdgcn_exp2f(x); }
__device__ __forceinline__ float rcp(float x) { return __builtin_amdgcn_rcpf(x); }
__device__ __forceinline__ float silu(float g) { return g * rcp(1.f + ex2(-g * LOG2E)); }
__device__ __forceinline__ v4u seal(v4f t, unsigned tg) {
  v4u o;
#pragma unroll
  for (int j = 0; j < 4; ++j) o[j] = ((asu(t[j]) + 0x80u) & ~0xffu) | ((tg >> (8 * j)) & 0xffu);
  return o;
}
__device__ __forceinline__ unsigned unseal(v4u& v, unsigned tg) {
#pragma unroll
  for (int j = 0; j < 4; ++j) v[j] ^= (tg >> (8 * j)) & 0xffu;
  asm volatile("" : "+v"(v));
  return (v[0] | v[1]) | (v[2] | v[3]);
}
__device__ __forceinline__ bool all_true(bool ok) { return __builtin_amdgcn_ballot_w64(!ok) == 0; }
__device__ __forceinline__ rsrc_t mk(const void* p, unsigned n) { return __builtin_amdgcn_make_buffer_rsrc((void*)p, 0, n, 0x00020000); }
#define LD128(r, v, aux) __builtin_amdgcn_raw_buffer_load_b128(r, v, 0, aux)
#define LD64(r, v, aux) __builtin_amdgcn_raw_buffer_load_b64(r, v, 0, aux)
#define LD32(r, v, aux) __builtin_amdgcn_raw_buffer_load_b32(r, v, 0, aux)
#define LD16(r, v, aux) __builtin_amdgcn_raw_buffer_load_b16(r, v, 0, aux)
#define SB() __builtin_amdgcn_sched_barrier(0)
#define MEMFENCE() asm volatile("" ::: "memory")
#define WAIT_LGKM0() asm volatile("s_waitcnt lgkmcnt(0)" ::: "memory")
#define WAIT_VM0() asm volatile("s_waitcnt vmcnt(0)" ::: "memory")
#define PIN(x) asm volatile("" : "+v"(x))
#define PINL(p) do { auto q_ = (__attribute__((address_space(3))) char*)(p); PIN(q_); p = (decltype(p))q_; } while (0)
__device__ __forceinline__ void wg_barrier() { WAIT_LGKM0(); __builtin_amdgcn_s_barrier(); MEMFENCE(); }

struct __attribute__((aligned(16))) Lds {
  float rec_o[8][176];
  float rec_m[16];
  float dn[4][576];
  float hp[4][64];
  unsigned ob[4][96];
  unsigned qs[64];
  unsigned hs[40];
  unsigned sink[64];
  float st[4][2];
  v4u stg[8][NL ? NL : 1][8][64];
  unsigned cnt[8];
};

__device__ __forceinline__ void lds_wait(const unsigned* w, unsigned target) {
  _Pragma("unroll 1") for (int it = 0; it < LCAP; ++it) {
    unsigned v = __hip_atomic_load(w, __ATOMIC_RELAXED, __HIP_MEMORY_SCOPE_WORKGROUP);
    if (__builtin_amdgcn_readfirstlane(v) >= target) break;
    __builtin_amdgcn_s_sleep(1);
  }
  MEMFENCE();
}

__device__ __forceinline__ unsigned now32() { return (unsigned)__builtin_amdgcn_s_memrealtime(); }
__device__ __forceinline__ void wait_until(unsigned t) {
  _Pragma("unroll 1") for (int it = 0; it < 200 && (int)(now32() - t) < 0; ++it) __builtin_amdgcn_s_sleep(1);
}

struct Slab { unsigned p[4][8]; unsigned z[4]; };
__device__ __forceinline__ void interleave(unsigned (&o)[8], v4u a, v4u b) {
#pragma unroll
  for (int d = 0; d < 4; ++d) {
    unsigned x = a[d], y = b[d];
    o[2 * d] = __builtin_amdgcn_perm(y, x, 0x05040100u);
    o[2 * d + 1] = __builtin_amdgcn_perm(y, x, 0x07060302u);
  }
}
__device__ __forceinline__ void slab_make(Slab& s, const v4u (&w)[9]) {
#pragma unroll
  for (int p = 0; p < 4; ++p) {
    interleave(s.p[p], w[2 * p], w[2 * p + 1]); s.z[p] = w[8][p];
#pragma unroll
    for (int d = 0; d < 8; ++d) PIN(s.p[p][d]);
    PIN(s.z[p]);
  }
}
struct Hv { unsigned p[4], za, zb; };
__device__ __forceinline__ void hv_silu(Hv& h, v4u a, unsigned z) {
#pragma unroll
  for (int p = 0; p < 4; ++p) h.p[p] = pack2(silu(lo16(a[p])), silu(hi16(a[p])));
  h.za = pack2(silu(lo16(z)), 0.f); h.zb = h.za << 16;
}
__device__ __forceinline__ void slab_dot(float (&acc)[8], const Slab& s, const Hv& h) {
#pragma unroll
  for (int p = 0; p < 4; ++p)
#pragma unroll
    for (int d = 0; d < 8; ++d) acc[d] = dot2(s.p[p][d], h.p[p], acc[d]);
#pragma unroll
  for (int d = 0; d < 4; ++d) { acc[2 * d] = dot2(s.z[d], h.za, acc[2 * d]); acc[2 * d + 1] = dot2(s.z[d], h.zb, acc[2 * d + 1]); }
}
typedef __attribute__((address_space(3))) float* lds_f;
#define LF(a) ((lds_f)(a))
#define LF4(a) ((__attribute__((address_space(3))) v4f*)(a))
#define ZERO8(a) _Pragma("unroll") for (int d_ = 0; d_ < 8; ++d_) { a[d_] = 0.f; PIN(a[d_]); }
__device__ __forceinline__ v4f fold8(const float (&acc)[8]) {
  v4f y;
#pragma unroll
  for (int j = 0; j < 4; ++j) { float t = rs32(acc[j], acc[j + 4]); t = rs16(t, t); y[j] = t + dppf<R8>(t); }
  return y;
}
__device__ __forceinline__ void ln_stats(float s, float q, float& m, float& rsd) {
  float mean = s * (1.f / 576.f);
  float var = fmaxf(q * (1.f / 576.f) - mean * mean, 0.f);
  m = rbf(mean);
  float sd = rbf(__builtin_amdgcn_sqrtf(rbf(rbf(var) + 1e-5f)));
  rsd = rcp(sd);
}
__device__ __forceinline__ float modulate(float t, float scale, float shift) { return rbf(rbf(t * rbf(1.f + scale)) + shift); }


struct Args { const u16 *xin, *wq, *cin, *wmod; char* ws; u16* out; const u16 *wo, *wd, *wg, *wu, *Kc, *Vc; };
typedef const Args __attribute__((address_space(4))) * args_t;
__device__ __forceinline__ args_t args_ptr() {
  args_t p = (args_t)__builtin_amdgcn_kernarg_segment_ptr();
  asm volatile("" : "+s"(p));
  return p;
}

__global__ void __launch_bounds__(768, 3) fused_block(Args) {
  __shared__ Lds L;
  const int tid = threadIdx.x, lane = tid & 63, wave = __builtin_amdgcn_readfirstlane(tid >> 6);
  const int b = blockIdx.x, x = b & 7, head = b >> 3, r = (head + 4 * x) & 31;
  const int k = r >> 2;
  const int r8 = lane >> 3, c8 = lane & 7;
  const unsigned sink = (unsigned)(size_t)(__attribute__((address_space(3))) void*)L.sink;

  const bool is_mod = wave == 0, ffn = r >= 9 && r < 27;
  unsigned ep, cz;
  v4u ci, w[9];
  const u16 *xin, *cin, *wmod, *wo, *wd, *wg, *wu; u16* out; char* ws;
  if (wave < 4) {
    __builtin_amdgcn_s_setprio(3);
    const args_t A = args_ptr();
    const u16* wq = A->wq;
    xin = A->xin; cin = A->cin; wmod = A->wmod; ws = A->ws; out = A->out; wo = A->wo; wd = A->wd; wg = A->wg; wu = A->wu;
    if (tid < 8) L.cnt[tid] = 0;
    if (CPRE && wave == 3) {
      u64 pc; unsigned junk;
      asm volatile("s_getpc_b64 %0" : "=s"(pc));
      const v4u rc = {(unsigned)pc & ~255u, (unsigned)(pc >> 32) & 0xffffu, CSZ, 0x00020000u};
      asm volatile("s_mov_b32 m0, %2\n\ts_nop 1\n\tbuffer_load_dword %0, %1, 0 offen lds" :: "v"(lane < CPRE ? (lane * 32 + r) * 128 : OOB), "s"(rc), "s"(sink) : "m0");
    }
    unsigned ld = is_mod ? (ffn ? 6912 : 0) : (wave == 3 ? 0 : 8192);
    unsigned moff = 72 * x * ld + (is_mod ? r - 9 : 2 * head + wave - 1) * 128;
    unsigned v0 = 8 * r8 * ld + c8 * 16, v8 = (64 + r8) * ld + c8 * 16, vi = r8 * 16, vz = 128 + r8 * 2;
    unsigned vt = (lane == 0 && wave == 1 && x == 0) ? OFF_T + head * 256 : OOB, t0 = 64 * NL;
    asm volatile("" : "+s"(ld), "+s"(moff));
    asm volatile("" : "+v"(v0), "+v"(v8), "+v"(vz), "+v"(vi), "+v"(vt), "+v"(t0));
    SB();
    asm volatile("" :: "s"(xin), "s"(wq), "s"(wo), "s"(wg), "s"(wu), "s"(wd), "s"(cin), "s"(wmod), "s"(out), "s"(ws));
    const u64 wa = (u64)ws, ia = (u64)cin + 144 * x, ma = (u64)(is_mod ? wmod : wq) + moff;
    const v4u r0 = {(unsigned)wa, (unsigned)(wa >> 32) & 0xffffu, WS_END, 0x00020000u};
    const v4u ri = {(unsigned)ia, (unsigned)(ia >> 32) & 0xffffu, 144u, 0x00020000u};
    const v4u rm = {(unsigned)ma, (unsigned)(ma >> 32) & 0xffffu, 71 * ld + 128, 0x00020000u};
    unsigned so;
#define LDW(n) "buffer_load_dwordx4 %" #n ", %[v0], %[rm], %[so] offen nt\n\ts_add_u32 %[so], %[so], %[ld]\n\t"
    asm volatile("s_cmp_eq_u32 %[ld], 0\n\ts_cbranch_scc1 1f\n\ts_mov_b32 %[so], 0\n\t"
                 "buffer_load_dword %9, off, %[r0], 0\n\tbuffer_load_dwordx4 %10, %[vi], %[ri], 0 offen\n\t"
                 "buffer_load_ushort %11, %[vz], %[ri], 0 offen\n\t" LDW(0) LDW(1) LDW(2) LDW(3) LDW(4) LDW(5) LDW(6) LDW(7)
                 "buffer_load_dwordx4 %8, %[v8], %[rm], 0 offen nt\n\tbuffer_store_dword %[t0], %[vt], %[r0], 0 offen sc1\n"
                 "1:\n\ts_waitcnt lgkmcnt(0)\n\ts_barrier"
                 : "=&v"(w[0]), "=&v"(w[1]), "=&v"(w[2]), "=&v"(w[3]), "=&v"(w[4]), "=&v"(w[5]), "=&v"(w[6]), "=&v"(w[7]), "=&v"(w[8]),
                   "=&v"(ep), "=&v"(ci), "=&v"(cz), [so] "=&s"(so)
                 : [r0] "s"(r0), [ri] "s"(ri), [rm] "s"(rm), [v0] "v"(v0), [v8] "v"(v8), [vi] "v"(vi), [vz] "v"(vz), [ld] "s"(ld),
                   [vt] "v"(vt), [t0] "v"(t0)
                 : "scc", "memory");
  }

  if (wave >= 4) {
    const int s = wave - 4;
    const args_t A = args_ptr();
    const u16 *Kc = A->Kc, *Vc = A->Vc, *wg = A->wg, *wu = A->wu; char* ws = A->ws;
    const rsrc_t rws = mk(ws, WS_END);
    const u64 ka = (u64)(Kc + ((size_t)head << 20)), va = (u64)(Vc + ((size_t)head << 20)), wa = (u64)ws;
    const v4u rk = {(unsigned)ka, (unsigned)(ka >> 32) & 0xffffu, 1u << 21, 0x00020000u};
    const v4u rv = {(unsigned)va, (unsigned)(va >> 32) & 0xffffu, 1u << 21, 0x00020000u};
    const v4u rt = {(unsigned)wa, (unsigned)(wa >> 32) & 0xffffu, OFF_P, 0x00020000u};
    const unsigned vo = lane * 16, vt = lane == 0 ? OFF_T + head * 256 : OOB;
    const unsigned rot = ROTX, p0 = rot + k * 8 + s;
    v4u Kr[3][4], Vr[3][4];
    asm volatile("s_barrier" :: "s"(rk), "s"(rv), "s"(p0), "v"(vo) : "memory");
#define S_(x) #x
#define S(x) S_(x)
#define LD1(n, off, T) "s_waitcnt vmcnt(" S(T) "-1)\n\tbuffer_load_dwordx4 %" #n ", %[vo], %[rs], %[so] offen offset:" #off " nt\n\t"
#define VOS [vo] "v"(vo)
#define CHUNK(c) (((c) & 511) << 12)
#define LD4(T) LD1(0, 0, T) LD1(1, 1024, T) LD1(2, 2048, T) LD1(3, 3072, T)
#define LL1(off) "s_waitcnt vmcnt(" S(TF) "-1)\n\tbuffer_load_dwordx4 %[vo], %[rs], %[so] offen offset:" #off " nt lds\n\t"
#pragma unroll
    for (int i = 0; i < NL; ++i) {
      const unsigned so = CHUNK(p0 + 64 * i), lb = (unsigned)(size_t)(__attribute__((address_space(3))) void*)L.stg[s][i];
      asm volatile("s_mov_b32 m0, %[lb]\n\t" LL1(0) LL1(1024) LL1(2048) LL1(3072) :: VOS, [rs] "s"(rk), [so] "s"(so), [lb] "s"(lb) : "m0", "memory");
      asm volatile("s_mov_b32 m0, %[lb]\n\t" LL1(0) LL1(1024) LL1(2048) LL1(3072) :: VOS, [rs] "s"(rv), [so] "s"(so), [lb] "s"(lb + 4096) : "m0", "memory");
    }
#pragma unroll
    for (int i = 0; i < 3; ++i) {
      const unsigned so = CHUNK(p0 + 64 * (NL + i));
      asm volatile(LD4(TF) : "=&v"(Kr[i][0]), "=&v"(Kr[i][1]), "=&v"(Kr[i][2]), "=&v"(Kr[i][3]) : VOS, [rs] "s"(rk), [so] "s"(so));
      asm volatile(LD4(TF) : "=&v"(Vr[i][0]), "=&v"(Vr[i][1]), "=&v"(Vr[i][2]), "=&v"(Vr[i][3]) : VOS, [rs] "s"(rv), [so] "s"(so));
    }
    const int pi = (s & 3) * 41 + lane;
    const unsigned pv = (lane < 41 && pi < 162) ? (18 * r + pi / 9) * 9216 + x * 1152 + (pi % 9) * 128 : OOB;
    const int prk = (POUT && r < 9) ? 256 : 256 + PR;
    unsigned tz = 0;
    const u64 pa = (u64)(s < 4 ? wg : wu);
    const v4u rp = {(unsigned)pa, (unsigned)(pa >> 32) & 0xffffu, 576u * 9216, 0x00020000u};
    lds_wait(&L.cnt[4], 1);
    const v4u qv = *(const v4u*)&L.qs[(lane & 15) * 4];
    unsigned q0 = qv[0], q1 = qv[1], q2 = qv[2], q3 = qv[3];
    PIN(q0); PIN(q1); PIN(q2); PIN(q3);
#define LA(p) ((unsigned)(size_t)(__attribute__((address_space(3))) void*)(p))
    const int d0 = (lane & 15) * 8 + (lane >> 4) * 2;
    unsigned wa0 = LA(&L.rec_o[s][d0 + (d0 * 171 >> 9)]), wa1 = LA(&L.rec_o[s][d0 + 1 + ((d0 + 1) * 171 >> 9)]);
    unsigned ra0 = LA(&L.rec_o[0][(lane < 44 ? lane : 0) * 4]), ra1 = LA(&L.rec_m[c8]), wam = LA(&L.rec_m[s]), wac = LA(L.cnt);
    unsigned off = lane < 44 ? OFF_AO + b * 768 + lane * 16 : OOB, tgs = L.cnt[7];
    PIN(wa0); PIN(wa1); PIN(ra0); PIN(ra1); PIN(wam); PIN(wac); PIN(off); PIN(tgs);
    float m = -INFINITY, l = 0.f;
    v2f acc[4] = {{0.f, 0.f}, {0.f, 0.f}, {0.f, 0.f}, {0.f, 0.f}};
    unsigned tk = k * 8 + s - 64 + 64 * NL;
#define TICKET "v_mov_b32 %[tk], 1\n\tbuffer_atomic_add %[tk], %[vt], %[rt], 0 offen sc0 sc1\n\t"
#define PFI "buffer_load_dword %[pv], %[rp], 0 offen nt lds\n\tbuffer_load_dword %[pv], %[rp], 0 offen sc1 lds"
#define PREF0 "s_mov_b32 m0, %[sink]\n\ts_nop 1\n\t" PFI "\n\t"
#define PREF "s_cmp_lt_i32 %[tn], %[prk]\n\ts_cbranch_scc1 7f\n\t" PREF0 "7:\n\t"
#define LASTW "s_waitcnt vmcnt(0)\n\t" PREF
#define DOTS(K) \
        _Pragma("unroll") for (int t = 0; t < 4; ++t)                                               \
          sc[t] = dot2(K[t][3], q3, dot2(K[t][2], q2, dot2(K[t][1], q1, dot2(K[t][0], q0, 0.f))));
#define PV(V) \
        _Pragma("unroll") for (int t = 0; t < 4; ++t) {                                             \
          const v2f pp = {p[t], p[t]};                                                              \
          _Pragma("unroll") for (int j = 0; j < 4; ++j) {                                           \
            const v2f e = {lo16(V[t][j]), hi16(V[t][j])};                                           \
            acc[j] = __builtin_elementwise_fma(pp, e, acc[j]);                                      \
          }                                                                                         \
        }
#define SOFT \
        SB();                                                                                       \
        _Pragma("unroll") for (int t = 0; t < 4; ++t) sc[t] += dppf<X1>(sc[t]);                     \
        SB();                                                                                       \
        _Pragma("unroll") for (int t = 0; t < 4; ++t) sc[t] += dppf<X2>(sc[t]);                     \
        SB();                                                                                       \
        _Pragma("unroll") for (int t = 0; t < 4; ++t) sc[t] += dppf<R4>(sc[t]);                     \
        SB();                                                                                       \
        _Pragma("unroll") for (int t = 0; t < 4; ++t) sc[t] += dppf<R8>(sc[t]);                     \
        SB();                                                                                       \
        _Pragma("unroll") for (int t = 0; t < 4; ++t) sc[t] = rbf(rbf(sc[t]) * 0.08838834764831845f); \
        const float mn = fmaxf(fmaxf(fmaxf(sc[0], sc[1]), fmaxf(sc[2], sc[3])), m);                 \
        const float al = ex2((m - mn) * LOG2E);                                                     \
        m = mn;                                                                                     \
        float p[4];                                                                                 \
        _Pragma("unroll") for (int t = 0; t < 4; ++t) p[t] = ex2((sc[t] - mn) * LOG2E);             \
        l = l * al + ((p[0] + p[1]) + (p[2] + p[3]));                                               \
        _Pragma("unroll") for (int j = 0; j < 4; ++j) acc[j] *= al;
    int nb = 3, tn = 0;
    const unsigned sa = (unsigned)(size_t)(__attribute__((address_space(3))) void*)&L.stg[s][0][0][lane];
    asm volatile("s_waitcnt vmcnt(4)" ::: "memory");
    _Pragma("unroll 1") for (int it = 0; it < 600; ++it) {
      if (tn < 256) {
        float sc[4];
        asm volatile("s_waitcnt vmcnt(5)" : "+v"(Kr[2][0]), "+v"(Kr[2][1]), "+v"(Kr[2][2]), "+v"(Kr[2][3]));
        DOTS(Kr[2])
        SOFT
        asm volatile("s_waitcnt vmcnt(4)" : "+v"(tk), "+v"(p[0]), "+v"(p[1]), "+v"(p[2]), "+v"(p[3]), "+v"(l),
                     "+v"(acc[0]), "+v"(acc[1]), "+v"(acc[2]), "+v"(acc[3]));
        tn = __builtin_amdgcn_readfirstlane(tk);
        const unsigned so = CHUNK(tn + 256 + rot);
        tz = now32();
        asm volatile("s_cmp_lt_i32 %[tn], 0x100\n\ts_cbranch_scc1 5f\n\ts_setprio 3\n\t" LASTW "s_branch 6f\n5:\n\t" LD4(TS) "s_waitcnt vmcnt(4)\n6:"
                     : "+v"(Kr[2][0]), "+v"(Kr[2][1]), "+v"(Kr[2][2]), "+v"(Kr[2][3]), "+v"(Vr[2][0]), "+v"(Vr[2][1]), "+v"(Vr[2][2]), "+v"(Vr[2][3])
                     : VOS, [rs] "s"(rk), [so] "s"(so), [tn] "s"(tn), [pv] "v"(pv), [rp] "s"(rp), [sink] "s"(sink), [prk] "s"(prk) : "scc", "m0");
        PV(Vr[2])
        asm volatile("s_cmp_lt_i32 %[tn], 0x100\n\ts_cbranch_scc0 1f\n\t" TICKET LD4(TS) "1:"
                     : "+v"(Vr[2][0]), "+v"(Vr[2][1]), "+v"(Vr[2][2]), "+v"(Vr[2][3]), [tk] "+v"(tk),
                       "+v"(acc[0]), "+v"(acc[1]), "+v"(acc[2]), "+v"(acc[3])
                     : VOS, [rs] "s"(rv), [so] "s"(so), [vt] "v"(vt), [rt] "s"(rt), [tn] "s"(tn) : "scc", "memory");
      }
      if (nb) {
        --nb;
        float sc[4];
        asm volatile("s_waitcnt lgkmcnt(0)" : "+v"(Kr[0][0]), "+v"(Kr[0][1]), "+v"(Kr[0][2]), "+v"(Kr[0][3]), "+v"(Vr[0][0]), "+v"(Vr[0][1]), "+v"(Vr[0][2]), "+v"(Vr[0][3]));
        DOTS(Kr[0])
        SOFT
        SB();
        PV(Vr[0])
#define DR(n, off) "ds_read_b128 %" #n ", %[sa] offset:" #off "\n\t"
#define DW(n, off) "ds_write_b128 %[sa], %" #n " offset:" #off "\n\t"
        asm volatile(DR(0, 0) DR(1, 1024) DR(2, 2048) DR(3, 3072) DR(4, 4096) DR(5, 5120) DR(6, 6144) DR(7, 7168)
                     DW(12, 0) DW(13, 1024) DW(14, 2048) DW(15, 3072) DW(16, 4096) DW(17, 5120) DW(18, 6144) DW(19, 7168)
                     : "+v"(Kr[0][0]), "+v"(Kr[0][1]), "+v"(Kr[0][2]), "+v"(Kr[0][3]), "+v"(Vr[0][0]), "+v"(Vr[0][1]), "+v"(Vr[0][2]), "+v"(Vr[0][3]),
                       "+v"(acc[0]), "+v"(acc[1]), "+v"(acc[2]), "+v"(acc[3])
                     : "v"(Kr[1][0]), "v"(Kr[1][1]), "v"(Kr[1][2]), "v"(Kr[1][3]), "v"(Vr[1][0]), "v"(Vr[1][1]), "v"(Vr[1][2]), "v"(Vr[1][3]), [sa] "v"(sa)
                     : "memory");
      }
      if (nb == 0 && tn >= 256) break;
    }
    asm volatile("s_waitcnt lgkmcnt(0)" :: "v"(Kr[0][0]), "v"(Kr[0][1]), "v"(Kr[0][2]), "v"(Kr[0][3]), "v"(Vr[0][0]), "v"(Vr[0][1]), "v"(Vr[0][2]), "v"(Vr[0][3]));
    tz -= ((tn - 256) * PC) >> 1;
    const float M = mx32(mx16(m));
    const float w = ex2((m - M) * LOG2E);
    l *= w; l = rs16(l, l); l = rs32(l, l);
#pragma unroll
    for (int j = 0; j < 4; ++j) acc[j] *= w;
    const float y0 = rs16(rs32(acc[0].x, acc[2].x), rs32(acc[1].x, acc[3].x)), y1 = rs16(rs32(acc[0].y, acc[2].y), rs32(acc[1].y, acc[3].y));
    unsigned old = 1; float mi; v4f o[8];
#define RR(n, off) "ds_read_b128 %" #n ", %[ra0] offset:" #off "\n\t"
    asm volatile("ds_write_b32 %[wa0], %[y0]\n\tds_write_b32 %[wa1], %[y1]\n\ts_mov_b64 exec, 1\n\tds_write_b32 %[wa0], %[l] offset:692\n\t"
                 "ds_write_b32 %[wam], %[M]\n\tds_max_u32 %[wac], %[tz] offset:20\n\tds_add_rtn_u32 %9, %[wac], %9 offset:12\n\ts_mov_b64 exec, -1\n\t"
                 "ds_read_b32 %8, %[ra1]\n\t" RR(0, 0) RR(1, 704) RR(2, 1408) RR(3, 2112) RR(4, 2816) RR(5, 3520) RR(6, 4224) RR(7, 4928) "s_waitcnt lgkmcnt(8)"
                 : "=&v"(o[0]), "=&v"(o[1]), "=&v"(o[2]), "=&v"(o[3]), "=&v"(o[4]), "=&v"(o[5]), "=&v"(o[6]), "=&v"(o[7]), "=&v"(mi), "+v"(old)
                 : [wa0] "v"(wa0), [wa1] "v"(wa1), [ra0] "v"(ra0), [ra1] "v"(ra1), [wam] "v"(wam), [wac] "v"(wac), [y0] "v"(y0), [y1] "v"(y1),
                   [l] "v"(l), [M] "v"(M), [tz] "v"(tz) : "memory");
    float MM = fmx(mi, dppf<X1>(mi)); MM = fmx(MM, dppf<X2>(MM)); MM = fmx(MM, dppf<HM>(MM));
    const float e = ex2((mi - MM) * LOG2E);
    float es[8];
#pragma unroll
    for (int j = 0; j < 8; ++j) es[j] = asf(__builtin_amdgcn_readlane(asu(e), j));
    asm volatile("s_waitcnt lgkmcnt(0)" : "+v"(o[0]), "+v"(o[1]), "+v"(o[2]), "+v"(o[3]), "+v"(o[4]), "+v"(o[5]), "+v"(o[6]), "+v"(o[7])
                 : "s"(es[0]), "s"(es[1]), "s"(es[2]), "s"(es[3]), "s"(es[4]), "s"(es[5]), "s"(es[6]), "s"(es[7]));
    if (__builtin_amdgcn_readfirstlane(old) == 7) {
      v2f oa; float oc;
#pragma unroll
      for (int j = 0; j < 8; ++j) {
        const float ej = es[j];
        const v2f e2 = {ej, ej}, t = {o[j].x, o[j].y};
        oa = j ? __builtin_elementwise_fma(e2, t, oa) : e2 * t; oc = j ? fmaf(ej, o[j].z, oc) : ej * o[j].z;
      }
      const v4u pv = {asu(lane < 43 ? oa.x : MM), asu(oa.y), asu(oc), tgs};
      __builtin_amdgcn_raw_buffer_store_b128(pv, rws, off, 0, AUX_SC1);
      if (lane == 0) __hip_atomic_fetch_add(&L.cnt[3], 1u, __ATOMIC_RELAXED, __HIP_MEMORY_SCOPE_WORKGROUP);
    }
    if (PR && tn < prk) {
      __builtin_amdgcn_s_setprio(0);
      wait_until(tz + XP);
      asm volatile(PREF0 :: [pv] "v"(pv), [rp] "s"(rp), [sink] "s"(sink) : "m0");
    }
    WAIT_VM0();
    return;
  }

  const rsrc_t rws = mk(ws, WS_END);
  if (wave == 3) {
    const int hrow = (lane & 8) ? 64 + 3 * (lane & 3) + ((lane & 16) ? 2 : lane >> 5) : 8 * c8 + ((lane >> 3) & 6);
    const rsrc_t rin = mk(xin, 1152);
    const unsigned e3p = LD32(rws, 0, 0);
    const v4u xa = LD128(rin, lane * 16, 0), xb = LD128(rin, lane * 16 + 1024, 0);
    const unsigned xp = LD32(rin, (72 * x + (hrow & ~1)) * 2, 0);
    const unsigned tg = __builtin_amdgcn_readfirstlane(e3p) + TAG_STEP;
    float s = 0.f, q = 0.f;
#pragma unroll
    for (int d = 0; d < 4; ++d) {
      float e0 = lo16(xa[d]), e1 = hi16(xa[d]), e2 = lo16(xb[d]), e3 = hi16(xb[d]);
      s += (e0 + e1) + (e2 + e3); q += (e0 * e0 + e1 * e1) + (e2 * e2 + e3 * e3);
    }
    s = wave_sum(s); q = wave_sum(q);
    float m, rsd; ln_stats(s, q, m, rsd);
    float n0 = rbf(rbf(lo16(xp >> ((hrow & 1) * 16)) - m) * rsd), n1 = rbf(rbf(hi16(xp) - m) * rsd);
    PIN(n0); PIN(n1);
    if (PD1) __builtin_amdgcn_s_sleep(PD1);
    const unsigned vp = OFF_P + (x * 8 + r8) * 896 + c8 * 16;
    v4u g[7];
    _Pragma("unroll 1") for (int it = 0; it < GCAP; ++it) {
      unsigned bad = 0;
#pragma unroll
      for (int t = 0; t < 6; ++t) g[t] = LD128(rws, vp + t * 128, AUX_SC1);
      g[6] = LD128(rws, vp + ((lane & 3) == 3 ? 752 : 768), AUX_SC1);
#pragma unroll
      for (int t = 0; t < 7; ++t) bad |= g[t][3] ^ tg;
      if (all_true(bad == 0)) break;
      MEMFENCE();
    }
#define G3(t, j) asf(g[(t) + (j) / 3][(j) % 3])
    float sa[2], ca[2];
#pragma unroll
    for (int e = 0; e < 2; ++e) {
      sa[e] = rs16(rs32(G3(0, e), G3(0, e + 4)), rs32(G3(0, e + 2), G3(0, e + 6)));
      ca[e] = rs16(rs32(G3(3, e), G3(3, e + 4)), rs32(G3(3, e + 2), G3(3, e + 6)));
      sa[e] += dppf<R8>(sa[e]); ca[e] += dppf<R8>(ca[e]);
    }
    float z = rs16(rs32(G3(6, 0), G3(6, 1)), rs32(G3(6, 2), G3(6, 2)));
    z += dppf<R8>(z);
    const float zc = dppf<R4>(z);
    if (lane & 8) { sa[0] = z; ca[0] = zc; }
    const unsigned hh = pack2(modulate(n0, rbf(ca[0]), rbf(sa[0])), modulate(n1, rbf(ca[1]), rbf(sa[1])));
    if ((lane & 8) == 0) L.hs[hrow >> 1] = hh;
    else if ((lane & 4) == 0) ((u16*)L.hs)[hrow] = (u16)hh;
    WAIT_LGKM0();
    if (lane == 0) __hip_atomic_store(&L.cnt[0], 1u, __ATOMIC_RELAXED, __HIP_MEMORY_SCOPE_WORKGROUP);
    if (PD2) __builtin_amdgcn_s_sleep(PD2);
    const unsigned vq = OFF_Q + (head * 8 + r8) * 768 + c8 * 16;
    _Pragma("unroll 1") for (int it = 0; it < GCAP; ++it) {
      unsigned bad = 0;
#pragma unroll
      for (int t = 0; t < 6; ++t) g[t] = LD128(rws, vq + t * 128, AUX_SC1);
#pragma unroll
      for (int t = 0; t < 6; ++t) bad |= g[t][3] ^ tg;
      if (all_true(bad == 0)) break;
      MEMFENCE();
    }
#define Q3(j) rs32(asf(g[(j) / 3 * 2][(j) % 3]), asf(g[(j) / 3 * 2 + 1][(j) % 3]))
    float qq[4];
#pragma unroll
    for (int e = 0; e < 4; ++e) { const float t = rs16(Q3(e), Q3(e + 4)); qq[e] = t + dppf<R8>(t); }
    const v2u qp = {pack2(qq[0], qq[1]), pack2(qq[2], qq[3])};
    *(v2u*)&L.qs[((lane >> 5) * 8 + c8) * 4 + ((lane >> 3) & 2)] = qp;
    L.cnt[7] = tg;
    WAIT_LGKM0();
    if (lane == 0) __hip_atomic_store(&L.cnt[4], 1u, __ATOMIC_RELAXED, __HIP_MEMORY_SCOPE_WORKGROUP);
  } else if (!is_mod || ffn) {
    asm volatile("s_waitcnt vmcnt(9)" : "+v"(ep), "+v"(ci), "+v"(cz) :: "memory");
    const unsigned tg0 = __builtin_amdgcn_readfirstlane(ep) + TAG_STEP;
    const int u = lane >> 4;
    unsigned vo;
    Hv H;
    if (is_mod) {
      const int o = (r - 9) * 8 + c8, kind = o >= 72, oc = o - 72 * kind, gr = oc / 9, i = oc - 9 * gr;
      vo = OFF_P + (gr * 8 + x) * 896 + (i < 8 ? (3 * kind + u) * 128 + i * 16 : 768 + (4 * kind + u) * 16);
      hv_silu(H, ci, cz);
    } else {
      vo = OFF_Q + b * 768 + u * 256 + ((wave - 1) * 8 + c8) * 16;
    }
    if ((lane & 8) || lane >= 48) vo = OOB;
    PIN(vo);
    asm volatile("s_waitcnt vmcnt(0)" : "+v"(w[0]), "+v"(w[1]), "+v"(w[2]), "+v"(w[3]), "+v"(w[4]), "+v"(w[5]), "+v"(w[6]), "+v"(w[7]), "+v"(w[8]) :: "memory");
    Slab S;
    slab_make(S, w);
    if (!is_mod) {
      lds_wait(&L.cnt[0], 1);
      const v4u hp = *(const v4u*)&L.hs[4 * r8];
      H.za = ((const u16*)L.hs)[64 + r8]; H.zb = H.za << 16;
#pragma unroll
      for (int p = 0; p < 4; ++p) H.p[p] = hp[p];
    }
    float a[8] = {0.f, 0.f, 0.f, 0.f, 0.f, 0.f, 0.f, 0.f}; slab_dot(a, S, H);
    float e0 = rs16(rs32(a[0], a[6]), rs32(a[3], a[3])), e1 = rs16(rs32(a[1], a[7]), rs32(a[4], a[4])), e2 = rs16(rs32(a[2], a[2]), rs32(a[5], a[5]));
    e0 += dppf<R8>(e0); e1 += dppf<R8>(e1); e2 += dppf<R8>(e2);
    const v4u pv = {asu(e0), asu(e1), asu(e2), tg0};
    __builtin_amdgcn_raw_buffer_store_b128(pv, rws, vo, 0, AUX_SC1);
  }
  const int qr = lane >> 2, qc = lane & 3;
  const unsigned mcopy = OFF_M + x * M_COPY;
  if (r < 9) {
    const rsrc_t rw = mk(wo + (size_t)(512 * x + 128 * wave) * 576 + 64 * r, 127 * 1152 + 128);
    v4u wo_w[16];
    if (LATEQ) lds_wait(&L.cnt[4], 1);
#pragma unroll
    for (int t = 0; t < 16; ++t) wo_w[t] = LD128(rw, (16 * r8 + t) * 1152 + c8 * 16, AUX_NT);
    unsigned P[8][8];
#pragma unroll
    for (int p = 0; p < 8; ++p) {
      interleave(P[p], wo_w[2 * p], wo_w[2 * p + 1]);
#pragma unroll
      for (int d = 0; d < 8; ++d) PIN(P[p][d]);
    }
    lds_wait(&L.cnt[4], 1);
    const unsigned tg = __builtin_amdgcn_readfirstlane(L.cnt[7]);
    u16* ot = (u16*)L.ob[wave] + 3 * lane; const unsigned* oi = &L.ob[wave][r8 * 8];
    float* hw = &L.hp[wave][c8 * 8 + (lane >> 4) * 2]; const float* hr = &L.hp[0][(lane & 15) * 4];
    unsigned vs = OFF_B + (4 * wave + (lane >> 4)) * 18432 + x * 2304 + r * 256 + (lane & 15) * 16;
    PINL(ot); PINL(oi); PINL(hw); PINL(hr); PIN(vs);
    lds_wait(&L.cnt[3], 9);
    const unsigned va = OFF_AO + (4 * x + wave) * 8 * 768 + (lane < 43 ? lane : 43) * 16, va0 = va + (lane < 48 ? 0 : c8 * 768);
    v4u o[8];
    {
      int left = GCAP; u64 miss;
      _Pragma("unroll 1") do {
        MEMFENCE();
#pragma unroll
        for (int j = 0; j < 8; ++j) { o[j] = __builtin_amdgcn_raw_buffer_load_b128(rws, j ? va : va0, j * 768, AUX_SC1); SB(); }
        --left; miss = 0;
#pragma unroll
        for (int j = 0; j < 8; ++j) { miss |= __builtin_amdgcn_ballot_w64(o[j][3] != tg); SB(); }
        miss &= left ? ~0ull : 0ull;
      } while (miss);
    }
    const float mi = asf(o[0][0]);
    float M = fmx(mi, dppf<X1>(mi)); M = fmx(M, dppf<X2>(M)); M = fmx(M, dppf<HM>(M));
    const float e = ex2((mi - M) * LOG2E);
    float Ls = e * asf(o[0][1]); Ls += dppf<X1>(Ls); Ls += dppf<X2>(Ls); Ls += dppf<HM>(Ls);
    float oo[3] = {0.f, 0.f, 0.f};
#pragma unroll
    for (int j = 0; j < 8; ++j) {
      const float ej = asf(__builtin_amdgcn_readlane(asu(e), 48 + j));
#pragma unroll
      for (int d = 0; d < 3; ++d) oo[d] = fmaf(ej, asf(o[j][d]), oo[d]);
    }
    const float rl = asf(__builtin_amdgcn_readlane(asu(rcp(Ls)), 48));
    {
      const unsigned p01 = pack2(oo[0] * rl, oo[1] * rl), p2 = pack2(oo[2] * rl, 0.f);
      ot[0] = (u16)p01; ot[1] = (u16)(p01 >> 16); ot[2] = (u16)p2;
    }
    MEMFENCE();
    const v4u oa = *(const v4u*)oi, ob = *(const v4u*)(oi + 4);
    float acc[8] = {0.f, 0.f, 0.f, 0.f, 0.f, 0.f, 0.f, 0.f};
#pragma unroll
    for (int p = 0; p < 8; ++p) {
      const unsigned hp = p < 4 ? oa[p & 3] : ob[p & 3];
#pragma unroll
      for (int d = 0; d < 8; ++d) acc[d] = dot2(P[p][d], hp, acc[d]);
    }
    float y[4];
#pragma unroll
    for (int j = 0; j < 4; ++j) y[j] = rs32(acc[j], acc[j + 4]);
    float z0 = rs16(y[0], y[2]), z1 = rs16(y[1], y[3]);
    z0 += dppf<R8>(z0); z1 += dppf<R8>(z1);
    { PIN(z0); PIN(z1); v2f z = {z0, z1}; *(v2f*)hw = z; }
    wg_barrier();
    if (wave < 2) {
      const v4f t = (*(const v4f*)hr + *(const v4f*)(hr + 64)) + (*(const v4f*)(hr + 128) + *(const v4f*)(hr + 192));
      __builtin_amdgcn_raw_buffer_store_b128(seal(t, tg), rws, vs, 0, AUX_SC1);
    }
  } else if (r < 27) {
    const int f = 18 * x + (r - 9);
    const bool act3 = qc < 3;
    const int un4 = 144 * wave + (qc < 2 ? 8 * qr + 4 * qc : 128 + (qr & 12));
    const v2u xi = LD64(mk(xin, 1152), act3 ? un4 * 2 : OOB, 0);
    v2f xv[2], ga[2], sb[2], cb[2];
    lds_wait(&L.cnt[4], 1);
    const unsigned tg = __builtin_amdgcn_readfirstlane(L.cnt[7]);
    {
      for (int i = 0; i < MDEL; ++i) __builtin_amdgcn_s_sleep(127);
      const unsigned vm = act3 ? mcopy + un4 * 4 : OOB;
      v4u g[3];
      _Pragma("unroll 1") for (int it = 0; it < GCAP; ++it) {
        unsigned bad = 0;
#pragma unroll
        for (int j = 0; j < 3; ++j) g[j] = LD128(rws, vm + j * 2304, AUX_SC1);
#pragma unroll
        for (int j = 0; j < 3; ++j) bad |= (g[j][1] ^ tg) | (g[j][3] ^ tg);
        if (all_true(bad == 0 || !act3)) break;
        __builtin_amdgcn_s_sleep(32);
        MEMFENCE();
      }
#pragma unroll
      for (int e = 0; e < 4; ++e) {
        const bool on = qc < 2 || (qc == 2 && (qr & 3) == e);
        const float a = (e & 1) ? hi16(xi[e >> 1]) : lo16(xi[e >> 1]), g0 = (e & 1) ? hi16(g[0][e & 2]) : lo16(g[0][e & 2]);
        const float g1 = (e & 1) ? hi16(g[1][e & 2]) : lo16(g[1][e & 2]), g2 = (e & 1) ? hi16(g[2][e & 2]) : lo16(g[2][e & 2]);
        xv[e >> 1][e & 1] = on ? a : 0.f; ga[e >> 1][e & 1] = on ? g0 : 0.f; sb[e >> 1][e & 1] = on ? g1 : 0.f; cb[e >> 1][e & 1] = on ? rbf(1.f + g2) : 0.f;
        if (e & 1) { PIN(xv[e >> 1]); PIN(ga[e >> 1]); PIN(sb[e >> 1]); PIN(cb[e >> 1]); }
      }
    }
    __builtin_amdgcn_s_setprio(0);
    const rsrc_t rg = mk(wg + f * 32, 575 * 9216 + 64), ru = mk(wu + f * 32, 575 * 9216 + 64);
    const rsrc_t rd = mk(wd + (size_t)(32 * f + 8 * wave) * 576, 8 * 1152);
    v4u w1[9], w2[9], w3[8]; unsigned w4[8];
#define LDD(t0, t1) _Pragma("unroll") for (int t = t0; t < t1; ++t) { w3[t] = LD128(rd, t * 1152 + lane * 16, AUX_NT); w4[t] = LD16(rd, t * 1152 + 1024 + lane * 2, AUX_NT); SB(); }
    LDD(0, DE)
    SB(); lds_wait(&L.cnt[3], DTRIG - 8); if (DDEL) __builtin_amdgcn_s_sleep(DDEL);
    const unsigned tz = __builtin_amdgcn_readfirstlane(L.cnt[5]);
    if (XD) wait_until(tz + XD);
    SB();
    LDD(DE, DL)
    SB();
    if (XG) wait_until(tz + XG);
    SB();
    const unsigned vg = (144 * wave + 8 * qr) * 9216 + qc * 16, vz = (144 * wave + 128 + qr) * 9216 + qc * 16;
#pragma unroll
    for (int t = 0; t < 9; ++t) { w1[t] = __builtin_amdgcn_raw_buffer_load_b128(rg, t < 8 ? vg : vz, t < 8 ? t * 9216 : 0, AUX_NT); SB(); }
#pragma unroll
    for (int t = 0; t < 9; ++t) { w2[t] = __builtin_amdgcn_raw_buffer_load_b128(ru, t < 8 ? vg : vz, t < 8 ? t * 9216 : 0, AUX_NT); SB(); }
    SB();
    LDD(DL, DB)
    SB();
    unsigned Dp[4][8], Dt[4];
#define MKD(p0, p1) _Pragma("unroll") for (int p = p0; p < p1; ++p) { \
      interleave(Dp[p], w3[2 * p], w3[2 * p + 1]); Dt[p] = w4[2 * p] | (w4[2 * p + 1] << 16); \
      _Pragma("unroll") for (int d = 0; d < 8; ++d) PIN(Dp[p][d]); \
      PIN(Dt[p]); SB(); }
    MKD(0, DB / 2)
    const unsigned vb = act3 ? OFF_B + ((x + 8 * r) & (BC - 1)) * 18432 + un4 * 4 : OOB;
    v4u yb[8];
    unsigned bm = act3 ? 0xffu : 0u; float* sw = &L.st[wave][lane >> 5];
    unsigned hws = LA(L.hp[wave]), hrs = LA(&L.hp[0][8 * wave]), dws = LA(L.dn[wave]), drs = LA(&L.dn[0][144 * wave]), vss = OFF_C + f * 3072 + 768 * wave;
    const v4u tq = {tg & 0xffu, (tg >> 8) & 0xffu, (tg >> 16) & 0xffu, tg >> 24};
    PIN(bm); PINL(sw);
    asm volatile("" : "+s"(hws), "+s"(hrs), "+s"(dws), "+s"(drs), "+s"(vss));
#define LDB _Pragma("unroll") for (int j = 0; j < 8; ++j) { yb[j] = __builtin_amdgcn_raw_buffer_load_b128(rws, vb, j * 2304, AUX_SC1); SB(); }
    Slab G, U;
    SB();
    slab_make(G, w1); SB(); slab_make(U, w2); SB();
    v4f ys;
    {
      int left = GCAP; u64 miss;
      _Pragma("unroll 1") do {
        MEMFENCE();
        LDB
        --left;
        unsigned bad = 0;
#pragma unroll
        for (int j = 0; j < 8; ++j) {
          yb[j] ^= tq; bad |= (yb[j][0] | yb[j][1]) | (yb[j][2] | yb[j][3]);
          ys = j ? ys + __builtin_bit_cast(v4f, yb[j]) : __builtin_bit_cast(v4f, yb[j]);
          SB();
        }
        miss = __builtin_amdgcn_ballot_w64((bad & bm) != 0) & (left ? ~0ull : 0ull);
        PIN(ys);
      } while (miss);
    }
    v2f xr[2] = {{ys.x, ys.y}, {ys.z, ys.w}};
#pragma unroll
    for (int j = 0; j < 2; ++j) xr[j] = ga[j] * rbf2(xr[j]);
    SB();
#pragma unroll
    for (int j = 0; j < 2; ++j) xr[j] = xv[j] + rbf2(xr[j]);
    SB();
#pragma unroll
    for (int j = 0; j < 2; ++j) xr[j] = rbf2(xr[j]);
    SB();
    {
      const v2f s2 = xr[0] + xr[1], q2 = __builtin_elementwise_fma(xr[0], xr[0], xr[1] * xr[1]);
      float t = rs32(s2.x + s2.y, q2.x + q2.y);
      t += dppf<X1>(t); t += dppf<X2>(t); t += dppf<R4>(t); t += dppf<R8>(t);
      *sw = rs16(t, t);
    }
    float ag[8], au[8];
    ZERO8(ag) ZERO8(au)
    wg_barrier();
    const v4f t0 = *(const v4f*)&L.st[0][0], t1 = *(const v4f*)&L.st[2][0];
    SB();
    unsigned hw = hws + (lane & 32) * 4 + qc * 32 + (lane & 16), hr = hrs + c8 * 4, dw = dws + lane * 32, de = dws + 2048 + lane * 4;
    unsigned dr0 = drs + (lane < 47 ? lane : 47) * 12, dr1 = dr0 + 2304, dr2 = dr0 + 4608, dr3 = dr0 + 6912, vs = lane < 48 ? vss + lane * 16 : OOB;
    PIN(hw); PIN(hr); PIN(dw); PIN(de); PIN(dr0); PIN(dr1); PIN(dr2); PIN(dr3); PIN(vs);
    SB();
    const v2f sq = (v2f{t0.x, t0.y} + v2f{t0.z, t0.w}) + (v2f{t1.x, t1.y} + v2f{t1.z, t1.w});
    float m, rsd; ln_stats(sq.x, sq.y, m, rsd);
#pragma unroll
    for (int j = 0; j < 2; ++j) xr[j] = rbf2(xr[j] - m) * rsd;
    SB();
#pragma unroll
    for (int j = 0; j < 2; ++j) xr[j] = rbf2(xr[j]) * cb[j];
    SB();
#pragma unroll
    for (int j = 0; j < 2; ++j) xr[j] = rbf2(xr[j]) + sb[j];
    SB();
    const unsigned h01 = pack2(xr[0].x, xr[0].y), h23 = pack2(xr[1].x, xr[1].y);
    Hv H;
    H.p[0] = dppu<0x00>(h01); H.p[1] = dppu<0x00>(h23); H.p[2] = dppu<0x55>(h01); H.p[3] = dppu<0x55>(h23);
    { const unsigned hz = dppu<0xAA>(h01 | h23); H.zb = (hz | (hz << 16)) & 0xffff0000u; H.za = H.zb >> 16; }
    slab_dot(ag, G, H); slab_dot(au, U, H);
    float y[8];
#pragma unroll
    for (int j = 0; j < 8; ++j) y[j] = rs32(ag[j], au[j]);
    float z[4];
#pragma unroll
    for (int j = 0; j < 4; ++j) z[j] = rs16(y[j], y[j + 4]);
#pragma unroll
    for (int j = 0; j < 4; ++j) { z[j] += dppf<R4>(z[j]); PIN(z[j]); }
#pragma unroll
    for (int j = 0; j < 4; ++j) { z[j] += dppf<R8>(z[j]); PIN(z[j]); }
    *LF4(hw) = v4f{z[0], z[1], z[2], z[3]};
    float d8[8], dt = 0.f;
    ZERO8(d8) PIN(dt);
    wg_barrier();
    const v2f gu = rbf2((v2f{LF(hr)[0], LF(hr)[32]} + v2f{LF(hr)[64], LF(hr)[96]}) + (v2f{LF(hr)[128], LF(hr)[160]} + v2f{LF(hr)[192], LF(hr)[224]}));
    const float av = rbf(silu(gu.x)) * gu.y;
    const unsigned ap = pack2(av, dppf<X1>(av));
    unsigned a2[4];
#pragma unroll
    for (int p = 0; p < 4; ++p) a2[p] = __builtin_amdgcn_readlane(ap, 2 * p);
    asm volatile("" :: "s"(a2[0]), "s"(a2[1]), "s"(a2[2]), "s"(a2[3]));
    SB();
#pragma unroll
    for (int p = 0; p < 4; ++p) {
#pragma unroll
      for (int d = 0; d < 8; ++d) d8[d] = dot2(Dp[p][d], a2[p], d8[d]);
      dt = dot2(Dt[p], a2[p], dt);
    }
    LF4(dw)[0] = v4f{d8[0], d8[1], d8[2], d8[3]}; LF4(dw)[1] = v4f{d8[4], d8[5], d8[6], d8[7]}; *LF(de) = dt;
    wg_barrier();
    {
      const v2f u = (v2f{LF(dr0)[0], LF(dr0)[1]} + v2f{LF(dr1)[0], LF(dr1)[1]}) + (v2f{LF(dr2)[0], LF(dr2)[1]} + v2f{LF(dr3)[0], LF(dr3)[1]});
      float u0 = LF(dr0)[2] + LF(dr1)[2], u1 = LF(dr2)[2] + LF(dr3)[2];
      PIN(u0); PIN(u1);
      const v4u t = {asu(u.x), asu(u.y), asu(u0 + u1), tg};
      __builtin_amdgcn_raw_buffer_store_b128(t, rws, vs, 0, AUX_SC1);
    }
  } else {
    const int j = (r - 27) * 8 + x;
    if (j >= 36) return;
    unsigned tg;
    {
      const rsrc_t rw = mk(wmod + 144 * wave * 3456 + 1152 + 64 * j, 143 * 6912 + 128), rin = mk(cin + 144 * wave, 288);
      if (LATEQ) lds_wait(&L.cnt[4], 1);
      v4u ci[2], w[2][9]; unsigned cz[2];
#pragma unroll
      for (int u = 0; u < 2; ++u) { ci[u] = LD128(rin, u * 144 + r8 * 16, 0); cz[u] = LD16(rin, u * 144 + 128 + r8 * 2, 0); }
#pragma unroll
      for (int u = 0; u < 2; ++u)
#pragma unroll
        for (int t = 0; t < 9; ++t) w[u][t] = LD128(rw, (72 * u + (t < 8 ? 8 * r8 + t : 64 + r8)) * 6912 + c8 * 16, AUX_NT);
      SB();
      v4f y = {0.f, 0.f, 0.f, 0.f};
#pragma unroll
      for (int u = 0; u < 2; ++u) {
        Hv H; Slab S; float acc[8] = {0.f, 0.f, 0.f, 0.f, 0.f, 0.f, 0.f, 0.f};
        hv_silu(H, ci[u], cz[u]); slab_make(S, w[u]); slab_dot(acc, S, H);
        y += fold8(acc);
      }
      if ((lane & 24) == 0) *(v4f*)&L.hp[wave][c8 * 8 + (lane >> 5) * 4] = y;
      WAIT_LGKM0();
      if (lane == 0) __hip_atomic_fetch_add(&L.cnt[1], 1u, __ATOMIC_RELAXED, __HIP_MEMORY_SCOPE_WORKGROUP);
      if (wave > 1) return;
      lds_wait(&L.cnt[1], 4);
      tg = __builtin_amdgcn_readfirstlane(L.cnt[7]);
      const int c4 = (lane & 15) * 4;
      const v4f t = (*(const v4f*)&L.hp[0][c4] + *(const v4f*)&L.hp[1][c4]) + (*(const v4f*)&L.hp[2][c4] + *(const v4f*)&L.hp[3][c4]);
      const v4u g = {pack2(t.x, t.y), tg, pack2(t.z, t.w), tg};
      __builtin_amdgcn_raw_buffer_store_b128(g, rws, OFF_M + (4 * wave + (lane >> 4)) * M_COPY + j * 256 + c4 * 4, 0, AUX_SC1);
      if (j >= 24) return;
    }
    lds_wait(&L.cnt[3], 9);
    const int jj = 2 * j + wave, col = 12 * jj + 3 * qc + ((lane & 16) ? 2 : lane >> 5);
    const float xv = lo16(LD16(mk(xin, 1152), col * 2, 0));
    v2u ga, gb;
    _Pragma("unroll 1") for (int it = 0; it < GCAP; ++it) {
      ga = LD64(rws, mcopy + (col >> 1) * 8, AUX_SC1); gb = LD64(rws, mcopy + 3 * 2304 + (col >> 1) * 8, AUX_SC1);
      unsigned bad = (ga[1] ^ tg) | (gb[1] ^ tg);
      if (all_true(bad == 0)) break;
      __builtin_amdgcn_s_sleep(32);
      MEMFENCE();
    }
    float gav = lo16(ga[0] >> ((col & 1) * 16)), gbv = lo16(gb[0] >> ((col & 1) * 16));
    const unsigned vb = OFF_B + ((x + 8 * r) & (BC - 1)) * 18432 + (qr & 3) * 4608 + col * 4, tb = (tg >> (8 * (col & 3))) & 0xff;
    unsigned y0, y1;
    _Pragma("unroll 1") for (int it = 0; it < GCAP; ++it) {
      y0 = LD32(rws, vb, AUX_SC1) ^ tb; y1 = LD32(rws, vb + 2304, AUX_SC1) ^ tb;
      if (all_true(((y0 | y1) & 0xff) == 0)) break;
      __builtin_amdgcn_s_sleep(4);
      MEMFENCE();
    }
    float ys = asf(y0) + asf(y1);
    ys += dppf<R4>(ys); ys += dppf<R8>(ys);
    float x1 = rbf(xv + rbf(gav * rbf(ys)));
    unsigned vout = ((lane & 12) == 0 && lane < 48) ? col * 2 : OOB;
    PIN(x1); PIN(gbv); PIN(vout);
    const unsigned vc = OFF_C + qr * 3072 + jj * 64 + qc * 16;
    const u64 wa = (u64)ws;
    const v4u rc = {(unsigned)wa, (unsigned)(wa >> 32) & 0xffffu, WS_END, 0x00020000u};
    v4u cc[CN][9];
    v2f a01; float a2;
#define CL1(n) "buffer_load_dwordx4 %" #n ", %[vc], %[rc], %[so] offen sc1\n\ts_add_u32 %[so], %[so], 0xc000\n\t"
#define CREG(m, n) m(cc[n][0]), m(cc[n][1]), m(cc[n][2]), m(cc[n][3]), m(cc[n][4]), m(cc[n][5]), m(cc[n][6]), m(cc[n][7]), m(cc[n][8])
#define CLD(n) { unsigned so; asm volatile("s_mov_b32 %[so], 0\n\t" CL1(0) CL1(1) CL1(2) CL1(3) CL1(4) CL1(5) CL1(6) CL1(7) CL1(8) \
                              : CREG("=&v", n), [so] "=&s"(so) : [vc] "v"(vc), [rc] "s"(rc) : "scc", "memory"); }
#pragma unroll
    for (int n = 0; n < CN; ++n) { CLD(n) if (n < CN - 1) __builtin_amdgcn_s_sleep(CD); }
    _Pragma("unroll 1") for (int it = 0; it < GCAP; ++it) {
#pragma unroll
      for (int n = 0; n < CN; ++n) {
        unsigned bad = 0;
#pragma unroll
        for (int t = 0; t < 9; t += 3) {
          asm volatile("s_waitcnt vmcnt(%3)" : "+v"(cc[n][t]), "+v"(cc[n][t + 1]), "+v"(cc[n][t + 2]) : "n"(9 * CN - 3 - t));
#pragma unroll
          for (int u = t; u < t + 3; ++u) {
            const v2f e = {asf(cc[n][u][0]), asf(cc[n][u][1])};
            bad |= cc[n][u][3] ^ tg; a01 = u ? a01 + e : e; a2 = u ? a2 + asf(cc[n][u][2]) : asf(cc[n][u][2]);
          }
          PIN(a01); PIN(a2); PIN(bad);
        }
        if (all_true(bad == 0)) goto have_c;
        CLD(n)
      }
    }
  have_c:
    float t = rs16(rs32(a01.x, a01.y), rs32(a2, a2));
    t += dppf<R4>(t); t += dppf<R8>(t);
    const float res = x1 + rbf(gbv * rbf(t));
    __builtin_amdgcn_raw_buffer_store_b16((u16)pack2(res, 0.f), mk(out, 1152), vout, 0, 0);
    if (jj == 0) __builtin_amdgcn_raw_buffer_store_b32(tg, rws, lane == 0 ? 0u : OOB, 0, AUX_SC1);
#pragma unroll
    for (int n = 0; n < CN; ++n) asm volatile("s_waitcnt vmcnt(0)" :: CREG("v", n));
  }
}

extern "C" void kernel_launch(void* const* d_in, const int* in_sizes, int n_in, void* d_out, int out_size, void* d_ws,
                              size_t ws_size, hipStream_t stream) {
  const Args a = {(const u16*)d_in[0], (const u16*)d_in[3], (const u16*)d_in[9], (const u16*)d_in[10], (char*)d_ws, (u16*)d_out,
                  (const u16*)d_in[4], (const u16*)d_in[8], (const u16*)d_in[6], (const u16*)d_in[7], (const u16*)d_in[1], (const u16*)d_in[2]};
  fused_block<<<256, 768, 0, stream>>>(a);
}
```
